```python
import torch

def _softmax(x, axis=-1):
    return torch.softmax(x, dim=axis)
def _gelu(x):
    return torch.nn.functional.gelu(x, approximate="tanh")
def _silu(x):
    return torch.nn.functional.silu(x)
def _sigmoid(x):
    return torch.sigmoid(x)
def _layernorm(x, axis=-1, eps=1e-5):
    m = x.mean(dim=axis, keepdim=True)
    v = x.var(dim=axis, keepdim=True, unbiased=False)
    return (x - m) / torch.sqrt(v + eps)
def _scatter_add(tgt, idx, src, axis=0):
    return tgt.clone().index_add_(axis, idx, src)
def _segment_sum(src, seg, n):
    out = torch.zeros((n,) + src.shape[1:], dtype=src.dtype)
    return out.index_add_(0, seg, src)
def _pad(x, pw):
    flat = tuple(v for pair in reversed(pw) for v in pair)
    return torch.nn.functional.pad(x, flat)

kernel_name = 'interleave_layers__softmax_full_mha__dense_activation_gelu__prompt'

def setup_inputs():

    _w = (lambda a: (lambda t: t.to(torch.bfloat16) if t.is_floating_point() else t)(torch.as_tensor(a)))
    def _A_setup():
        B, S, D = 1, 2048, 512
        return {
            "x": _w((lambda *s: torch.randn(*s, dtype=torch.bfloat16) * 0.05)(B, S, D)),
            "wqkv": _w((lambda *s: torch.randn(*s, dtype=torch.bfloat16) * 0.05)(D, 3 * D)),
            "wo": _w((lambda *s: torch.randn(*s, dtype=torch.bfloat16) * 0.05)(D, D)),
        }
    _A_in = _A_setup()

    def _B_setup():
        B, S, D, F = 1, 2048, 512, 1792
        return {
            "x": _w((lambda *s: torch.randn(*s, dtype=torch.bfloat16) * 0.05)(B, S, D)),
            "w1": _w((lambda *s: torch.randn(*s, dtype=torch.bfloat16) * 0.8)(D, F) * (D ** -0.5)),
            "b1": _w((lambda *s: torch.randn(*s, dtype=torch.bfloat16) * 0.2)(F) * 0.01),
            "w2": _w((lambda *s: torch.randn(*s, dtype=torch.bfloat16) * 0.2)(F, D) * (F ** -0.5)),
            "b2": _w((lambda *s: torch.randn(*s, dtype=torch.bfloat16) * 0.2)(D) * 0.01),
        }
    _B_in = _B_setup()

    return {
        "A_x": _A_in["x"],
        "A_wqkv": _A_in["wqkv"],
        "A_wo": _A_in["wo"],
        "B_x": _B_in["x"],
        "B_w1": _B_in["w1"],
        "B_b1": _B_in["b1"],
        "B_w2": _B_in["w2"],
        "B_b2": _B_in["b2"],
    }

def reference(A_x, A_wqkv, A_wo, B_x, B_w1, B_b1, B_w2, B_b2):
    def _A_ref(x, wqkv, wo):
        B, S, D = x.shape
        H = 32; Dh = D // H
        qkv = (lambda x, *d: x.permute(*d))((x @ wqkv).reshape(B, S, 3, H, Dh), 2, 0, 3, 1, 4)
        q, k, v = qkv[0], qkv[1], qkv[2]
        scale = Dh ** -0.5
        scores = torch.einsum("bhqd,bhkd->bhqk", q, k) * scale
        p = _softmax(scores, axis=-1)
        o = torch.einsum("bhqk,bhkd->bhqd", p, v)
        return (lambda x, *d: x.permute(*d))(o, 0, 2, 1, 3).reshape(B, S, D) @ wo

    def _B_ref(x, w1, b1, w2, b2):
        h = torch.einsum("bsd,df->bsf", x, w1) + b1
        h = _gelu(h)
        return torch.einsum("bsf,fd->bsd", h, w2) + b2

    x = A_x
    x = x + _A_ref(_layernorm(x, axis=-1), A_wqkv, A_wo)
    x = x + _B_ref(_layernorm(x, axis=-1), B_w1, B_b1, B_w2, B_b2)
    x = x + _A_ref(_layernorm(x, axis=-1), A_wqkv, A_wo)
    x = x + _B_ref(_layernorm(x, axis=-1), B_w1, B_b1, B_w2, B_b2)
    return x
```

```cpp
#include <hip/hip_runtime.h>
#include <hip/hip_bf16.h>
#include <stdint.h>

typedef __bf16 bf16_t;
typedef __bf16 bf16x2 __attribute__((ext_vector_type(2)));
typedef __bf16 bf16x4 __attribute__((ext_vector_type(4)));
typedef __bf16 bf16x8 __attribute__((ext_vector_type(8)));
typedef float f32x2 __attribute__((ext_vector_type(2)));
typedef float f32x4 __attribute__((ext_vector_type(4)));
typedef float f32x16 __attribute__((ext_vector_type(16)));
typedef unsigned u32x2 __attribute__((ext_vector_type(2)));
typedef unsigned u32x4 __attribute__((ext_vector_type(4)));

#define LDS_AS __attribute__((address_space(3)))
#define GLB_AS __attribute__((address_space(1)))

constexpr int SEQ = 2048, DM = 512, NH = 32, DH = 16, FF = 1792;

__device__ __forceinline__ void glds16(const void* g, LDS_AS char* l) {
  __builtin_amdgcn_global_load_lds((const GLB_AS void*)g, (LDS_AS void*)l, 16, 0, 0);
}
__device__ __forceinline__ float rbf(float v) { return (float)(__bf16)v; }
__device__ __forceinline__ u32x2 pack4(float a, float b, float c, float d) {
  f32x2 lo = {a, b}, hi = {c, d};
  bf16x2 l = __builtin_convertvector(lo, bf16x2), h = __builtin_convertvector(hi, bf16x2);
  u32x2 r = {__builtin_bit_cast(unsigned, l), __builtin_bit_cast(unsigned, h)};
  return r;
}
__device__ __forceinline__ float bf_lo(unsigned u) { return __builtin_bit_cast(float, u << 16); }
__device__ __forceinline__ float bf_hi(unsigned u) { return __builtin_bit_cast(float, u & 0xffff0000u); }

template <int N>
__device__ __forceinline__ void wait_bar() {
  asm volatile("s_waitcnt vmcnt(%0) lgkmcnt(0)\n\ts_barrier" ::"i"(N) : "memory");
}

__device__ __forceinline__ float gelu_tanh(float x) {
  const float c1 = -2.0f * 0.7978845608028654f * 1.4426950408889634f;
  const float c2 = c1 * 0.044715f;
  float t = x * __builtin_fmaf(x * x, c2, c1);
  float e = __builtin_amdgcn_exp2f(t);
  return x * __builtin_amdgcn_rcpf(1.0f + e);
}

template <int BM, int BN, int WVM, int WVN, int K, int LDW, int NST, int MODE>
__global__ __launch_bounds__(256, 1) void gemm_kernel(const bf16_t* __restrict__ X, const bf16_t* __restrict__ W,
                                                      const bf16_t* __restrict__ bias, const bf16_t* __restrict__ res,
                                                      bf16_t* __restrict__ out0, bf16_t* __restrict__ out1,
                                                      bf16_t* __restrict__ out2) {
  extern __shared__ __attribute__((aligned(16))) char smem_g[];
  LDS_AS char* smem = (LDS_AS char*)smem_g;
  constexpr int TM = BM / WVM / 16, TN = BN / WVN / 16, NB = BN / 16;
  constexpr int XB = BM * 128, WB = BN * 128, STB = XB + WB;
  constexpr int NXI = BM / 8, NI = (BM + BN) / 8, LPW = (NI + 3) / 4, NK = K / 64;
  constexpr bool LN = (MODE == 0 || MODE == 2);

  const int tid = threadIdx.x, lane = tid & 63;
  const int wave = __builtin_amdgcn_readfirstlane(tid >> 6);
  const int wm = wave / WVN, wn = wave % WVN;
  const int b = blockIdx.x, xcd = b & 7, j = b >> 3;
  int mt, nt;
  if (BM == 128) { mt = (xcd >> 1) * 4 + (j & 3); nt = (xcd & 1) * 8 + (j >> 2); }
  else           { mt = (xcd >> 1) * 8 + (j & 7); nt = (xcd & 1) * 4 + (j >> 3); }
  const int m0 = mt * BM;
  const int l15 = lane & 15, g = lane >> 4;

  auto colbase = [&](int nb) -> int {
    if (MODE == 0) return (nb >> 1) * 512 + (nt * 2 + (nb & 1)) * 16;
    return nt * BN + nb * 16;
  };
  auto nb_of = [&](int ni) -> int {
    if (MODE == 0) return 2 * ni + wn;
    return wn * TN + ni;
  };

  u32x2 resv[TM][TN];
  u32x2 biasv[TN];
  if (MODE == 1 || MODE == 3) {
#pragma unroll
    for (int mi = 0; mi < TM; ++mi)
#pragma unroll
      for (int ni = 0; ni < TN; ++ni)
        resv[mi][ni] = *(const u32x2*)(res + (size_t)(m0 + wm * TM * 16 + mi * 16 + l15) * DM + colbase(nb_of(ni)) + 4 * g);
  }
  if (MODE == 2 || MODE == 3) {
#pragma unroll
    for (int ni = 0; ni < TN; ++ni) biasv[ni] = *(const u32x2*)(bias + colbase(nb_of(ni)) + 4 * g);
  }

  const bf16_t* src[LPW];
  int inc[LPW];
  int dsto[LPW];
#pragma unroll
  for (int i = 0; i < LPW; ++i) {
    int ii = wave + 4 * i;
    if (ii >= NI) ii -= 4;
    if (ii < NXI) {
      const int row = 8 * ii + (lane >> 3);
      const int ch = (lane & 7) ^ ((row >> 1) & 7);
      src[i] = X + (size_t)(m0 + row) * K + ch * 8;
      inc[i] = 64;
    } else {
      const int wi = ii - NXI;
      const int sidx = 4 * wi + (lane >> 4);
      const int kb = sidx / NB, nb = sidx % NB;
      const int r = (lane >> 1) & 7, half = lane & 1;
      const int krow = kb * 8 + (r ^ ((kb & 1) << 2));
      src[i] = W + (size_t)krow * LDW + colbase(nb) + half * 8;
      inc[i] = 64 * LDW;
    }
    dsto[i] = ii * 1024;
  }
  auto issue = [&](int st) {
    LDS_AS char* base = smem + (st % NST) * STB;
#pragma unroll
    for (int i = 0; i < LPW; ++i) {
      glds16(src[i], base + dsto[i]);
      src[i] += inc[i];
    }
  };

  const int xo0 = (wm * TM * 16 + l15) * 128 + ((g ^ (l15 >> 1)) * 16);
  const int q_ = (lane & 15) >> 2, p_ = lane & 3;
  const int wo0 = XB + g * NB * 256 + (q_ + 4 * (g & 1)) * 32 + p_ * 8;
  const int wo1 = XB + g * NB * 256 + (q_ + 4 * (1 - (g & 1))) * 32 + p_ * 8;

  f32x4 acc[TM][TN];
#pragma unroll
  for (int mi = 0; mi < TM; ++mi)
#pragma unroll
    for (int ni = 0; ni < TN; ++ni) acc[mi][ni] = f32x4{0.f, 0.f, 0.f, 0.f};
  float rs[TM], rq[TM], cs[TN];
#pragma unroll
  for (int mi = 0; mi < TM; ++mi) rs[mi] = rq[mi] = 0.f;
#pragma unroll
  for (int ni = 0; ni < TN; ++ni) cs[ni] = 0.f;
  const bf16x2 ones2 = {(__bf16)1.0f, (__bf16)1.0f};

#pragma unroll
  for (int s = 0; s < NST - 1; ++s) issue(s);

#pragma unroll
  for (int kt = 0; kt < NK; ++kt) {
    constexpr int dummy = 0; (void)dummy;
    const int rem = (NK - 1 - kt) < (NST - 2) ? (NK - 1 - kt) : (NST - 2);
    switch (rem * LPW) {
#define WB_CASE(n) case n: wait_bar<n>(); break;
      WB_CASE(0) WB_CASE(4) WB_CASE(7) WB_CASE(8) WB_CASE(12) WB_CASE(14) WB_CASE(16) WB_CASE(20) WB_CASE(21) WB_CASE(24)
      WB_CASE(28) WB_CASE(32) WB_CASE(35) WB_CASE(40) WB_CASE(42) WB_CASE(48)
#undef WB_CASE
      default: wait_bar<0>(); break;
    }
    if (kt + NST - 1 < NK) issue(kt + NST - 1);
    LDS_AS char* base = smem + (kt % NST) * STB;
#pragma unroll
    for (int ks = 0; ks < 2; ++ks) {
      bf16x8 xf[TM], wf[TN];
#pragma unroll
      for (int mi = 0; mi < TM; ++mi) xf[mi] = *(const LDS_AS bf16x8*)(base + ((xo0 ^ (ks * 64)) + mi * 2048));
#pragma unroll
      for (int ni = 0; ni < TN; ++ni) {
        const int o = (ks * 4 * NB + nb_of(ni)) * 256;
        bf16x4 a = __builtin_amdgcn_ds_read_tr16_b64_v4bf16((LDS_AS bf16x4*)(base + wo0 + o));
        bf16x4 c = __builtin_amdgcn_ds_read_tr16_b64_v4bf16((LDS_AS bf16x4*)(base + wo1 + o));
        wf[ni] = __builtin_shufflevector(a, c, 0, 1, 2, 3, 4, 5, 6, 7);
      }
#pragma unroll
      for (int mi = 0; mi < TM; ++mi)
#pragma unroll
        for (int ni = 0; ni < TN; ++ni) {
          if (MODE == 0 && ni == 2) acc[mi][ni] = __builtin_amdgcn_mfma_f32_16x16x32_bf16(xf[mi], wf[ni], acc[mi][ni], 0, 0, 0);
          else                      acc[mi][ni] = __builtin_amdgcn_mfma_f32_16x16x32_bf16(wf[ni], xf[mi], acc[mi][ni], 0, 0, 0);
        }
      if (LN) {
#pragma unroll
        for (int mi = 0; mi < TM; ++mi)
#pragma unroll
          for (int e = 0; e < 4; ++e) {
            bf16x2 pr = {xf[mi][2 * e], xf[mi][2 * e + 1]};
            rs[mi] = __builtin_amdgcn_fdot2_f32_bf16(pr, ones2, rs[mi], false);
            rq[mi] = __builtin_amdgcn_fdot2_f32_bf16(pr, pr, rq[mi], false);
          }
#pragma unroll
        for (int ni = 0; ni < TN; ++ni)
#pragma unroll
          for (int e = 0; e < 4; ++e) {
            bf16x2 pr = {wf[ni][2 * e], wf[ni][2 * e + 1]};
            cs[ni] = __builtin_amdgcn_fdot2_f32_bf16(pr, ones2, cs[ni], false);
          }
      }
    }
  }

  float mean[TM], rstd[TM];
  if (LN) {
#pragma unroll
    for (int mi = 0; mi < TM; ++mi) {
      float s = rs[mi], q = rq[mi];
      s += __shfl_xor(s, 16); s += __shfl_xor(s, 32);
      q += __shfl_xor(q, 16); q += __shfl_xor(q, 32);
      const float m = s * (1.0f / K);
      const float v = __builtin_fmaxf(q * (1.0f / K) - m * m, 0.f);
      mean[mi] = m;
      rstd[mi] = __builtin_amdgcn_rsqf(v + 1e-5f);
    }
#pragma unroll
    for (int ni = 0; ni < TN; ++ni) {
      float c = cs[ni];
      c += __shfl_xor(c, 16); c += __shfl_xor(c, 32);
      cs[ni] = c;
    }
  }

  if (MODE == 0) {
    const int head = nt * 2 + wn;
    const float qscale = 0.25f * 1.4426950408889634f;
    float csr[2][4];
#pragma unroll
    for (int ni = 0; ni < 2; ++ni)
#pragma unroll
      for (int r = 0; r < 4; ++r) csr[ni][r] = __shfl(cs[ni], 4 * g + r);
#pragma unroll
    for (int mi = 0; mi < TM; ++mi) {
      const int tok = m0 + wm * TM * 16 + mi * 16 + l15;
      {
        const float sc = rstd[mi] * qscale;
        float v[4];
#pragma unroll
        for (int r = 0; r < 4; ++r) v[r] = sc * (acc[mi][0][r] - mean[mi] * csr[0][r]);
        *(u32x2*)(out0 + ((size_t)head * SEQ + tok) * 16 + 4 * g) = pack4(v[0], v[1], v[2], v[3]);
      }
      {
        float v[4];
#pragma unroll
        for (int r = 0; r < 4; ++r) v[r] = rstd[mi] * (acc[mi][1][r] - mean[mi] * csr[1][r]);
        *(u32x2*)(out1 + (size_t)head * 32768 + (tok >> 5) * 512 + (g >> 1) * 256 + (tok & 31) * 8 + (g & 1) * 4) =
            pack4(v[0], v[1], v[2], v[3]);
      }
      {
        float v[4];
#pragma unroll
        for (int r = 0; r < 4; ++r) {
          const float mr = __shfl(mean[mi], 4 * g + r), rr = __shfl(rstd[mi], 4 * g + r);
          v[r] = rr * (acc[mi][2][r] - mr * cs[2]);
        }
        const int ks16 = (m0 + wm * TM * 16 + mi * 16) >> 4;
        *(u32x2*)(out2 + (size_t)head * 32768 + ks16 * 256 + (g & 1) * 128 + l15 * 8 + (g >> 1) * 4) =
            pack4(v[0], v[1], v[2], v[3]);
      }
    }
  } else if (MODE == 2) {
#pragma unroll
    for (int ni = 0; ni < TN; ++ni) {
      float csr[4];
#pragma unroll
      for (int r = 0; r < 4; ++r) csr[r] = __shfl(cs[ni], 4 * g + r);
      const float bb[4] = {bf_lo(biasv[ni][0]), bf_hi(biasv[ni][0]), bf_lo(biasv[ni][1]), bf_hi(biasv[ni][1])};
#pragma unroll
      for (int mi = 0; mi < TM; ++mi) {
        const int tok = m0 + wm * TM * 16 + mi * 16 + l15;
        float v[4];
#pragma unroll
        for (int r = 0; r < 4; ++r) {
          float t = rstd[mi] * (acc[mi][ni][r] - mean[mi] * csr[r]);
          t = rbf(rbf(t) + bb[r]);
          v[r] = gelu_tanh(t);
        }
        *(u32x2*)(out0 + (size_t)tok * FF + colbase(nb_of(ni)) + 4 * g) = pack4(v[0], v[1], v[2], v[3]);
      }
    }
  } else {
#pragma unroll
    for (int mi = 0; mi < TM; ++mi)
#pragma unroll
      for (int ni = 0; ni < TN; ++ni) {
        const int tok = m0 + wm * TM * 16 + mi * 16 + l15;
        const float rr[4] = {bf_lo(resv[mi][ni][0]), bf_hi(resv[mi][ni][0]), bf_lo(resv[mi][ni][1]), bf_hi(resv[mi][ni][1])};
        float v[4];
#pragma unroll
        for (int r = 0; r < 4; ++r) {
          float t = rbf(acc[mi][ni][r]);
          if (MODE == 3) {
            const float bb = (r == 0) ? bf_lo(biasv[ni][0]) : (r == 1) ? bf_hi(biasv[ni][0]) : (r == 2) ? bf_lo(biasv[ni][1]) : bf_hi(biasv[ni][1]);
            t = rbf(t + bb);
          }
          v[r] = rr[r] + t;
        }
        *(u32x2*)(out0 + (size_t)tok * DM + colbase(nb_of(ni)) + 4 * g) = pack4(v[0], v[1], v[2], v[3]);
      }
  }
}

constexpr int A_KOFF = 0, A_VOFF = 65536, A_ONES = 131072, A_QOFF = 139264, A_LDS = 147456;

__device__ __forceinline__ void attn_chunk(LDS_AS char* kb, LDS_AS char* vb, const bf16x8 qf, const f32x16 negm, f32x16& o) {
#pragma unroll
  for (int t = 0; t < 8; ++t) {
    const bf16x8 kf = *(const LDS_AS bf16x8*)(kb + t * 1024);
    const bf16x8 v0 = *(const LDS_AS bf16x8*)(vb + t * 1024);
    const bf16x8 v1 = *(const LDS_AS bf16x8*)(vb + t * 1024 + 512);
    f32x16 s = __builtin_amdgcn_mfma_f32_32x32x16_bf16(kf, qf, negm, 0, 0, 0);
    bf16x8 pa, pb;
#pragma unroll
    for (int r = 0; r < 8; r += 2) {
      f32x2 e0 = {__builtin_amdgcn_exp2f(s[r]), __builtin_amdgcn_exp2f(s[r + 1])};
      bf16x2 c0 = __builtin_convertvector(e0, bf16x2);
      pa[r] = c0[0]; pa[r + 1] = c0[1];
    }
#pragma unroll
    for (int r = 0; r < 8; r += 2) {
      f32x2 e1 = {__builtin_amdgcn_exp2f(s[8 + r]), __builtin_amdgcn_exp2f(s[8 + r + 1])};
      bf16x2 c1 = __builtin_convertvector(e1, bf16x2);
      pb[r] = c1[0]; pb[r + 1] = c1[1];
    }
    o = __builtin_amdgcn_mfma_f32_32x32x16_bf16(v0, pa, o, 0, 0, 0);
    o = __builtin_amdgcn_mfma_f32_32x32x16_bf16(v1, pb, o, 0, 0, 0);
  }
}

__device__ __forceinline__ float max16(const f32x16 s) {
  float a = __builtin_fmaxf(__builtin_fmaxf(s[0], s[1]), s[2]);
  float c = __builtin_fmaxf(__builtin_fmaxf(s[3], s[4]), s[5]);
  a = __builtin_fmaxf(__builtin_fmaxf(a, s[6]), s[7]);
  c = __builtin_fmaxf(__builtin_fmaxf(c, s[8]), s[9]);
  a = __builtin_fmaxf(__builtin_fmaxf(a, s[10]), s[11]);
  c = __builtin_fmaxf(__builtin_fmaxf(c, s[12]), s[13]);
  a = __builtin_fmaxf(__builtin_fmaxf(a, s[14]), s[15]);
  return __builtin_fmaxf(a, c);
}
__device__ __forceinline__ float xhalf_max(float m) {
  auto rr = __builtin_amdgcn_permlane32_swap(__float_as_uint(m), __float_as_uint(m), false, false);
  return __builtin_fmaxf(__uint_as_float(rr[0]), __uint_as_float(rr[1]));
}

__global__ __launch_bounds__(512, 2) void attn_kernel(const bf16_t* __restrict__ Q, const bf16_t* __restrict__ Kf,
                                                      const bf16_t* __restrict__ Vf, bf16_t* __restrict__ O) {
  extern __shared__ __attribute__((aligned(16))) char smem_g[];
  LDS_AS char* smem = (LDS_AS char*)smem_g;
  const int tid = threadIdx.x, lane = tid & 63;
  const int wave = __builtin_amdgcn_readfirstlane(tid >> 6);
  const int b = blockIdx.x, xcd = b & 7, j = b >> 3;
  const int head = xcd * 4 + (j & 3), qt = j >> 2;
  const int r32 = lane & 31, hh = lane >> 5;
  const int q0 = qt * 256 + wave * 32;

  glds16(Q + ((size_t)head * SEQ + q0 + r32) * 16 + hh * 8, smem + A_QOFF + wave * 1024);
  {
    const bf16_t* ks = Kf + (size_t)head * 32768 + wave * 512 + lane * 8;
    const bf16_t* vs = Vf + (size_t)head * 32768 + wave * 512 + lane * 8;
#pragma unroll
    for (int c = 0; c < 8; ++c) {
      glds16(ks + c * 4096, smem + A_KOFF + c * 8192 + wave * 1024);
      glds16(vs + c * 4096, smem + A_VOFF + c * 8192 + wave * 1024);
    }
  }
  *(LDS_AS u32x4*)(smem + A_ONES + tid * 16) = u32x4{0x3F803F80u, 0x3F803F80u, 0x3F803F80u, 0x3F803F80u};

  LDS_AS char* kb = smem + A_KOFF + lane * 16;
  const bool real = r32 < 16;
  LDS_AS char* vb = real ? (smem + A_VOFF + hh * 256 + r32 * 16) : (smem + A_ONES);
  const int vstride = real ? 8192 : 0;

  wait_bar<14>();
  const bf16x8 qf = *(const LDS_AS bf16x8*)(smem + A_QOFF + wave * 1024 + lane * 16);
  f32x16 negm;
  {
    const bf16x8 kf = *(const LDS_AS bf16x8*)(kb);
    f32x16 z = {};
    f32x16 s = __builtin_amdgcn_mfma_f32_32x32x16_bf16(kf, qf, z, 0, 0, 0);
    const float m = xhalf_max(max16(s));
#pragma unroll
    for (int r = 0; r < 16; ++r) negm[r] = -m;
  }
  f32x16 o = {};
  attn_chunk(kb, vb, qf, negm, o);
  wait_bar<12>(); attn_chunk(kb + 1 * 8192, vb + 1 * vstride, qf, negm, o);
  wait_bar<10>(); attn_chunk(kb + 2 * 8192, vb + 2 * vstride, qf, negm, o);
  wait_bar<8>();  attn_chunk(kb + 3 * 8192, vb + 3 * vstride, qf, negm, o);
  wait_bar<6>();  attn_chunk(kb + 4 * 8192, vb + 4 * vstride, qf, negm, o);
  wait_bar<4>();  attn_chunk(kb + 5 * 8192, vb + 5 * vstride, qf, negm, o);
  wait_bar<2>();  attn_chunk(kb + 6 * 8192, vb + 6 * vstride, qf, negm, o);
  wait_bar<0>();  attn_chunk(kb + 7 * 8192, vb + 7 * vstride, qf, negm, o);

  if (__builtin_expect(__any(!(o[8] < 1.2676506e30f)), 0)) {
    float m = -INFINITY;
    f32x16 z = {};
    for (int t = 0; t < 64; ++t) {
      const bf16x8 kf = *(const LDS_AS bf16x8*)(kb + t * 1024);
      f32x16 s = __builtin_amdgcn_mfma_f32_32x32x16_bf16(kf, qf, z, 0, 0, 0);
      m = __builtin_fmaxf(m, max16(s));
    }
    m = xhalf_max(m);
#pragma unroll
    for (int r = 0; r < 16; ++r) negm[r] = -m;
    o = z;
    for (int c = 0; c < 8; ++c) attn_chunk(kb + c * 8192, vb + c * vstride, qf, negm, o);
  }

  const float inv = __builtin_amdgcn_rcpf(o[8]);
  bf16_t* op = O + (size_t)(q0 + r32) * DM + head * 16 + 4 * hh;
  *(u32x2*)(op) = pack4(o[0] * inv, o[1] * inv, o[2] * inv, o[3] * inv);
  *(u32x2*)(op + 8) = pack4(o[4] * inv, o[5] * inv, o[6] * inv, o[7] * inv);
}

constexpr int QKV_NST = 5, WO_NST = 8, F1_NST = 5, F2_NST = 8;
#define QKV_K gemm_kernel<128, 96, 2, 2, 512, 1536, QKV_NST, 0>
#define WO_K gemm_kernel<64, 64, 2, 2, 512, 512, WO_NST, 1>
#define F1_K gemm_kernel<128, 112, 4, 1, 512, 1792, F1_NST, 2>
#define F2_K gemm_kernel<64, 64, 2, 2, 1792, 512, F2_NST, 3>
constexpr int QKV_LDS = QKV_NST * (128 + 96) * 128, WO_LDS = WO_NST * 128 * 128, F1_LDS = F1_NST * (128 + 112) * 128,
              F2_LDS = F2_NST * 128 * 128;

extern "C" void kernel_launch(void* const* d_in, const int* in_sizes, int n_in, void* d_out, int out_size, void* d_ws,
                              size_t ws_size, hipStream_t stream) {
  const bf16_t* x = (const bf16_t*)d_in[0];
  const bf16_t* wqkv = (const bf16_t*)d_in[1];
  const bf16_t* wo = (const bf16_t*)d_in[2];
  const bf16_t* w1 = (const bf16_t*)d_in[4];
  const bf16_t* b1 = (const bf16_t*)d_in[5];
  const bf16_t* w2 = (const bf16_t*)d_in[6];
  const bf16_t* b2 = (const bf16_t*)d_in[7];
  char* ws = (char*)d_ws;
  bf16_t* Qb = (bf16_t*)(ws + (0 << 20));
  bf16_t* Kb = (bf16_t*)(ws + (2 << 20));
  bf16_t* Vb = (bf16_t*)(ws + (4 << 20));
  bf16_t* Ob = (bf16_t*)(ws + (6 << 20));
  bf16_t* X1 = (bf16_t*)(ws + (8 << 20));
  bf16_t* Hb = (bf16_t*)(ws + (10 << 20));
  bf16_t* X2 = (bf16_t*)(ws + (18 << 20));
  bf16_t* out = (bf16_t*)d_out;

  hipFuncSetAttribute((const void*)QKV_K, hipFuncAttributeMaxDynamicSharedMemorySize, QKV_LDS);
  hipFuncSetAttribute((const void*)WO_K, hipFuncAttributeMaxDynamicSharedMemorySize, WO_LDS);
  hipFuncSetAttribute((const void*)F1_K, hipFuncAttributeMaxDynamicSharedMemorySize, F1_LDS);
  hipFuncSetAttribute((const void*)F2_K, hipFuncAttributeMaxDynamicSharedMemorySize, F2_LDS);
  hipFuncSetAttribute((const void*)attn_kernel, hipFuncAttributeMaxDynamicSharedMemorySize, A_LDS);

  for (int layer = 0; layer < 2; ++layer) {
    const bf16_t* xin = layer == 0 ? x : X2;
    bf16_t* xout = layer == 0 ? X2 : out;
    QKV_K<<<256, 256, QKV_LDS, stream>>>(xin, wqkv, nullptr, nullptr, Qb, Kb, Vb);
    attn_kernel<<<256, 512, A_LDS, stream>>>(Qb, Kb, Vb, Ob);
    WO_K<<<256, 256, WO_LDS, stream>>>(Ob, wo, nullptr, xin, X1, nullptr, nullptr);
    F1_K<<<256, 256, F1_LDS, stream>>>(X1, w1, b1, nullptr, Hb, nullptr, nullptr);
    F2_K<<<256, 256, F2_LDS, stream>>>(Hb, w2, b2, X1, xout, nullptr, nullptr);
  }
}
```

```cpp
#include <hip/hip_runtime.h>
#include <hip/hip_bf16.h>
#include <stdint.h>

typedef __bf16 bf16_t;
typedef __bf16 bf16x2 __attribute__((ext_vector_type(2)));
typedef __bf16 bf16x4 __attribute__((ext_vector_type(4)));
typedef __bf16 bf16x8 __attribute__((ext_vector_type(8)));
typedef float f32x2 __attribute__((ext_vector_type(2)));
typedef float f32x4 __attribute__((ext_vector_type(4)));
typedef float f32x16 __attribute__((ext_vector_type(16)));
typedef unsigned u32x2 __attribute__((ext_vector_type(2)));
typedef unsigned u32x4 __attribute__((ext_vector_type(4)));

#define LDS_AS __attribute__((address_space(3)))
#define GLB_AS __attribute__((address_space(1)))

constexpr int SEQ = 2048, DM = 512, NH = 32, DH = 16, FF = 1792;

__device__ __forceinline__ void glds16(const void* g, unsigned lds_base) {
  unsigned keep;
  asm volatile("s_mov_b32 %0, m0\n\ts_mov_b32 m0, %2\n\ts_nop 0\n\tglobal_load_lds_dwordx4 %1, off\n\ts_mov_b32 m0, %0"
               : "=&s"(keep) : "v"(g), "s"(lds_base) : "memory");
}
#define SBAR() __builtin_amdgcn_sched_barrier(0)
#define PIN(x) asm volatile("" : "+v"(x))
__device__ __forceinline__ float rbf(float v) { return (float)(__bf16)v; }
__device__ __forceinline__ u32x2 pack4(float a, float b, float c, float d) {
  f32x2 lo = {a, b}, hi = {c, d};
  bf16x2 l = __builtin_convertvector(lo, bf16x2), h = __builtin_convertvector(hi, bf16x2);
  u32x2 r = {__builtin_bit_cast(unsigned, l), __builtin_bit_cast(unsigned, h)};
  return r;
}
__device__ __forceinline__ float bf_lo(unsigned u) { return __builtin_bit_cast(float, u << 16); }
__device__ __forceinline__ float bf_hi(unsigned u) { return __builtin_bit_cast(float, u & 0xffff0000u); }

template <int N>
__device__ __forceinline__ void wait_bar() {
  asm volatile("s_waitcnt vmcnt(%0) lgkmcnt(0)\n\ts_barrier" ::"i"(N) : "memory");
}

__device__ __forceinline__ float gelu_tanh(float x) {
  const float c1 = -2.0f * 0.7978845608028654f * 1.4426950408889634f;
  const float c2 = c1 * 0.044715f;
  float t = x * __builtin_fmaf(x * x, c2, c1);
  float e = __builtin_amdgcn_exp2f(t);
  return x * __builtin_amdgcn_rcpf(1.0f + e);
}

template <int BM, int BN, int WVM, int WVN, int K, int LDW, int NST, int MODE>
__global__ __launch_bounds__(256, 1) void gemm_kernel(const bf16_t* __restrict__ X, const bf16_t* __restrict__ W,
                                                      const bf16_t* __restrict__ bias, const bf16_t* __restrict__ res,
                                                      bf16_t* __restrict__ out0, bf16_t* __restrict__ out1,
                                                      bf16_t* __restrict__ out2) {
  extern __shared__ __attribute__((aligned(16))) char smem_g[];
  LDS_AS char* smem = (LDS_AS char*)smem_g;
  constexpr int TM = BM / WVM / 16, TN = BN / WVN / 16, NB = BN / 16;
  constexpr int XB = BM * 128, WB = BN * 128, STB = XB + WB;
  constexpr int NXI = BM / 8, NI = (BM + BN) / 8, LPW = (NI + 3) / 4, NK = K / 64;
  constexpr bool LN = (MODE == 0 || MODE == 2);

  const int tid = threadIdx.x, lane = tid & 63;
  const int wave = __builtin_amdgcn_readfirstlane(tid >> 6);
  const int wm = wave / WVN, wn = wave % WVN;
  const int b = blockIdx.x, xcd = b & 7, j = b >> 3;
  int mt, nt;
  if (BM == 128) { mt = (xcd >> 1) * 4 + (j & 3); nt = (xcd & 1) * 8 + (j >> 2); }
  else           { mt = (xcd >> 1) * 8 + (j & 7); nt = (xcd & 1) * 4 + (j >> 3); }
  const int m0 = mt * BM;
  const int l15 = lane & 15, g = lane >> 4;

  auto colbase = [&](int nb) -> int {
    if (MODE == 0) return (nb >> 1) * 512 + (nt * 2 + (nb & 1)) * 16;
    return nt * BN + nb * 16;
  };
  auto nb_of = [&](int ni) -> int {
    if (MODE == 0) return 2 * ni + wn;
    return wn * TN + ni;
  };

  u32x2 resv[TM][TN];
  u32x2 biasv[TN];
  if (MODE == 1 || MODE == 3) {
#pragma unroll
    for (int mi = 0; mi < TM; ++mi)
#pragma unroll
      for (int ni = 0; ni < TN; ++ni)
        resv[mi][ni] = *(const u32x2*)(res + (size_t)(m0 + wm * TM * 16 + mi * 16 + l15) * DM + colbase(nb_of(ni)) + 4 * g);
  }
  if (MODE == 2 || MODE == 3) {
#pragma unroll
    for (int ni = 0; ni < TN; ++ni) biasv[ni] = *(const u32x2*)(bias + colbase(nb_of(ni)) + 4 * g);
  }

  const bf16_t* src[LPW];
  int inc[LPW];
  int dsto[LPW];
#pragma unroll
  for (int i = 0; i < LPW; ++i) {
    int ii = wave + 4 * i;
    if (ii >= NI) ii -= 4;
    if (ii < NXI) {
      const int row = 8 * ii + (lane >> 3);
      const int ch = (lane & 7) ^ ((row >> 1) & 7);
      src[i] = X + (size_t)(m0 + row) * K + ch * 8;
      inc[i] = 64;
    } else {
      const int wi = ii - NXI;
      const int sidx = 4 * wi + (lane >> 4);
      const int kb = sidx / NB, nb = sidx % NB;
      const int r = (lane >> 1) & 7, half = lane & 1;
      const int krow = kb * 8 + (r ^ ((kb & 1) << 2));
      src[i] = W + (size_t)krow * LDW + colbase(nb) + half * 8;
      inc[i] = 64 * LDW;
    }
    dsto[i] = ii * 1024;
  }
  const unsigned lds0 = (unsigned)(uintptr_t)smem;
  auto issue = [&](int st) {
    const unsigned base = lds0 + (st % NST) * STB;
#pragma unroll
    for (int i = 0; i < LPW; ++i) {
      glds16(src[i], __builtin_amdgcn_readfirstlane(base + dsto[i]));
      src[i] += inc[i];
    }
  };

  const int xo0 = (wm * TM * 16 + l15) * 128 + ((g ^ (l15 >> 1)) * 16);
  const int q_ = (lane & 15) >> 2, p_ = lane & 3;
  const int wo0 = XB + g * NB * 256 + (q_ + 4 * (g & 1)) * 32 + p_ * 8;
  const int wo1 = XB + g * NB * 256 + (q_ + 4 * (1 - (g & 1))) * 32 + p_ * 8;

  f32x4 acc[TM][TN];
#pragma unroll
  for (int mi = 0; mi < TM; ++mi)
#pragma unroll
    for (int ni = 0; ni < TN; ++ni) acc[mi][ni] = f32x4{0.f, 0.f, 0.f, 0.f};
  float rs[TM], rq[TM], cs[TN];
#pragma unroll
  for (int mi = 0; mi < TM; ++mi) rs[mi] = rq[mi] = 0.f;
#pragma unroll
  for (int ni = 0; ni < TN; ++ni) cs[ni] = 0.f;
  const bf16x2 ones2 = {(__bf16)1.0f, (__bf16)1.0f};

#pragma unroll
  for (int s = 0; s < NST - 1; ++s) issue(s);

#pragma unroll
  for (int kt = 0; kt < NK; ++kt) {
    constexpr int dummy = 0; (void)dummy;
    const int rem = (NK - 1 - kt) < (NST - 2) ? (NK - 1 - kt) : (NST - 2);
    switch (rem * LPW) {
#define WB_CASE(n) case n: wait_bar<n>(); break;
      WB_CASE(0) WB_CASE(4) WB_CASE(7) WB_CASE(8) WB_CASE(12) WB_CASE(14) WB_CASE(16) WB_CASE(20) WB_CASE(21) WB_CASE(24)
      WB_CASE(28) WB_CASE(32) WB_CASE(35) WB_CASE(40) WB_CASE(42) WB_CASE(48)
#undef WB_CASE
      default: wait_bar<0>(); break;
    }
    if (kt + NST - 1 < NK) issue(kt + NST - 1);
    LDS_AS char* base = smem + (kt % NST) * STB;
#pragma unroll
    for (int ks = 0; ks < 2; ++ks) {
      bf16x8 xf[TM], wf[TN];
#pragma unroll
      for (int mi = 0; mi < TM; ++mi) xf[mi] = *(const LDS_AS bf16x8*)(base + ((xo0 ^ (ks * 64)) + mi * 2048));
#pragma unroll
      for (int ni = 0; ni < TN; ++ni) {
        const int o = (ks * 4 * NB + nb_of(ni)) * 256;
        bf16x4 a = __builtin_amdgcn_ds_read_tr16_b64_v4bf16((LDS_AS bf16x4*)(base + wo0 + o));
        bf16x4 c = __builtin_amdgcn_ds_read_tr16_b64_v4bf16((LDS_AS bf16x4*)(base + wo1 + o));
        wf[ni] = __builtin_shufflevector(a, c, 0, 1, 2, 3, 4, 5, 6, 7);
      }
#pragma unroll
      for (int mi = 0; mi < TM; ++mi)
#pragma unroll
        for (int ni = 0; ni < TN; ++ni) {
          if (MODE == 0 && ni == 2) acc[mi][ni] = __builtin_amdgcn_mfma_f32_16x16x32_bf16(xf[mi], wf[ni], acc[mi][ni], 0, 0, 0);
          else                      acc[mi][ni] = __builtin_amdgcn_mfma_f32_16x16x32_bf16(wf[ni], xf[mi], acc[mi][ni], 0, 0, 0);
        }
      if (LN) {
#pragma unroll
        for (int mi = 0; mi < TM; ++mi)
#pragma unroll
          for (int e = 0; e < 4; ++e) {
            bf16x2 pr = {xf[mi][2 * e], xf[mi][2 * e + 1]};
            rs[mi] = __builtin_amdgcn_fdot2_f32_bf16(pr, ones2, rs[mi], false);
            rq[mi] = __builtin_amdgcn_fdot2_f32_bf16(pr, pr, rq[mi], false);
          }
#pragma unroll
        for (int ni = 0; ni < TN; ++ni)
#pragma unroll
          for (int e = 0; e < 4; ++e) {
            bf16x2 pr = {wf[ni][2 * e], wf[ni][2 * e + 1]};
            cs[ni] = __builtin_amdgcn_fdot2_f32_bf16(pr, ones2, cs[ni], false);
          }
#pragma unroll
        for (int mi = 0; mi < TM; ++mi) { PIN(rs[mi]); PIN(rq[mi]); }
#pragma unroll
        for (int ni = 0; ni < TN; ++ni) PIN(cs[ni]);
      }
    }
  }

  float mean[TM], rstd[TM];
  if (LN) {
#pragma unroll
    for (int mi = 0; mi < TM; ++mi) {
      float s = rs[mi], q = rq[mi];
      s += __shfl_xor(s, 16); s += __shfl_xor(s, 32);
      q += __shfl_xor(q, 16); q += __shfl_xor(q, 32);
      const float m = s * (1.0f / K);
      const float v = __builtin_fmaxf(q * (1.0f / K) - m * m, 0.f);
      mean[mi] = m;
      rstd[mi] = __builtin_amdgcn_rsqf(v + 1e-5f);
    }
#pragma unroll
    for (int ni = 0; ni < TN; ++ni) {
      float c = cs[ni];
      c += __shfl_xor(c, 16); c += __shfl_xor(c, 32);
      cs[ni] = c;
    }
  }

  if (MODE == 0) {
    const int head = nt * 2 + wn;
    const float qscale = 0.25f * 1.4426950408889634f;
    float csr[2][4];
#pragma unroll
    for (int ni = 0; ni < 2; ++ni)
#pragma unroll
      for (int r = 0; r < 4; ++r) csr[ni][r] = __shfl(cs[ni], 4 * g + r);
#pragma unroll
    for (int mi = 0; mi < TM; ++mi) {
      const int tok = m0 + wm * TM * 16 + mi * 16 + l15;
      {
        const float sc = rstd[mi] * qscale;
        float v[4];
#pragma unroll
        for (int r = 0; r < 4; ++r) v[r] = sc * (acc[mi][0][r] - mean[mi] * csr[0][r]);
        *(u32x2*)(out0 + ((size_t)head * SEQ + tok) * 16 + 4 * g) = pack4(v[0], v[1], v[2], v[3]);
      }
      {
        float v[4];
#pragma unroll
        for (int r = 0; r < 4; ++r) v[r] = rstd[mi] * (acc[mi][1][r] - mean[mi] * csr[1][r]);
        *(u32x2*)(out1 + (size_t)head * 32768 + (tok >> 5) * 512 + (g >> 1) * 256 + (tok & 31) * 8 + (g & 1) * 4) =
            pack4(v[0], v[1], v[2], v[3]);
      }
      {
        float v[4];
#pragma unroll
        for (int r = 0; r < 4; ++r) {
          const float mr = __shfl(mean[mi], 4 * g + r), rr = __shfl(rstd[mi], 4 * g + r);
          v[r] = rr * (acc[mi][2][r] - mr * cs[2]);
        }
        const int ks16 = (m0 + wm * TM * 16 + mi * 16) >> 4;
        *(u32x2*)(out2 + (size_t)head * 32768 + ks16 * 256 + (g & 1) * 128 + l15 * 8 + (g >> 1) * 4) =
            pack4(v[0], v[1], v[2], v[3]);
      }
    }
  } else if (MODE == 2) {
#pragma unroll
    for (int ni = 0; ni < TN; ++ni) {
      float csr[4];
#pragma unroll
      for (int r = 0; r < 4; ++r) csr[r] = __shfl(cs[ni], 4 * g + r);
      const float bb[4] = {bf_lo(biasv[ni][0]), bf_hi(biasv[ni][0]), bf_lo(biasv[ni][1]), bf_hi(biasv[ni][1])};
#pragma unroll
      for (int mi = 0; mi < TM; ++mi) {
        const int tok = m0 + wm * TM * 16 + mi * 16 + l15;
        float v[4];
#pragma unroll
        for (int r = 0; r < 4; ++r) {
          float t = rstd[mi] * (acc[mi][ni][r] - mean[mi] * csr[r]);
          t = rbf(rbf(t) + bb[r]);
          v[r] = gelu_tanh(t);
        }
        *(u32x2*)(out0 + (size_t)tok * FF + colbase(nb_of(ni)) + 4 * g) = pack4(v[0], v[1], v[2], v[3]);
      }
    }
  } else {
#pragma unroll
    for (int mi = 0; mi < TM; ++mi)
#pragma unroll
      for (int ni = 0; ni < TN; ++ni) {
        const int tok = m0 + wm * TM * 16 + mi * 16 + l15;
        const float rr[4] = {bf_lo(resv[mi][ni][0]), bf_hi(resv[mi][ni][0]), bf_lo(resv[mi][ni][1]), bf_hi(resv[mi][ni][1])};
        float v[4];
#pragma unroll
        for (int r = 0; r < 4; ++r) {
          float t = rbf(acc[mi][ni][r]);
          if (MODE == 3) {
            const float bb = (r == 0) ? bf_lo(biasv[ni][0]) : (r == 1) ? bf_hi(biasv[ni][0]) : (r == 2) ? bf_lo(biasv[ni][1]) : bf_hi(biasv[ni][1]);
            t = rbf(t + bb);
          }
          v[r] = rr[r] + t;
        }
        *(u32x2*)(out0 + (size_t)tok * DM + colbase(nb_of(ni)) + 4 * g) = pack4(v[0], v[1], v[2], v[3]);
      }
  }
}

constexpr int A_KOFF = 0, A_VOFF = 65536, A_ONES = 131072, A_QOFF = 139264, A_LDS = 147456;

#define MFMA32(a, b, c) __builtin_amdgcn_mfma_f32_32x32x16_bf16(a, b, c, 0, 0, 0)
__device__ __forceinline__ bf16x8 exp_pack8(const f32x16& s, int o) {
  bf16x8 p;
#pragma unroll
  for (int r = 0; r < 8; r += 2) {
    f32x2 e = {__builtin_amdgcn_exp2f(s[o + r]), __builtin_amdgcn_exp2f(s[o + r + 1])};
    bf16x2 c = __builtin_convertvector(e, bf16x2);
    p[r] = c[0]; p[r + 1] = c[1];
  }
  return p;
}
#define LDF(p) (*(const LDS_AS bf16x8*)(p))
#define ASTEP(SC, SN, VA, VB, VNA, VNB, KO, VO)                              \
  do {                                                                       \
    SN = MFMA32(kf, qf, negm); SBAR();                                       \
    kf = LDF(kp + (KO)); VNA = LDF(vp + (VO)); VNB = LDF(vp + (VO) + 512); SBAR(); \
    { bf16x8 pa = exp_pack8(SC, 0); SBAR(); o = MFMA32(VA, pa, o); SBAR(); } \
    { bf16x8 pb = exp_pack8(SC, 8); SBAR(); o = MFMA32(VB, pb, o); SBAR(); } \
  } while (0)

__device__ __forceinline__ float max16(const f32x16 s) {
  float a = __builtin_fmaxf(__builtin_fmaxf(s[0], s[1]), s[2]);
  float c = __builtin_fmaxf(__builtin_fmaxf(s[3], s[4]), s[5]);
  a = __builtin_fmaxf(__builtin_fmaxf(a, s[6]), s[7]);
  c = __builtin_fmaxf(__builtin_fmaxf(c, s[8]), s[9]);
  a = __builtin_fmaxf(__builtin_fmaxf(a, s[10]), s[11]);
  c = __builtin_fmaxf(__builtin_fmaxf(c, s[12]), s[13]);
  a = __builtin_fmaxf(__builtin_fmaxf(a, s[14]), s[15]);
  return __builtin_fmaxf(a, c);
}
__device__ __forceinline__ float xhalf_max(float m) {
  auto rr = __builtin_amdgcn_permlane32_swap(__float_as_uint(m), __float_as_uint(m), false, false);
  return __builtin_fmaxf(__uint_as_float(rr[0]), __uint_as_float(rr[1]));
}

__global__ __launch_bounds__(512, 2) void attn_kernel(const bf16_t* __restrict__ Q, const bf16_t* __restrict__ Kf,
                                                      const bf16_t* __restrict__ Vf, bf16_t* __restrict__ O) {
  extern __shared__ __attribute__((aligned(16))) char smem_g[];
  LDS_AS char* smem = (LDS_AS char*)smem_g;
  const int tid = threadIdx.x, lane = tid & 63;
  const int wave = __builtin_amdgcn_readfirstlane(tid >> 6);
  const int b = blockIdx.x, xcd = b & 7, j = b >> 3;
  const int head = xcd * 4 + (j & 3), qt = j >> 2;
  const int r32 = lane & 31, hh = lane >> 5;
  const int q0 = qt * 256 + wave * 32;

  const unsigned lds0 = (unsigned)(uintptr_t)smem;
  glds16(Q + ((size_t)head * SEQ + q0 + r32) * 16 + hh * 8, lds0 + A_QOFF + wave * 1024);
  {
    const bf16_t* ks = Kf + (size_t)head * 32768 + wave * 512 + lane * 8;
    const bf16_t* vs = Vf + (size_t)head * 32768 + wave * 512 + lane * 8;
#pragma unroll
    for (int c = 0; c < 8; ++c) {
      glds16(ks + c * 4096, lds0 + A_KOFF + c * 8192 + wave * 1024);
      glds16(vs + c * 4096, lds0 + A_VOFF + c * 8192 + wave * 1024);
    }
  }
  *(LDS_AS u32x4*)(smem + A_ONES + tid * 16) = u32x4{0x3F803F80u, 0x3F803F80u, 0x3F803F80u, 0x3F803F80u};

  LDS_AS char* kb = smem + A_KOFF + lane * 16;
  const bool real = r32 < 16;
  LDS_AS char* vb = real ? (smem + A_VOFF + hh * 256 + r32 * 16) : (smem + A_ONES);
  const int vstride = real ? 2048 : 0;

  wait_bar<14>();
  const bf16x8 qf = *(const LDS_AS bf16x8*)(smem + A_QOFF + wave * 1024 + lane * 16);
  f32x16 negm;
  {
    const bf16x8 kf = *(const LDS_AS bf16x8*)(kb);
    f32x16 z = {};
    f32x16 s = __builtin_amdgcn_mfma_f32_32x32x16_bf16(kf, qf, z, 0, 0, 0);
    const float m = xhalf_max(max16(s));
#pragma unroll
    for (int r = 0; r < 16; ++r) negm[r] = -m;
  }
  f32x16 o = {};
#pragma unroll 1
  for (int pass = 0; pass < 2; ++pass) {
    LDS_AS char* kp = kb;
    LDS_AS char* vp = vb;
    bf16x8 kf = LDF(kp), va0 = LDF(vp), vb0 = LDF(vp + 512), va1, vb1;
    f32x16 sA = MFMA32(kf, qf, negm), sB;
    kf = LDF(kp + 1024);
    SBAR();
#pragma unroll 1
    for (int t = 0; t < 64; t += 2) {
      if (t == 6 && pass == 0) wait_bar<0>();
      ASTEP(sA, sB, va0, vb0, va1, vb1, 2048, 1024);
      ASTEP(sB, sA, va1, vb1, va0, vb0, 3072, 2048);
      kp += 2048; vp += vstride;
    }
    if (__builtin_expect(pass == 1 || !__any(!(o[8] < 1.2676506e30f)), 1)) break;
    float m = -INFINITY;
    f32x16 z = {};
    for (int t = 0; t < 64; ++t) {
      f32x16 sc = MFMA32(LDF(kb + t * 1024), qf, z);
      m = __builtin_fmaxf(m, max16(sc));
    }
    m = xhalf_max(m);
#pragma unroll
    for (int r = 0; r < 16; ++r) negm[r] = -m;
    o = z;
  }

  const float inv = __builtin_amdgcn_rcpf(o[8]);
  bf16_t* op = O + (size_t)(q0 + r32) * DM + head * 16 + 4 * hh;
  *(u32x2*)(op) = pack4(o[0] * inv, o[1] * inv, o[2] * inv, o[3] * inv);
  *(u32x2*)(op + 8) = pack4(o[4] * inv, o[5] * inv, o[6] * inv, o[7] * inv);
}

constexpr int QKV_NST = 5, WO_NST = 8, F1_NST = 5, F2_NST = 8;
#define QKV_K gemm_kernel<128, 96, 2, 2, 512, 1536, QKV_NST, 0>
#define WO_K gemm_kernel<64, 64, 2, 2, 512, 512, WO_NST, 1>
#define F1_K gemm_kernel<128, 112, 4, 1, 512, 1792, F1_NST, 2>
#define F2_K gemm_kernel<64, 64, 2, 2, 1792, 512, F2_NST, 3>
constexpr int QKV_LDS = QKV_NST * (128 + 96) * 128, WO_LDS = WO_NST * 128 * 128, F1_LDS = F1_NST * (128 + 112) * 128,
              F2_LDS = F2_NST * 128 * 128;

extern "C" void kernel_launch(void* const* d_in, const int* in_sizes, int n_in, void* d_out, int out_size, void* d_ws,
                              size_t ws_size, hipStream_t stream) {
  const bf16_t* x = (const bf16_t*)d_in[0];
  const bf16_t* wqkv = (const bf16_t*)d_in[1];
  const bf16_t* wo = (const bf16_t*)d_in[2];
  const bf16_t* w1 = (const bf16_t*)d_in[4];
  const bf16_t* b1 = (const bf16_t*)d_in[5];
  const bf16_t* w2 = (const bf16_t*)d_in[6];
  const bf16_t* b2 = (const bf16_t*)d_in[7];
  char* ws = (char*)d_ws;
  bf16_t* Qb = (bf16_t*)(ws + (0 << 20));
  bf16_t* Kb = (bf16_t*)(ws + (2 << 20));
  bf16_t* Vb = (bf16_t*)(ws + (4 << 20));
  bf16_t* Ob = (bf16_t*)(ws + (6 << 20));
  bf16_t* X1 = (bf16_t*)(ws + (8 << 20));
  bf16_t* Hb = (bf16_t*)(ws + (10 << 20));
  bf16_t* X2 = (bf16_t*)(ws + (18 << 20));
  bf16_t* out = (bf16_t*)d_out;

  hipFuncSetAttribute((const void*)QKV_K, hipFuncAttributeMaxDynamicSharedMemorySize, QKV_LDS);
  hipFuncSetAttribute((const void*)WO_K, hipFuncAttributeMaxDynamicSharedMemorySize, WO_LDS);
  hipFuncSetAttribute((const void*)F1_K, hipFuncAttributeMaxDynamicSharedMemorySize, F1_LDS);
  hipFuncSetAttribute((const void*)F2_K, hipFuncAttributeMaxDynamicSharedMemorySize, F2_LDS);
  hipFuncSetAttribute((const void*)attn_kernel, hipFuncAttributeMaxDynamicSharedMemorySize, A_LDS);

  for (int layer = 0; layer < 2; ++layer) {
    const bf16_t* xin = layer == 0 ? x : X2;
    bf16_t* xout = layer == 0 ? X2 : out;
    QKV_K<<<256, 256, QKV_LDS, stream>>>(xin, wqkv, nullptr, nullptr, Qb, Kb, Vb);
    attn_kernel<<<256, 512, A_LDS, stream>>>(Qb, Kb, Vb, Ob);
    WO_K<<<256, 256, WO_LDS, stream>>>(Ob, wo, nullptr, xin, X1, nullptr, nullptr);
    F1_K<<<256, 256, F1_LDS, stream>>>(X1, w1, b1, nullptr, Hb, nullptr, nullptr);
    F2_K<<<256, 256, F2_LDS, stream>>>(Hb, w2, b2, X1, xout, nullptr, nullptr);
  }
}
```

```cpp
#include <hip/hip_runtime.h>
#include <hip/hip_bf16.h>
#include <stdint.h>

typedef __bf16 bf16_t;
typedef __bf16 bf16x2 __attribute__((ext_vector_type(2)));
typedef __bf16 bf16x4 __attribute__((ext_vector_type(4)));
typedef __bf16 bf16x8 __attribute__((ext_vector_type(8)));
typedef float f32x2 __attribute__((ext_vector_type(2)));
typedef float f32x4 __attribute__((ext_vector_type(4)));
typedef float f32x16 __attribute__((ext_vector_type(16)));
typedef unsigned u32x2 __attribute__((ext_vector_type(2)));
typedef unsigned u32x4 __attribute__((ext_vector_type(4)));

#define LDS_AS __attribute__((address_space(3)))
#define GLB_AS __attribute__((address_space(1)))

constexpr int SEQ = 2048, DM = 512, NH = 32, DH = 16, FF = 1792;

__device__ __forceinline__ void glds16(const void* g, unsigned lds_base) {
  unsigned keep;
  asm volatile("s_mov_b32 %0, m0\n\ts_mov_b32 m0, %2\n\ts_nop 0\n\tglobal_load_lds_dwordx4 %1, off\n\ts_mov_b32 m0, %0"
               : "=&s"(keep) : "v"(g), "s"(lds_base) : "memory");
}
#define SBAR() __builtin_amdgcn_sched_barrier(0)
#define PIN(x) asm volatile("" : "+v"(x))
__device__ __forceinline__ float rbf(float v) { return (float)(__bf16)v; }
__device__ __forceinline__ u32x2 pack4(float a, float b, float c, float d) {
  f32x2 lo = {a, b}, hi = {c, d};
  bf16x2 l = __builtin_convertvector(lo, bf16x2), h = __builtin_convertvector(hi, bf16x2);
  u32x2 r = {__builtin_bit_cast(unsigned, l), __builtin_bit_cast(unsigned, h)};
  return r;
}
__device__ __forceinline__ float bf_lo(unsigned u) { return __builtin_bit_cast(float, u << 16); }
__device__ __forceinline__ float bf_hi(unsigned u) { return __builtin_bit_cast(float, u & 0xffff0000u); }

#define GST(i)
template <int N>
__device__ __forceinline__ void wait_bar() {
  asm volatile("s_waitcnt vmcnt(%0) lgkmcnt(0)\n\ts_barrier" ::"i"(N) : "memory");
}

template <int I> struct IC { static constexpr int value = I; };
template <int I, int N, class F>
__device__ __forceinline__ void static_for(F& f) {
  if constexpr (I < N) { f(IC<I>{}); static_for<I + 1, N>(f); }
}

__device__ __forceinline__ float gelu_tanh(float x) {
  const float c1 = -2.0f * 0.7978845608028654f * 1.4426950408889634f;
  const float c2 = c1 * 0.044715f;
  float t = x * __builtin_fmaf(x * x, c2, c1);
  float e = __builtin_amdgcn_exp2f(t);
  return x * __builtin_amdgcn_rcpf(1.0f + e);
}

template <int N>
__device__ __forceinline__ void wait_vm() { asm volatile("s_waitcnt vmcnt(%0)" ::"i"(N) : "memory"); }
__device__ __forceinline__ void bar_lgkm() { asm volatile("s_waitcnt lgkmcnt(0)\n\ts_barrier" ::: "memory"); }

template <int BM, int BN, int WVM, int WVN, int K, int LDW, int NST, int MODE>
__global__ __launch_bounds__(512, 2) void gemm_kernel(const bf16_t* __restrict__ X, const bf16_t* __restrict__ W,
                                                      const bf16_t* __restrict__ bias, const bf16_t* __restrict__ res,
                                                      bf16_t* __restrict__ out0, bf16_t* __restrict__ out1,
                                                      bf16_t* __restrict__ out2) {
  extern __shared__ __attribute__((aligned(16))) char smem_g[];
  LDS_AS char* smem = (LDS_AS char*)smem_g;
  constexpr int TM = BM / WVM / 16, TN = BN / WVN / 16, NB = BN / 16, HM = TM / 2;
  constexpr int XB = BM * 128, WB = BN * 128, STB = XB + WB;
  constexpr int NXI = BM / 8, NI = (BM + BN) / 8, LPW = (NI + 7) / 8, NK = K / 64;
  constexpr int NFULL = NI - 8 * (LPW - 1);
  constexpr bool LN = (MODE == 0 || MODE == 2);

  const int tid = threadIdx.x, lane = tid & 63;
  const int wave = __builtin_amdgcn_readfirstlane(tid >> 6);
  const int kg = wave >> 2, w4 = wave & 3;
  const int wm = w4 / WVN, wn = w4 % WVN;
  const int b = blockIdx.x, xcd = b & 7, j = b >> 3;
  int mt, nt;
  if (BM == 128) { mt = (xcd >> 1) * 4 + (j & 3); nt = (xcd & 1) * 8 + (j >> 2); }
  else           { mt = (xcd >> 1) * 8 + (j & 7); nt = (xcd & 1) * 4 + (j >> 3); }
  GST(0);
  const int m0 = mt * BM;
  const int l15 = lane & 15, g = lane >> 4;

  auto colbase = [&](int nb) -> int {
    if (MODE == 0) return (nb >> 1) * 512 + (nt * 2 + (nb & 1)) * 16;
    return nt * BN + nb * 16;
  };
  auto nb_of = [&](int ni) -> int {
    if (MODE == 0) return 2 * ni + wn;
    return wn * TN + ni;
  };
  const int tok0 = m0 + wm * TM * 16 + kg * HM * 16;

  const bf16_t* src[LPW];
  int inc[LPW];
#pragma unroll
  for (int i = 0; i < LPW; ++i) {
    int ii = wave + 8 * i;
    if (ii >= NI) ii -= 8;
    if (ii < NXI) {
      const int row = 8 * ii + (lane >> 3);
      const int ch = (lane & 7) ^ ((row >> 1) & 7);
      src[i] = X + (size_t)(m0 + row) * K + ch * 8;
      inc[i] = 64;
    } else {
      const int wi = ii - NXI;
      const int sidx = 4 * wi + (lane >> 4);
      const int kb = sidx / NB, nb = sidx % NB;
      const int r = (lane >> 1) & 7, half = lane & 1;
      const int krow = kb * 8 + (r ^ ((kb & 1) << 2));
      src[i] = W + (size_t)krow * LDW + colbase(nb) + half * 8;
      inc[i] = 64 * LDW;
    }
  }
  const unsigned lds0 = (unsigned)(uintptr_t)smem + wave * 1024;
  const bool full = wave < NFULL;
  auto issue = [&](int st) {
    const unsigned base = lds0 + (st % NST) * STB;
#pragma unroll
    for (int i = 0; i < LPW; ++i) {
      if (i < LPW - 1 || NFULL == 8 || full) glds16(src[i], base + i * 8192);
      src[i] += inc[i];
    }
  };
  u32x2 resv[HM][TN];
  u32x2 biasv[TN];
  if (MODE == 1 || MODE == 3) {
#pragma unroll
    for (int mi = 0; mi < HM; ++mi)
#pragma unroll
      for (int ni = 0; ni < TN; ++ni)
        resv[mi][ni] = *(const u32x2*)(res + (size_t)(tok0 + mi * 16 + l15) * DM + colbase(nb_of(ni)) + 4 * g);
  }
  if (MODE == 2 || MODE == 3) {
#pragma unroll
    for (int ni = 0; ni < TN; ++ni) biasv[ni] = *(const u32x2*)(bias + colbase(nb_of(ni)) + 4 * g);
  }
#pragma unroll
  for (int s = 0; s < NST - 1; ++s) issue(s);

  const int xo_ = ((wm * TM * 16 + l15) * 128 + ((g ^ (l15 >> 1)) * 16)) ^ (kg * 64);
  const int xoA = xo_ + kg * HM * 2048, xoB = xo_ + (1 - kg) * HM * 2048;
  const int q_ = (lane & 15) >> 2, p_ = lane & 3;
  const int wo0 = XB + (kg * 4 + g) * NB * 256 + (q_ + 4 * (g & 1)) * 32 + p_ * 8;
  const int wo1 = XB + (kg * 4 + g) * NB * 256 + (q_ + 4 * (1 - (g & 1))) * 32 + p_ * 8;

  f32x4 acc[TM][TN];
#pragma unroll
  for (int mi = 0; mi < TM; ++mi)
#pragma unroll
    for (int ni = 0; ni < TN; ++ni) acc[mi][ni] = f32x4{0.f, 0.f, 0.f, 0.f};
  float rs[TM], rq[TM], cs[TN];
#pragma unroll
  for (int mi = 0; mi < TM; ++mi) rs[mi] = rq[mi] = 0.f;
#pragma unroll
  for (int ni = 0; ni < TN; ++ni) cs[ni] = 0.f;
  const bf16x2 ones2 = {(__bf16)1.0f, (__bf16)1.0f};

  auto kstep = [&](auto KT) {
    constexpr int kt = decltype(KT)::value;
    constexpr int rem = (NK - 1 - kt) < (NST - 2) ? (NK - 1 - kt) : (NST - 2);
    if (NFULL == 8 || full) wait_vm<rem * LPW>(); else wait_vm<rem * (LPW - 1)>();
    bar_lgkm();
    GST(1 + kt);
    if (kt + NST - 1 < NK) issue(kt + NST - 1);
    LDS_AS char* base = smem + (kt % NST) * STB;
    bf16x8 xf[TM], wf[TN];
#pragma unroll
    for (int mi = 0; mi < TM; ++mi) xf[mi] = *(const LDS_AS bf16x8*)(base + (mi < HM ? xoA + mi * 2048 : xoB + (mi - HM) * 2048));
#pragma unroll
    for (int ni = 0; ni < TN; ++ni) {
      const int o = nb_of(ni) * 256;
      bf16x4 a = __builtin_amdgcn_ds_read_tr16_b64_v4bf16((LDS_AS bf16x4*)(base + wo0 + o));
      bf16x4 c = __builtin_amdgcn_ds_read_tr16_b64_v4bf16((LDS_AS bf16x4*)(base + wo1 + o));
      wf[ni] = __builtin_shufflevector(a, c, 0, 1, 2, 3, 4, 5, 6, 7);
    }
#pragma unroll
    for (int mi = 0; mi < TM; ++mi)
#pragma unroll
      for (int ni = 0; ni < TN; ++ni) {
        if (MODE == 0 && ni == 2) acc[mi][ni] = __builtin_amdgcn_mfma_f32_16x16x32_bf16(xf[mi], wf[ni], acc[mi][ni], 0, 0, 0);
        else                      acc[mi][ni] = __builtin_amdgcn_mfma_f32_16x16x32_bf16(wf[ni], xf[mi], acc[mi][ni], 0, 0, 0);
      }
    if (LN) {
#pragma unroll
      for (int mi = 0; mi < TM; ++mi)
#pragma unroll
        for (int e = 0; e < 4; ++e) {
          bf16x2 pr = {xf[mi][2 * e], xf[mi][2 * e + 1]};
          rs[mi] = __builtin_amdgcn_fdot2_f32_bf16(pr, ones2, rs[mi], false);
          rq[mi] = __builtin_amdgcn_fdot2_f32_bf16(pr, pr, rq[mi], false);
        }
#pragma unroll
      for (int ni = 0; ni < TN; ++ni)
#pragma unroll
        for (int e = 0; e < 4; ++e) {
          bf16x2 pr = {wf[ni][2 * e], wf[ni][2 * e + 1]};
          cs[ni] = __builtin_amdgcn_fdot2_f32_bf16(pr, ones2, cs[ni], false);
        }
#pragma unroll
      for (int mi = 0; mi < TM; ++mi) { PIN(rs[mi]); PIN(rq[mi]); }
#pragma unroll
      for (int ni = 0; ni < TN; ++ni) PIN(cs[ni]);
    }
  };
  static_for<0, NK>(kstep);

  GST(1 + NK);
  if (MODE == 1 || MODE == 3) {
#pragma unroll
    for (int mi = 0; mi < HM; ++mi)
#pragma unroll
      for (int ni = 0; ni < TN; ++ni) PIN(resv[mi][ni]);
  }
  if (MODE == 2 || MODE == 3) {
#pragma unroll
    for (int ni = 0; ni < TN; ++ni) PIN(biasv[ni]);
  }
  bar_lgkm();
  constexpr int NX4 = HM * TN + (LN ? (2 * HM + TN + 3) / 4 : 0);
  {
    LDS_AS f32x4* wr = (LDS_AS f32x4*)(smem + wave * (NX4 * 1024) + lane * 16);
#pragma unroll
    for (int mi = 0; mi < HM; ++mi)
#pragma unroll
      for (int ni = 0; ni < TN; ++ni) wr[(mi * TN + ni) * 64] = acc[HM + mi][ni];
    if (LN) {
      float st[((2 * HM + TN + 3) / 4) * 4];
#pragma unroll
      for (int i = 0; i < ((2 * HM + TN + 3) / 4) * 4; ++i) st[i] = 0.f;
#pragma unroll
      for (int mi = 0; mi < HM; ++mi) { st[2 * mi] = rs[HM + mi]; st[2 * mi + 1] = rq[HM + mi]; }
#pragma unroll
      for (int ni = 0; ni < TN; ++ni) st[2 * HM + ni] = cs[ni];
#pragma unroll
      for (int i = 0; i < (2 * HM + TN + 3) / 4; ++i) wr[(HM * TN + i) * 64] = f32x4{st[4 * i], st[4 * i + 1], st[4 * i + 2], st[4 * i + 3]};
    }
  }
  bar_lgkm();
  f32x4 fa[HM][TN];
  float mean[HM], rstd[HM];
  {
    const LDS_AS f32x4* rd = (const LDS_AS f32x4*)(smem + (wave ^ 4) * (NX4 * 1024) + lane * 16);
#pragma unroll
    for (int mi = 0; mi < HM; ++mi)
#pragma unroll
      for (int ni = 0; ni < TN; ++ni) fa[mi][ni] = acc[mi][ni] + rd[(mi * TN + ni) * 64];
    if (LN) {
      float st[((2 * HM + TN + 3) / 4) * 4];
#pragma unroll
      for (int i = 0; i < (2 * HM + TN + 3) / 4; ++i) {
        const f32x4 v = rd[(HM * TN + i) * 64];
        st[4 * i] = v[0]; st[4 * i + 1] = v[1]; st[4 * i + 2] = v[2]; st[4 * i + 3] = v[3];
      }
#pragma unroll
      for (int mi = 0; mi < HM; ++mi) {
        float s = rs[mi] + st[2 * mi], q = rq[mi] + st[2 * mi + 1];
        s += __shfl_xor(s, 16); s += __shfl_xor(s, 32);
        q += __shfl_xor(q, 16); q += __shfl_xor(q, 32);
        const float m = s * (1.0f / K);
        const float v = __builtin_fmaxf(q * (1.0f / K) - m * m, 0.f);
        mean[mi] = m;
        rstd[mi] = __builtin_amdgcn_rsqf(v + 1e-5f);
      }
#pragma unroll
      for (int ni = 0; ni < TN; ++ni) {
        float c = cs[ni] + st[2 * HM + ni];
        c += __shfl_xor(c, 16); c += __shfl_xor(c, 32);
        cs[ni] = c;
      }
    }
  }

  if (MODE == 0) {
    const int head = nt * 2 + wn;
    const float qscale = 0.25f * 1.4426950408889634f;
    float csr[2][4];
#pragma unroll
    for (int ni = 0; ni < 2; ++ni)
#pragma unroll
      for (int r = 0; r < 4; ++r) csr[ni][r] = __shfl(cs[ni], 4 * g + r);
#pragma unroll
    for (int mi = 0; mi < HM; ++mi) {
      const int tok = tok0 + mi * 16 + l15;
      {
        const float sc = rstd[mi] * qscale;
        float v[4];
#pragma unroll
        for (int r = 0; r < 4; ++r) v[r] = sc * (fa[mi][0][r] - mean[mi] * csr[0][r]);
        *(u32x2*)(out0 + ((size_t)head * SEQ + tok) * 16 + 4 * g) = pack4(v[0], v[1], v[2], v[3]);
      }
      {
        float v[4];
#pragma unroll
        for (int r = 0; r < 4; ++r) v[r] = rstd[mi] * (fa[mi][1][r] - mean[mi] * csr[1][r]);
        *(u32x2*)(out1 + (size_t)head * 32768 + (tok >> 5) * 512 + (g >> 1) * 256 + (tok & 31) * 8 + (g & 1) * 4) =
            pack4(v[0], v[1], v[2], v[3]);
      }
      {
        float v[4];
#pragma unroll
        for (int r = 0; r < 4; ++r) {
          const float mr = __shfl(mean[mi], 4 * g + r), rr = __shfl(rstd[mi], 4 * g + r);
          v[r] = rr * (fa[mi][2][r] - mr * cs[2]);
        }
        const int ks16 = (tok0 + mi * 16) >> 4;
        *(u32x2*)(out2 + (size_t)head * 32768 + ks16 * 256 + (g & 1) * 128 + l15 * 8 + (g >> 1) * 4) =
            pack4(v[0], v[1], v[2], v[3]);
      }
    }
  } else if (MODE == 2) {
#pragma unroll
    for (int ni = 0; ni < TN; ++ni) {
      float csr[4];
#pragma unroll
      for (int r = 0; r < 4; ++r) csr[r] = __shfl(cs[ni], 4 * g + r);
      const float bb[4] = {bf_lo(biasv[ni][0]), bf_hi(biasv[ni][0]), bf_lo(biasv[ni][1]), bf_hi(biasv[ni][1])};
#pragma unroll
      for (int mi = 0; mi < HM; ++mi) {
        const int tok = tok0 + mi * 16 + l15;
        float v[4];
#pragma unroll
        for (int r = 0; r < 4; ++r) {
          float t = rstd[mi] * (fa[mi][ni][r] - mean[mi] * csr[r]);
          t = rbf(rbf(t) + bb[r]);
          v[r] = gelu_tanh(t);
        }
        *(u32x2*)(out0 + (size_t)tok * FF + colbase(nb_of(ni)) + 4 * g) = pack4(v[0], v[1], v[2], v[3]);
      }
    }
  } else {
#pragma unroll
    for (int mi = 0; mi < HM; ++mi)
#pragma unroll
      for (int ni = 0; ni < TN; ++ni) {
        const int tok = tok0 + mi * 16 + l15;
        const float rr[4] = {bf_lo(resv[mi][ni][0]), bf_hi(resv[mi][ni][0]), bf_lo(resv[mi][ni][1]), bf_hi(resv[mi][ni][1])};
        float v[4];
#pragma unroll
        for (int r = 0; r < 4; ++r) {
          float t = rbf(fa[mi][ni][r]);
          if (MODE == 3) {
            const float bb = (r == 0) ? bf_lo(biasv[ni][0]) : (r == 1) ? bf_hi(biasv[ni][0]) : (r == 2) ? bf_lo(biasv[ni][1]) : bf_hi(biasv[ni][1]);
            t = rbf(t + bb);
          }
          v[r] = rr[r] + t;
        }
        *(u32x2*)(out0 + (size_t)tok * DM + colbase(nb_of(ni)) + 4 * g) = pack4(v[0], v[1], v[2], v[3]);
      }
  }
  GST(2 + NK);
}

constexpr int A_KOFF = 0, A_VOFF = 65536, A_ONES = 131072, A_QOFF = 139264, A_LDS = 147456;

#define MFMA32(a, b, c) __builtin_amdgcn_mfma_f32_32x32x16_bf16(a, b, c, 0, 0, 0)
__device__ __forceinline__ bf16x8 exp_pack8(const f32x16& s, int o) {
  bf16x8 p;
#pragma unroll
  for (int r = 0; r < 8; r += 2) {
    f32x2 e = {__builtin_amdgcn_exp2f(s[o + r]), __builtin_amdgcn_exp2f(s[o + r + 1])};
    bf16x2 c = __builtin_convertvector(e, bf16x2);
    p[r] = c[0]; p[r + 1] = c[1];
  }
  return p;
}
#define LDF(p) (*(const LDS_AS bf16x8*)(p))
#define ASTEP(SC, SN, VA, VB, VNA, VNB, KO, VO)                              \
  do {                                                                       \
    SN = MFMA32(kf, qf, negm); SBAR();                                       \
    kf = LDF(kp + (KO)); VNA = LDF(vp + (VO)); VNB = LDF(vp + (VO) + 512); SBAR(); \
    { bf16x8 pa = exp_pack8(SC, 0); SBAR(); o = MFMA32(VA, pa, o); SBAR(); } \
    { bf16x8 pb = exp_pack8(SC, 8); SBAR(); o = MFMA32(VB, pb, o); SBAR(); } \
  } while (0)

__device__ __forceinline__ float max16(const f32x16 s) {
  float a = __builtin_fmaxf(__builtin_fmaxf(s[0], s[1]), s[2]);
  float c = __builtin_fmaxf(__builtin_fmaxf(s[3], s[4]), s[5]);
  a = __builtin_fmaxf(__builtin_fmaxf(a, s[6]), s[7]);
  c = __builtin_fmaxf(__builtin_fmaxf(c, s[8]), s[9]);
  a = __builtin_fmaxf(__builtin_fmaxf(a, s[10]), s[11]);
  c = __builtin_fmaxf(__builtin_fmaxf(c, s[12]), s[13]);
  a = __builtin_fmaxf(__builtin_fmaxf(a, s[14]), s[15]);
  return __builtin_fmaxf(a, c);
}
__device__ __forceinline__ float xhalf_max(float m) {
  auto rr = __builtin_amdgcn_permlane32_swap(__float_as_uint(m), __float_as_uint(m), false, false);
  return __builtin_fmaxf(__uint_as_float(rr[0]), __uint_as_float(rr[1]));
}

__global__ __launch_bounds__(512, 2) void attn_kernel(const bf16_t* __restrict__ Q, const bf16_t* __restrict__ Kf,
                                                      const bf16_t* __restrict__ Vf, bf16_t* __restrict__ O) {
  extern __shared__ __attribute__((aligned(16))) char smem_g[];
  LDS_AS char* smem = (LDS_AS char*)smem_g;
  const int tid = threadIdx.x, lane = tid & 63;
  const int wave = __builtin_amdgcn_readfirstlane(tid >> 6);
  const int b = blockIdx.x, xcd = b & 7, j = b >> 3;
  const int head = xcd * 4 + (j & 3), qt = j >> 2;
  const int r32 = lane & 31, hh = lane >> 5;
  const int q0 = qt * 256 + wave * 32;

  const unsigned lds0 = (unsigned)(uintptr_t)smem;
  glds16(Q + ((size_t)head * SEQ + q0 + r32) * 16 + hh * 8, lds0 + A_QOFF + wave * 1024);
  {
    const bf16_t* ks = Kf + (size_t)head * 32768 + wave * 512 + lane * 8;
    const bf16_t* vs = Vf + (size_t)head * 32768 + wave * 512 + lane * 8;
#pragma unroll
    for (int c = 0; c < 8; ++c) {
      glds16(ks + c * 4096, lds0 + A_KOFF + c * 8192 + wave * 1024);
      glds16(vs + c * 4096, lds0 + A_VOFF + c * 8192 + wave * 1024);
    }
  }
  *(LDS_AS u32x4*)(smem + A_ONES + tid * 16) = u32x4{0x3F803F80u, 0x3F803F80u, 0x3F803F80u, 0x3F803F80u};

  LDS_AS char* kb = smem + A_KOFF + lane * 16;
  const bool real = r32 < 16;
  LDS_AS char* vb = real ? (smem + A_VOFF + hh * 256 + r32 * 16) : (smem + A_ONES);
  const int vstride = real ? 2048 : 0;

  wait_bar<14>();
  const bf16x8 qf = *(const LDS_AS bf16x8*)(smem + A_QOFF + wave * 1024 + lane * 16);
  f32x16 negm;
  {
    const bf16x8 kf = *(const LDS_AS bf16x8*)(kb);
    f32x16 z = {};
    f32x16 s = __builtin_amdgcn_mfma_f32_32x32x16_bf16(kf, qf, z, 0, 0, 0);
    const float m = xhalf_max(max16(s));
#pragma unroll
    for (int r = 0; r < 16; ++r) negm[r] = -m;
  }
  f32x16 o = {};
#pragma unroll 1
  for (int pass = 0; pass < 2; ++pass) {
    LDS_AS char* kp = kb;
    LDS_AS char* vp = vb;
    bf16x8 kf = LDF(kp), va0 = LDF(vp), vb0 = LDF(vp + 512), va1, vb1;
    f32x16 sA = MFMA32(kf, qf, negm), sB;
    kf = LDF(kp + 1024);
    SBAR();
#pragma unroll 1
    for (int t = 0; t < 64; t += 2) {
      if (t == 6 && pass == 0) wait_bar<0>();
      ASTEP(sA, sB, va0, vb0, va1, vb1, 2048, 1024);
      ASTEP(sB, sA, va1, vb1, va0, vb0, 3072, 2048);
      kp += 2048; vp += vstride;
    }
    if (__builtin_expect(pass == 1 || !__any(!(o[8] < 1.2676506e30f)), 1)) break;
    float m = -INFINITY;
    f32x16 z = {};
    for (int t = 0; t < 64; ++t) {
      f32x16 sc = MFMA32(LDF(kb + t * 1024), qf, z);
      m = __builtin_fmaxf(m, max16(sc));
    }
    m = xhalf_max(m);
#pragma unroll
    for (int r = 0; r < 16; ++r) negm[r] = -m;
    o = z;
  }

  const float inv = __builtin_amdgcn_rcpf(o[8]);
  bf16_t* op = O + (size_t)(q0 + r32) * DM + head * 16 + 4 * hh;
  *(u32x2*)(op) = pack4(o[0] * inv, o[1] * inv, o[2] * inv, o[3] * inv);
  *(u32x2*)(op + 8) = pack4(o[4] * inv, o[5] * inv, o[6] * inv, o[7] * inv);
}

constexpr int QKV_NST = 5, WO_NST = 8, F1_NST = 5, F2_NST = 8;
#define QKV_K gemm_kernel<128, 96, 2, 2, 512, 1536, QKV_NST, 0>
#define WO_K gemm_kernel<64, 64, 2, 2, 512, 512, WO_NST, 1>
#define F1_K gemm_kernel<128, 112, 4, 1, 512, 1792, F1_NST, 2>
#define F2_K gemm_kernel<64, 64, 2, 2, 1792, 512, F2_NST, 3>
constexpr int QKV_LDS = QKV_NST * (128 + 96) * 128, WO_LDS = WO_NST * 128 * 128, F1_LDS = F1_NST * (128 + 112) * 128,
              F2_LDS = F2_NST * 128 * 128;

extern "C" void kernel_launch(void* const* d_in, const int* in_sizes, int n_in, void* d_out, int out_size, void* d_ws,
                              size_t ws_size, hipStream_t stream) {
  const bf16_t* x = (const bf16_t*)d_in[0];
  const bf16_t* wqkv = (const bf16_t*)d_in[1];
  const bf16_t* wo = (const bf16_t*)d_in[2];
  const bf16_t* w1 = (const bf16_t*)d_in[4];
  const bf16_t* b1 = (const bf16_t*)d_in[5];
  const bf16_t* w2 = (const bf16_t*)d_in[6];
  const bf16_t* b2 = (const bf16_t*)d_in[7];
  char* ws = (char*)d_ws;
  bf16_t* Qb = (bf16_t*)(ws + (0 << 20));
  bf16_t* Kb = (bf16_t*)(ws + (2 << 20));
  bf16_t* Vb = (bf16_t*)(ws + (4 << 20));
  bf16_t* Ob = (bf16_t*)(ws + (6 << 20));
  bf16_t* X1 = (bf16_t*)(ws + (8 << 20));
  bf16_t* Hb = (bf16_t*)(ws + (10 << 20));
  bf16_t* X2 = (bf16_t*)(ws + (18 << 20));
  bf16_t* out = (bf16_t*)d_out;

  hipFuncSetAttribute((const void*)QKV_K, hipFuncAttributeMaxDynamicSharedMemorySize, QKV_LDS);
  hipFuncSetAttribute((const void*)WO_K, hipFuncAttributeMaxDynamicSharedMemorySize, WO_LDS);
  hipFuncSetAttribute((const void*)F1_K, hipFuncAttributeMaxDynamicSharedMemorySize, F1_LDS);
  hipFuncSetAttribute((const void*)F2_K, hipFuncAttributeMaxDynamicSharedMemorySize, F2_LDS);
  hipFuncSetAttribute((const void*)attn_kernel, hipFuncAttributeMaxDynamicSharedMemorySize, A_LDS);

  for (int layer = 0; layer < 2; ++layer) {
    const bf16_t* xin = layer == 0 ? x : X2;
    bf16_t* xout = layer == 0 ? X2 : out;
    QKV_K<<<256, 512, QKV_LDS, stream>>>(xin, wqkv, nullptr, nullptr, Qb, Kb, Vb);
    attn_kernel<<<256, 512, A_LDS, stream>>>(Qb, Kb, Vb, Ob);
    WO_K<<<256, 512, WO_LDS, stream>>>(Ob, wo, nullptr, xin, X1, nullptr, nullptr);
    F1_K<<<256, 512, F1_LDS, stream>>>(X1, w1, b1, nullptr, Hb, nullptr, nullptr);
    F2_K<<<256, 512, F2_LDS, stream>>>(Hb, w2, b2, X1, xout, nullptr, nullptr);
  }
}
```

```cpp
#include <hip/hip_runtime.h>
#include <hip/hip_bf16.h>
#include <stdint.h>

typedef __bf16 bf16_t;
typedef __bf16 bf16x2 __attribute__((ext_vector_type(2)));
typedef __bf16 bf16x4 __attribute__((ext_vector_type(4)));
typedef __bf16 bf16x8 __attribute__((ext_vector_type(8)));
typedef float f32x2 __attribute__((ext_vector_type(2)));
typedef float f32x4 __attribute__((ext_vector_type(4)));
typedef float f32x16 __attribute__((ext_vector_type(16)));
typedef unsigned u32x2 __attribute__((ext_vector_type(2)));
typedef unsigned u32x4 __attribute__((ext_vector_type(4)));

#define LDS_AS __attribute__((address_space(3)))
#define GLB_AS __attribute__((address_space(1)))

constexpr int SEQ = 2048, DM = 512, NH = 32, DH = 16, FF = 1792;

__device__ __forceinline__ void glds16(const void* g, unsigned lds_base) {
  unsigned keep;
  asm volatile("s_mov_b32 %0, m0\n\ts_mov_b32 m0, %2\n\ts_nop 0\n\tglobal_load_lds_dwordx4 %1, off\n\ts_mov_b32 m0, %0"
               : "=&s"(keep) : "v"(g), "s"(lds_base) : "memory");
}
#define SBAR() __builtin_amdgcn_sched_barrier(0)
#define PIN(x) asm volatile("" : "+v"(x))
__device__ __forceinline__ float rbf(float v) { return (float)(__bf16)v; }
__device__ __forceinline__ u32x2 pack4(float a, float b, float c, float d) {
  f32x2 lo = {a, b}, hi = {c, d};
  bf16x2 l = __builtin_convertvector(lo, bf16x2), h = __builtin_convertvector(hi, bf16x2);
  u32x2 r = {__builtin_bit_cast(unsigned, l), __builtin_bit_cast(unsigned, h)};
  return r;
}
__device__ __forceinline__ float bf_lo(unsigned u) { return __builtin_bit_cast(float, u << 16); }
__device__ __forceinline__ float bf_hi(unsigned u) { return __builtin_bit_cast(float, u & 0xffff0000u); }

#define GST(i)
template <int N>
__device__ __forceinline__ void wait_bar() {
  asm volatile("s_waitcnt vmcnt(%0) lgkmcnt(0)\n\ts_barrier" ::"i"(N) : "memory");
}

template <int I> struct IC { static constexpr int value = I; };
template <int I, int N, class F>
__device__ __forceinline__ void static_for(F& f) {
  if constexpr (I < N) { f(IC<I>{}); static_for<I + 1, N>(f); }
}

__device__ __forceinline__ float gelu_tanh(float x) {
  const float c1 = -2.0f * 0.7978845608028654f * 1.4426950408889634f;
  const float c2 = c1 * 0.044715f;
  float t = x * __builtin_fmaf(x * x, c2, c1);
  float e = __builtin_amdgcn_exp2f(t);
  return x * __builtin_amdgcn_rcpf(1.0f + e);
}

template <int N>
__device__ __forceinline__ void wait_vm() { asm volatile("s_waitcnt vmcnt(%0)" ::"i"(N) : "memory"); }
__device__ __forceinline__ void bar_lgkm() { asm volatile("s_waitcnt lgkmcnt(0)\n\ts_barrier" ::: "memory"); }
#define MFMA16(a, b, c) __builtin_amdgcn_mfma_f32_16x16x32_bf16(a, b, c, 0, 0, 0)
__device__ __forceinline__ float sum_g(float v) {
  auto a = __builtin_amdgcn_permlane16_swap(__float_as_uint(v), __float_as_uint(v), false, false);
  v = __uint_as_float(a[0]) + __uint_as_float(a[1]);
  auto b = __builtin_amdgcn_permlane32_swap(__float_as_uint(v), __float_as_uint(v), false, false);
  return __uint_as_float(b[0]) + __uint_as_float(b[1]);
}

template <int BM, int BN, int WVM, int WVN, int K, int LDW, int NST, int MODE, int SELF>
__global__ __launch_bounds__(512, 2) void gemm_kernel(const bf16_t* __restrict__ X, const bf16_t* __restrict__ W,
                                                      const bf16_t* __restrict__ bias, const bf16_t* __restrict__ res,
                                                      bf16_t* __restrict__ out0, bf16_t* __restrict__ out1,
                                                      bf16_t* __restrict__ out2, float* __restrict__ stats) {
  extern __shared__ __attribute__((aligned(16))) char smem_g[];
  LDS_AS char* smem = (LDS_AS char*)smem_g;
  constexpr int TM = BM / WVM / 16, TN = BN / WVN / 16, NB = BN / 16, HM = TM / 2;
  constexpr int XB = BM * 128, WB = BN * 128, STB = XB + WB;
  constexpr int NXI = BM / 8, NI = (BM + BN) / 8, LPW = (NI + 7) / 8, NK = K / 64;
  constexpr int NFULL = NI - 8 * (LPW - 1);
  constexpr bool LN = (MODE == 0 || MODE == 2);

  const int tid = threadIdx.x, lane = tid & 63;
  const int wave = __builtin_amdgcn_readfirstlane(tid >> 6);
  const int kg = wave >> 2, w4 = wave & 3;
  const int wm = w4 / WVN, wn = w4 % WVN;
  const int b = blockIdx.x, xcd = b & 7, j = b >> 3;
  int mt, nt;
  if (BM == 128) { mt = (xcd >> 1) * 4 + (j & 3); nt = (xcd & 1) * 8 + (j >> 2); }
  else           { mt = (xcd >> 1) * 8 + (j & 7); nt = (xcd & 1) * 4 + (j >> 3); }
  GST(0);
  const int m0 = mt * BM;
  const int l15 = lane & 15, g = lane >> 4;

  auto colbase = [&](int nb) -> int {
    if (MODE == 0) return (nb >> 1) * 512 + (nt * 2 + (nb & 1)) * 16;
    return nt * BN + nb * 16;
  };
  auto nb_of = [&](int ni) -> int {
    if (MODE == 0) return 2 * ni + wn;
    return wn * TN + ni;
  };
  const int tl0 = wm * TM * 16 + kg * HM * 16;
  const int tok0 = m0 + tl0;

  u32x2 resv[HM][TN];
  u32x2 biasv[TN];
  f32x4 pst[8];
  if (MODE == 1 || MODE == 3) {
#pragma unroll
    for (int mi = 0; mi < HM; ++mi)
#pragma unroll
      for (int ni = 0; ni < TN; ++ni)
        resv[mi][ni] = *(const u32x2*)(res + (size_t)(tok0 + mi * 16 + l15) * DM + colbase(nb_of(ni)) + 4 * g);
  }
  if (MODE == 2 || MODE == 3) {
#pragma unroll
    for (int ni = 0; ni < TN; ++ni) biasv[ni] = *(const u32x2*)(bias + colbase(nb_of(ni)) + 4 * g);
  }
  if (LN && !SELF) {
    if (wave < 2) {
#pragma unroll
      for (int i = 0; i < 8; ++i) pst[i] = *(const f32x4*)(stats + (size_t)(m0 + tid) * 32 + i * 4);
    }
  }

  const bf16_t* src[LPW];
  int inc[LPW];
#pragma unroll
  for (int i = 0; i < LPW; ++i) {
    int ii = wave + 8 * i;
    if (ii >= NI) ii -= 8;
    if (ii < NXI) {
      const int row = 8 * ii + (lane >> 3);
      const int ch = (lane & 7) ^ ((row >> 1) & 7);
      src[i] = X + (size_t)(m0 + row) * K + ch * 8;
      inc[i] = 64;
    } else {
      const int wi = ii - NXI;
      const int sidx = 4 * wi + (lane >> 4);
      const int kb = sidx / NB, nb = sidx % NB;
      const int r = (lane >> 1) & 7, half = lane & 1;
      const int krow = kb * 8 + (r ^ ((kb & 1) << 2));
      src[i] = W + (size_t)krow * LDW + colbase(nb) + half * 8;
      inc[i] = 64 * LDW;
    }
  }
  const unsigned lds0 = (unsigned)(uintptr_t)smem + wave * 1024;
  const bool full = wave < NFULL;
  auto issue = [&](int st) {
    const unsigned base = lds0 + (st % NST) * STB;
#pragma unroll
    for (int i = 0; i < LPW; ++i) {
      if (i < LPW - 1 || NFULL == 8 || full) glds16(src[i], base + i * 8192);
      src[i] += inc[i];
    }
  };
#pragma unroll
  for (int s = 0; s < NST - 1; ++s) issue(s);

  const int xo_ = ((wm * TM * 16 + l15) * 128 + ((g ^ (l15 >> 1)) * 16)) ^ (kg * 64);
  const int xoA = xo_ + kg * HM * 2048, xoB = xo_ + (1 - kg) * HM * 2048;
  const int q_ = (lane & 15) >> 2, p_ = lane & 3;
  const int wo0 = XB + (kg * 4 + g) * NB * 256 + (q_ + 4 * (g & 1)) * 32 + p_ * 8;
  const int wo1 = XB + (kg * 4 + g) * NB * 256 + (q_ + 4 * (1 - (g & 1))) * 32 + p_ * 8;
  const int cnb0 = w4, cnb1 = (w4 + 4 < NB) ? w4 + 4 : NB - 1;

  f32x4 acc[TM][TN];
#pragma unroll
  for (int mi = 0; mi < TM; ++mi)
#pragma unroll
    for (int ni = 0; ni < TN; ++ni) acc[mi][ni] = f32x4{0.f, 0.f, 0.f, 0.f};
  f32x4 cs0 = {0.f, 0.f, 0.f, 0.f}, cs1 = {0.f, 0.f, 0.f, 0.f};
  f32x4 racc[TM];
#pragma unroll
  for (int mi = 0; mi < TM; ++mi) racc[mi] = f32x4{0.f, 0.f, 0.f, 0.f};
  const __bf16 one = (__bf16)1.0f;
  const bf16x8 ones8 = {one, one, one, one, one, one, one, one};

  auto ldw = [&](LDS_AS char* base, int o) -> bf16x8 {
    bf16x4 a = __builtin_amdgcn_ds_read_tr16_b64_v4bf16((LDS_AS bf16x4*)(base + wo0 + o));
    bf16x4 c = __builtin_amdgcn_ds_read_tr16_b64_v4bf16((LDS_AS bf16x4*)(base + wo1 + o));
    return __builtin_shufflevector(a, c, 0, 1, 2, 3, 4, 5, 6, 7);
  };

  auto kstep = [&](auto KT) {
    constexpr int kt = decltype(KT)::value;
    constexpr int rem = (NK - 1 - kt) < (NST - 2) ? (NK - 1 - kt) : (NST - 2);
    if (NFULL == 8 || full) wait_vm<rem * LPW>(); else wait_vm<rem * (LPW - 1)>();
    bar_lgkm();
    GST(1 + kt);
    if (kt + NST - 1 < NK) issue(kt + NST - 1);
    LDS_AS char* base = smem + (kt % NST) * STB;
    bf16x8 xf[TM], wf[TN];
#pragma unroll
    for (int mi = 0; mi < TM; ++mi) xf[mi] = *(const LDS_AS bf16x8*)(base + (mi < HM ? xoA + mi * 2048 : xoB + (mi - HM) * 2048));
#pragma unroll
    for (int ni = 0; ni < TN; ++ni) wf[ni] = ldw(base, nb_of(ni) * 256);
#pragma unroll
    for (int mi = 0; mi < TM; ++mi)
#pragma unroll
      for (int ni = 0; ni < TN; ++ni) {
        if (MODE == 0 && ni == 2) acc[mi][ni] = MFMA16(xf[mi], wf[ni], acc[mi][ni]);
        else                      acc[mi][ni] = MFMA16(wf[ni], xf[mi], acc[mi][ni]);
      }
    if (LN) {
      const bf16x8 c0 = ldw(base, cnb0 * 256), c1 = ldw(base, cnb1 * 256);
      cs0 = MFMA16(c0, ones8, cs0);
      cs1 = MFMA16(c1, ones8, cs1);
      if (SELF) {
        if (wn) {
#pragma unroll
          for (int mi = 0; mi < TM; ++mi) racc[mi] = MFMA16(xf[mi], xf[mi], racc[mi]);
        } else {
#pragma unroll
          for (int mi = 0; mi < TM; ++mi) racc[mi] = MFMA16(ones8, xf[mi], racc[mi]);
        }
      }
    }
  };
  static_for<0, NK>(kstep);

  GST(1 + NK);
  if (MODE == 1 || MODE == 3) {
#pragma unroll
    for (int mi = 0; mi < HM; ++mi)
#pragma unroll
      for (int ni = 0; ni < TN; ++ni) PIN(resv[mi][ni]);
  }
  if (MODE == 2 || MODE == 3) {
#pragma unroll
    for (int ni = 0; ni < TN; ++ni) PIN(biasv[ni]);
  }
  constexpr int NX4 = HM * TN;
  constexpr int CS_OFF = 8 * NX4 * 1024;
  constexpr int ST_OFF = CS_OFF + 1024;
  bar_lgkm();
  {
    LDS_AS f32x4* wr = (LDS_AS f32x4*)(smem + wave * (NX4 * 1024) + lane * 16);
#pragma unroll
    for (int mi = 0; mi < HM; ++mi)
#pragma unroll
      for (int ni = 0; ni < TN; ++ni) wr[(mi * TN + ni) * 64] = acc[HM + mi][ni];
    if (LN) {
      if (l15 == 0) {
        *(LDS_AS f32x4*)(smem + CS_OFF + (kg * 8 + cnb0) * 64 + g * 16) = cs0;
        *(LDS_AS f32x4*)(smem + CS_OFF + (kg * 8 + cnb1) * 64 + g * 16) = cs1;
      }
      if (SELF) {
        if (g == (wn ? (l15 >> 2) : 0)) {
#pragma unroll
          for (int mi = 0; mi < TM; ++mi) {
            const int r = wn ? (l15 & 3) : 0;
            const float v = r == 0 ? racc[mi][0] : r == 1 ? racc[mi][1] : r == 2 ? racc[mi][2] : racc[mi][3];
            const int tl = wm * TM * 16 + (mi < HM ? kg * HM + mi : (1 - kg) * HM + (mi - HM)) * 16 + l15;
            *(LDS_AS float*)(smem + ST_OFF + ((kg * 2 + wn) * BM + tl) * 4) = v;
          }
        }
      } else if (wave < 2) {
        float sm = 0.f, sq = 0.f;
#pragma unroll
        for (int i = 0; i < 8; ++i) { PIN(pst[i]); sm += pst[i][0] + pst[i][2]; sq += pst[i][1] + pst[i][3]; }
        const float m = sm * (1.0f / K);
        const float v = __builtin_fmaxf(sq * (1.0f / K) - m * m, 0.f);
        *(LDS_AS f32x2*)(smem + ST_OFF + tid * 8) = f32x2{m, __builtin_amdgcn_rsqf(v + 1e-5f)};
      }
    }
  }
  bar_lgkm();
  f32x4 fa[HM][TN];
  {
    const LDS_AS f32x4* rd = (const LDS_AS f32x4*)(smem + (wave ^ 4) * (NX4 * 1024) + lane * 16);
#pragma unroll
    for (int mi = 0; mi < HM; ++mi)
#pragma unroll
      for (int ni = 0; ni < TN; ++ni) fa[mi][ni] = acc[mi][ni] + rd[(mi * TN + ni) * 64];
  }
  auto tok_stats = [&](int tl) -> f32x2 {
    if (SELF) {
      const LDS_AS float* p = (const LDS_AS float*)(smem + ST_OFF + tl * 4);
      const float sm = p[0] + p[2 * BM], sq = p[BM] + p[3 * BM];
      const float m = sm * (1.0f / K);
      const float v = __builtin_fmaxf(sq * (1.0f / K) - m * m, 0.f);
      return f32x2{m, __builtin_amdgcn_rsqf(v + 1e-5f)};
    }
    return *(const LDS_AS f32x2*)(smem + ST_OFF + tl * 8);
  };
  auto col_sum4 = [&](int nb) -> f32x4 {
    return *(const LDS_AS f32x4*)(smem + CS_OFF + nb * 64 + g * 16) + *(const LDS_AS f32x4*)(smem + CS_OFF + (8 + nb) * 64 + g * 16);
  };

  if (MODE == 0) {
    const int head = nt * 2 + wn;
    const float qscale = 0.25f * 1.4426950408889634f;
    const f32x4 cq = col_sum4(nb_of(0)), ck = col_sum4(nb_of(1));
    const float cv = *(const LDS_AS float*)(smem + CS_OFF + nb_of(2) * 64 + l15 * 4) +
                     *(const LDS_AS float*)(smem + CS_OFF + (8 + nb_of(2)) * 64 + l15 * 4);
#pragma unroll
    for (int mi = 0; mi < HM; ++mi) {
      const int tok = tok0 + mi * 16 + l15;
      const f32x2 st = tok_stats(tl0 + mi * 16 + l15);
      {
        const float sc = st[1] * qscale;
        float v[4];
#pragma unroll
        for (int r = 0; r < 4; ++r) v[r] = sc * __builtin_fmaf(-st[0], cq[r], fa[mi][0][r]);
        *(u32x2*)(out0 + ((size_t)head * SEQ + tok) * 16 + 4 * g) = pack4(v[0], v[1], v[2], v[3]);
      }
      {
        float v[4];
#pragma unroll
        for (int r = 0; r < 4; ++r) v[r] = st[1] * __builtin_fmaf(-st[0], ck[r], fa[mi][1][r]);
        *(u32x2*)(out1 + (size_t)head * 32768 + (tok >> 5) * 512 + (g >> 1) * 256 + (tok & 31) * 8 + (g & 1) * 4) =
            pack4(v[0], v[1], v[2], v[3]);
      }
      {
        float v[4];
#pragma unroll
        for (int r = 0; r < 4; ++r) {
          const f32x2 sr = tok_stats(tl0 + mi * 16 + 4 * g + r);
          v[r] = sr[1] * __builtin_fmaf(-sr[0], cv, fa[mi][2][r]);
        }
        const int ks16 = (tok0 + mi * 16) >> 4;
        *(u32x2*)(out2 + (size_t)head * 32768 + ks16 * 256 + (g & 1) * 128 + l15 * 8 + (g >> 1) * 4) =
            pack4(v[0], v[1], v[2], v[3]);
      }
    }
  } else if (MODE == 2) {
#pragma unroll
    for (int mi = 0; mi < HM; ++mi) {
      const int tok = tok0 + mi * 16 + l15;
      const f32x2 st = tok_stats(tl0 + mi * 16 + l15);
      const float nrm = -st[0] * st[1];
#pragma unroll
      for (int ni = 0; ni < TN; ++ni) {
        const f32x4 cc = col_sum4(nb_of(ni));
        const float bb[4] = {bf_lo(biasv[ni][0]), bf_hi(biasv[ni][0]), bf_lo(biasv[ni][1]), bf_hi(biasv[ni][1])};
        float v[4];
#pragma unroll
        for (int r = 0; r < 4; ++r)
          v[r] = gelu_tanh(__builtin_fmaf(st[1], fa[mi][ni][r], __builtin_fmaf(nrm, cc[r], bb[r])));
        *(u32x2*)(out0 + (size_t)tok * FF + colbase(nb_of(ni)) + 4 * g) = pack4(v[0], v[1], v[2], v[3]);
      }
    }
  } else {
#pragma unroll
    for (int mi = 0; mi < HM; ++mi) {
      const int tok = tok0 + mi * 16 + l15;
      float sm = 0.f, sq = 0.f;
#pragma unroll
      for (int ni = 0; ni < TN; ++ni) {
        float rr[4] = {bf_lo(resv[mi][ni][0]), bf_hi(resv[mi][ni][0]), bf_lo(resv[mi][ni][1]), bf_hi(resv[mi][ni][1])};
        if (MODE == 3) {
          rr[0] += bf_lo(biasv[ni][0]); rr[1] += bf_hi(biasv[ni][0]); rr[2] += bf_lo(biasv[ni][1]); rr[3] += bf_hi(biasv[ni][1]);
        }
        const u32x2 pk = pack4(rr[0] + fa[mi][ni][0], rr[1] + fa[mi][ni][1], rr[2] + fa[mi][ni][2], rr[3] + fa[mi][ni][3]);
        *(u32x2*)(out0 + (size_t)tok * DM + colbase(nb_of(ni)) + 4 * g) = pk;
        const float w[4] = {bf_lo(pk[0]), bf_hi(pk[0]), bf_lo(pk[1]), bf_hi(pk[1])};
#pragma unroll
        for (int r = 0; r < 4; ++r) { sm += w[r]; sq = __builtin_fmaf(w[r], w[r], sq); }
      }
      sm = sum_g(sm); sq = sum_g(sq);
      if (g == 0) *(f32x2*)(stats + (size_t)tok * 32 + (nt * 2 + wn) * 2) = f32x2{sm, sq};
    }
  }
  GST(2 + NK);
}

constexpr int A_KOFF = 0, A_VOFF = 65536, A_ONES = 131072, A_QOFF = 135168, A_COMB = 143360, A_LDS = 143360 + 8 * 64 * 40;

#define MFMA32(a, b, c) __builtin_amdgcn_mfma_f32_32x32x16_bf16(a, b, c, 0, 0, 0)
__device__ __forceinline__ bf16x8 exp_pack8(const f32x16& s, int o) {
  bf16x8 p;
#pragma unroll
  for (int r = 0; r < 8; r += 2) {
    f32x2 e = {__builtin_amdgcn_exp2f(s[o + r]), __builtin_amdgcn_exp2f(s[o + r + 1])};
    bf16x2 c = __builtin_convertvector(e, bf16x2);
    p[r] = c[0]; p[r + 1] = c[1];
  }
  return p;
}
#define LDF(p) (*(const LDS_AS bf16x8*)(p))
#define ASTEP(SC, SN, VA, VB, VNA, VNB, KO, VO)                              \
  do {                                                                       \
    SN = MFMA32(kf, qf, negm); SBAR();                                       \
    kf = LDF(kp + (KO)); VNA = LDF(vp + (VO)); VNB = LDF(vp + (VO) + 512); SBAR(); \
    { bf16x8 pa = exp_pack8(SC, 0); SBAR(); o = MFMA32(VA, pa, o); SBAR(); } \
    { bf16x8 pb = exp_pack8(SC, 8); SBAR(); o = MFMA32(VB, pb, o); SBAR(); } \
  } while (0)

__device__ __forceinline__ float max16(const f32x16 s) {
  float a = __builtin_fmaxf(__builtin_fmaxf(s[0], s[1]), s[2]);
  float c = __builtin_fmaxf(__builtin_fmaxf(s[3], s[4]), s[5]);
  a = __builtin_fmaxf(__builtin_fmaxf(a, s[6]), s[7]);
  c = __builtin_fmaxf(__builtin_fmaxf(c, s[8]), s[9]);
  a = __builtin_fmaxf(__builtin_fmaxf(a, s[10]), s[11]);
  c = __builtin_fmaxf(__builtin_fmaxf(c, s[12]), s[13]);
  a = __builtin_fmaxf(__builtin_fmaxf(a, s[14]), s[15]);
  return __builtin_fmaxf(a, c);
}
__device__ __forceinline__ float xhalf_max(float m) {
  auto rr = __builtin_amdgcn_permlane32_swap(__float_as_uint(m), __float_as_uint(m), false, false);
  return __builtin_fmaxf(__uint_as_float(rr[0]), __uint_as_float(rr[1]));
}

__global__ __launch_bounds__(1024, 4) void attn_kernel(const bf16_t* __restrict__ Q, const bf16_t* __restrict__ Kf,
                                                       const bf16_t* __restrict__ Vf, bf16_t* __restrict__ O) {
  extern __shared__ __attribute__((aligned(16))) char smem_g[];
  LDS_AS char* smem = (LDS_AS char*)smem_g;
  const int tid = threadIdx.x, lane = tid & 63;
  const int wave = __builtin_amdgcn_readfirstlane(tid >> 6);
  const int kh = wave >> 3, qw = wave & 7;
  const int b = blockIdx.x, xcd = b & 7, j = b >> 3;
  const int head = xcd * 4 + (j & 3), qt = j >> 2;
  const int r32 = lane & 31, hh = lane >> 5;
  const int q0 = qt * 256 + qw * 32;

  const unsigned lds0 = (unsigned)(uintptr_t)smem;
  {
    const bf16_t* ks = Kf + (size_t)head * 32768 + kh * 16384 + qw * 512 + lane * 8;
    const bf16_t* vs = Vf + (size_t)head * 32768 + kh * 16384 + qw * 512 + lane * 8;
    const unsigned kd = lds0 + A_KOFF + kh * 32768 + qw * 1024, vd = lds0 + A_VOFF + kh * 32768 + qw * 1024;
    glds16(Q + ((size_t)head * SEQ + q0 + r32) * 16 + hh * 8, lds0 + A_QOFF + qw * 1024);
#pragma unroll
    for (int c = 0; c < 4; ++c) {
      glds16(ks + c * 4096, kd + c * 8192);
      glds16(vs + c * 4096, vd + c * 8192);
    }
  }
  if (tid < 256) *(LDS_AS u32x4*)(smem + A_ONES + tid * 16) = u32x4{0x3F803F80u, 0x3F803F80u, 0x3F803F80u, 0x3F803F80u};

  LDS_AS char* kb = smem + A_KOFF + kh * 32768 + lane * 16;
  const bool real = r32 < 16;
  LDS_AS char* vb = real ? (smem + A_VOFF + kh * 32768 + hh * 256 + r32 * 16) : (smem + A_ONES);
  const int vstride = real ? 2048 : 0;

  wait_bar<6>();
  const bf16x8 qf = LDF(smem + A_QOFF + qw * 1024 + lane * 16);
  f32x16 negm;
  float mref;
  {
    f32x16 z = {};
    f32x16 s = MFMA32(LDF(smem + A_KOFF + lane * 16), qf, z);
    mref = xhalf_max(max16(s));
#pragma unroll
    for (int r = 0; r < 16; ++r) negm[r] = -mref;
  }
  f32x16 o = {};
#pragma unroll 1
  for (int pass = 0; pass < 2; ++pass) {
    LDS_AS char* kp = kb;
    LDS_AS char* vp = vb;
    bf16x8 kf = LDF(kp), va0 = LDF(vp), vb0 = LDF(vp + 512), va1, vb1;
    f32x16 sA = MFMA32(kf, qf, negm), sB;
    kf = LDF(kp + 1024);
    SBAR();
#pragma unroll 1
    for (int t = 0; t < 32; t += 2) {
      if (t == 6 && pass == 0) wait_bar<0>();
      ASTEP(sA, sB, va0, vb0, va1, vb1, 2048, 1024);
      ASTEP(sB, sA, va1, vb1, va0, vb0, 3072, 2048);
      kp += 2048; vp += vstride;
    }
    if (__builtin_expect(pass == 1 || !__any(!(o[8] < 1.2676506e30f)), 1)) break;
    float m = -INFINITY;
    f32x16 z = {};
    for (int t = 0; t < 32; ++t) {
      f32x16 sc = MFMA32(LDF(kb + t * 1024), qf, z);
      m = __builtin_fmaxf(m, max16(sc));
    }
    mref = xhalf_max(m);
#pragma unroll
    for (int r = 0; r < 16; ++r) negm[r] = -mref;
    o = z;
  }

  LDS_AS float* cb = (LDS_AS float*)(smem + A_COMB + qw * (64 * 40) + lane * 4);
  if (kh) {
#pragma unroll
    for (int r = 0; r < 9; ++r) cb[r * 64] = o[r];
    cb[9 * 64] = mref;
  }
  bar_lgkm();
  if (!kh) {
    const float m2 = cb[9 * 64];
    const float mm = __builtin_fmaxf(mref, m2);
    const float f1 = __builtin_amdgcn_exp2f(mref - mm), f2 = __builtin_amdgcn_exp2f(m2 - mm);
    const float l = o[8] * f1 + cb[8 * 64] * f2;
    const float inv = __builtin_amdgcn_rcpf(l);
    const float a1 = f1 * inv, a2 = f2 * inv;
    float v[8];
#pragma unroll
    for (int r = 0; r < 8; ++r) v[r] = o[r] * a1 + cb[r * 64] * a2;
    bf16_t* op = O + (size_t)(q0 + r32) * DM + head * 16 + 4 * hh;
    *(u32x2*)(op) = pack4(v[0], v[1], v[2], v[3]);
    *(u32x2*)(op + 8) = pack4(v[4], v[5], v[6], v[7]);
  }
}

constexpr int QKV_NST = 5, WO_NST = 8, F1_NST = 5, F2_NST = 8;
#define QKV0_K gemm_kernel<128, 96, 2, 2, 512, 1536, QKV_NST, 0, 1>
#define QKV1_K gemm_kernel<128, 96, 2, 2, 512, 1536, QKV_NST, 0, 0>
#define WO_K gemm_kernel<64, 64, 2, 2, 512, 512, WO_NST, 1, 0>
#define F1_K gemm_kernel<128, 112, 4, 1, 512, 1792, F1_NST, 2, 0>
#define F2_K gemm_kernel<64, 64, 2, 2, 1792, 512, F2_NST, 3, 0>
constexpr int QKV_LDS = QKV_NST * (128 + 96) * 128, WO_LDS = WO_NST * 128 * 128, F1_LDS = F1_NST * (128 + 112) * 128,
              F2_LDS = F2_NST * 128 * 128;

extern "C" void kernel_launch(void* const* d_in, const int* in_sizes, int n_in, void* d_out, int out_size, void* d_ws,
                              size_t ws_size, hipStream_t stream) {
  const bf16_t* x = (const bf16_t*)d_in[0];
  const bf16_t* wqkv = (const bf16_t*)d_in[1];
  const bf16_t* wo = (const bf16_t*)d_in[2];
  const bf16_t* w1 = (const bf16_t*)d_in[4];
  const bf16_t* b1 = (const bf16_t*)d_in[5];
  const bf16_t* w2 = (const bf16_t*)d_in[6];
  const bf16_t* b2 = (const bf16_t*)d_in[7];
  char* ws = (char*)d_ws;
  bf16_t* Qb = (bf16_t*)(ws + (0 << 20));
  bf16_t* Kb = (bf16_t*)(ws + (2 << 20));
  bf16_t* Vb = (bf16_t*)(ws + (4 << 20));
  bf16_t* Ob = (bf16_t*)(ws + (6 << 20));
  bf16_t* X1 = (bf16_t*)(ws + (8 << 20));
  bf16_t* Hb = (bf16_t*)(ws + (10 << 20));
  bf16_t* X2 = (bf16_t*)(ws + (18 << 20));
  float* St1 = (float*)(ws + (20 << 20));
  float* St2 = (float*)(ws + (21 << 20));
  bf16_t* out = (bf16_t*)d_out;

  hipFuncSetAttribute((const void*)QKV0_K, hipFuncAttributeMaxDynamicSharedMemorySize, QKV_LDS);
  hipFuncSetAttribute((const void*)QKV1_K, hipFuncAttributeMaxDynamicSharedMemorySize, QKV_LDS);
  hipFuncSetAttribute((const void*)WO_K, hipFuncAttributeMaxDynamicSharedMemorySize, WO_LDS);
  hipFuncSetAttribute((const void*)F1_K, hipFuncAttributeMaxDynamicSharedMemorySize, F1_LDS);
  hipFuncSetAttribute((const void*)F2_K, hipFuncAttributeMaxDynamicSharedMemorySize, F2_LDS);
  hipFuncSetAttribute((const void*)attn_kernel, hipFuncAttributeMaxDynamicSharedMemorySize, A_LDS);

  for (int layer = 0; layer < 2; ++layer) {
    const bf16_t* xin = layer == 0 ? x : X2;
    bf16_t* xout = layer == 0 ? X2 : out;
    if (layer == 0) QKV0_K<<<256, 512, QKV_LDS, stream>>>(xin, wqkv, nullptr, nullptr, Qb, Kb, Vb, nullptr);
    else            QKV1_K<<<256, 512, QKV_LDS, stream>>>(xin, wqkv, nullptr, nullptr, Qb, Kb, Vb, St2);
    attn_kernel<<<256, 1024, A_LDS, stream>>>(Qb, Kb, Vb, Ob);
    WO_K<<<256, 512, WO_LDS, stream>>>(Ob, wo, nullptr, xin, X1, nullptr, nullptr, St1);
    F1_K<<<256, 512, F1_LDS, stream>>>(X1, w1, b1, nullptr, Hb, nullptr, nullptr, St1);
    F2_K<<<256, 512, F2_LDS, stream>>>(Hb, w2, b2, X1, xout, nullptr, nullptr, St2);
  }
}
```

```cpp
#include <hip/hip_runtime.h>
#include <hip/hip_bf16.h>
#include <stdint.h>

typedef __bf16 bf16_t;
typedef __bf16 bf16x2 __attribute__((ext_vector_type(2)));
typedef __bf16 bf16x4 __attribute__((ext_vector_type(4)));
typedef __bf16 bf16x8 __attribute__((ext_vector_type(8)));
typedef float f32x2 __attribute__((ext_vector_type(2)));
typedef float f32x4 __attribute__((ext_vector_type(4)));
typedef float f32x16 __attribute__((ext_vector_type(16)));
typedef unsigned u32x2 __attribute__((ext_vector_type(2)));
typedef unsigned u32x4 __attribute__((ext_vector_type(4)));

#define LDS_AS __attribute__((address_space(3)))
#define GLB_AS __attribute__((address_space(1)))

constexpr int SEQ = 2048, DM = 512, NH = 32, DH = 16, FF = 1792;

__device__ __forceinline__ void glds16(const void* g, unsigned lds_base) {
  unsigned keep;
  asm volatile("s_mov_b32 %0, m0\n\ts_mov_b32 m0, %2\n\ts_nop 0\n\tglobal_load_lds_dwordx4 %1, off\n\ts_mov_b32 m0, %0"
               : "=&s"(keep) : "v"(g), "s"(lds_base) : "memory");
}
__device__ __forceinline__ unsigned touch(const void* p) {
  unsigned v;
  asm volatile("global_load_dword %0, %1, off" : "=v"(v) : "v"(p) : "memory");
  return v;
}
#define SBAR() __builtin_amdgcn_sched_barrier(0)
#define PIN(x) asm volatile("" : "+v"(x))
__device__ __forceinline__ float rbf(float v) { return (float)(__bf16)v; }
__device__ __forceinline__ u32x2 pack4(float a, float b, float c, float d) {
  f32x2 lo = {a, b}, hi = {c, d};
  bf16x2 l = __builtin_convertvector(lo, bf16x2), h = __builtin_convertvector(hi, bf16x2);
  u32x2 r = {__builtin_bit_cast(unsigned, l), __builtin_bit_cast(unsigned, h)};
  return r;
}
__device__ __forceinline__ float bf_lo(unsigned u) { return __builtin_bit_cast(float, u << 16); }
__device__ __forceinline__ float bf_hi(unsigned u) { return __builtin_bit_cast(float, u & 0xffff0000u); }

#define GST(i)
template <int N>
__device__ __forceinline__ void wait_bar() {
  asm volatile("s_waitcnt vmcnt(%0) lgkmcnt(0)\n\ts_barrier" ::"i"(N) : "memory");
}

template <int I> struct IC { static constexpr int value = I; };
template <int I, int N, class F>
__device__ __forceinline__ void static_for(F& f) {
  if constexpr (I < N) { f(IC<I>{}); static_for<I + 1, N>(f); }
}

__device__ __forceinline__ float gelu_tanh(float x) {
  const float c1 = -2.0f * 0.7978845608028654f * 1.4426950408889634f;
  const float c2 = c1 * 0.044715f;
  float t = x * __builtin_fmaf(x * x, c2, c1);
  float e = __builtin_amdgcn_exp2f(t);
  return x * __builtin_amdgcn_rcpf(1.0f + e);
}

template <int N>
__device__ __forceinline__ void wait_vm() { asm volatile("s_waitcnt vmcnt(%0)" ::"i"(N) : "memory"); }
__device__ __forceinline__ void bar_lgkm() { asm volatile("s_waitcnt lgkmcnt(0)\n\ts_barrier" ::: "memory"); }
#define MFMA16(a, b, c) __builtin_amdgcn_mfma_f32_16x16x32_bf16(a, b, c, 0, 0, 0)
__device__ __forceinline__ float sum_g(float v) {
  auto a = __builtin_amdgcn_permlane16_swap(__float_as_uint(v), __float_as_uint(v), false, false);
  v = __uint_as_float(a[0]) + __uint_as_float(a[1]);
  auto b = __builtin_amdgcn_permlane32_swap(__float_as_uint(v), __float_as_uint(v), false, false);
  return __uint_as_float(b[0]) + __uint_as_float(b[1]);
}

template <int BM, int BN, int WVM, int WVN, int K, int LDW, int NST, int MODE, int SELF>
__global__ __launch_bounds__(512, 2) void gemm_kernel(const bf16_t* __restrict__ X, const bf16_t* __restrict__ W,
                                                      const bf16_t* __restrict__ bias, const bf16_t* __restrict__ res,
                                                      bf16_t* __restrict__ out0, bf16_t* __restrict__ out1,
                                                      bf16_t* __restrict__ out2, float* __restrict__ stats,
                                                      const char* __restrict__ pf, int pf_lines) {
  extern __shared__ __attribute__((aligned(16))) char smem_g[];
  LDS_AS char* smem = (LDS_AS char*)smem_g;
  constexpr int TM = BM / WVM / 16, TN = BN / WVN / 16, NB = BN / 16, HM = TM / 2;
  constexpr int XB = BM * 128, WB = BN * 128, STB = XB + WB;
  constexpr int NXI = BM / 8, NI = (BM + BN) / 8, LPW = (NI + 7) / 8, NK = K / 64;
  constexpr int NFULL = NI - 8 * (LPW - 1);
  constexpr bool LN = (MODE == 0 || MODE == 2);

  const int tid = threadIdx.x, lane = tid & 63;
  const int wave = __builtin_amdgcn_readfirstlane(tid >> 6);
  const int kg = wave >> 2, w4 = wave & 3;
  const int wm = w4 / WVN, wn = w4 % WVN;
  const int b = blockIdx.x, xcd = b & 7, j = b >> 3;
  int mt, nt;
  if (BM == 128) { mt = xcd * 2 + (j & 1); nt = j >> 1; }
  else           { mt = xcd * 4 + (j & 3); nt = j >> 2; }
  GST(0);
  const int m0 = mt * BM;
  const int l15 = lane & 15, g = lane >> 4;

  auto colbase = [&](int nb) -> int {
    if (MODE == 0) return (nb >> 1) * 512 + (nt * 2 + (nb & 1)) * 16;
    return nt * BN + nb * 16;
  };
  auto nb_of = [&](int ni) -> int {
    if (MODE == 0) return 2 * ni + wn;
    return wn * TN + ni;
  };
  const int tl0 = wm * TM * 16 + kg * HM * 16;
  const int tok0 = m0 + tl0;

  u32x2 resv[HM][TN];
  u32x2 biasv[TN];
  f32x4 pst[8];
  if (MODE == 1 || MODE == 3) {
#pragma unroll
    for (int mi = 0; mi < HM; ++mi)
#pragma unroll
      for (int ni = 0; ni < TN; ++ni)
        resv[mi][ni] = *(const u32x2*)(res + (size_t)(tok0 + mi * 16 + l15) * DM + colbase(nb_of(ni)) + 4 * g);
  }
  if (MODE == 2 || MODE == 3) {
#pragma unroll
    for (int ni = 0; ni < TN; ++ni) biasv[ni] = *(const u32x2*)(bias + colbase(nb_of(ni)) + 4 * g);
  }
  if (LN && !SELF) {
    if (wave < 2) {
#pragma unroll
      for (int i = 0; i < 8; ++i) pst[i] = *(const f32x4*)(stats + (size_t)(m0 + tid) * 32 + i * 4);
    }
  }

  const bf16_t* src[LPW];
  int inc[LPW];
#pragma unroll
  for (int i = 0; i < LPW; ++i) {
    int ii = wave + 8 * i;
    if (ii >= NI) ii -= 8;
    if (ii < NXI) {
      const int row = 8 * ii + (lane >> 3);
      const int ch = (lane & 7) ^ ((row >> 1) & 7);
      src[i] = X + (size_t)(m0 + row) * K + ch * 8;
      inc[i] = 64;
    } else {
      const int wi = ii - NXI;
      const int sidx = 4 * wi + (lane >> 4);
      const int kb = sidx / NB, nb = sidx % NB;
      const int r = (lane >> 1) & 7, half = lane & 1;
      const int krow = kb * 8 + (r ^ ((kb & 1) << 2));
      src[i] = W + (size_t)krow * LDW + colbase(nb) + half * 8;
      inc[i] = 64 * LDW;
    }
  }
  const unsigned lds0 = (unsigned)(uintptr_t)smem + wave * 1024;
  const bool full = wave < NFULL;
  auto issue = [&](int st) {
    const unsigned base = lds0 + (st % NST) * STB;
#pragma unroll
    for (int i = 0; i < LPW; ++i) {
      if (i < LPW - 1 || NFULL == 8 || full) glds16(src[i], base + i * 8192);
      src[i] += inc[i];
    }
  };
#pragma unroll
  for (int s = 0; s < NST; ++s) issue(s);

  const int xo_ = ((wm * TM * 16 + l15) * 128 + ((g ^ (l15 >> 1)) * 16)) ^ (kg * 64);
  const int xoA = xo_ + kg * HM * 2048, xoB = xo_ + (1 - kg) * HM * 2048;
  const int q_ = (lane & 15) >> 2, p_ = lane & 3;
  const int wo0 = XB + (kg * 4 + g) * NB * 256 + (q_ + 4 * (g & 1)) * 32 + p_ * 8;
  const int wo1 = XB + (kg * 4 + g) * NB * 256 + (q_ + 4 * (1 - (g & 1))) * 32 + p_ * 8;
  const int cnb0 = w4, cnb1 = (w4 + 4 < NB) ? w4 + 4 : NB - 1;

  f32x4 acc[TM][TN];
#pragma unroll
  for (int mi = 0; mi < TM; ++mi)
#pragma unroll
    for (int ni = 0; ni < TN; ++ni) acc[mi][ni] = f32x4{0.f, 0.f, 0.f, 0.f};
  f32x4 cs0 = {0.f, 0.f, 0.f, 0.f}, cs1 = {0.f, 0.f, 0.f, 0.f};
  f32x4 racc[TM];
#pragma unroll
  for (int mi = 0; mi < TM; ++mi) racc[mi] = f32x4{0.f, 0.f, 0.f, 0.f};
  const __bf16 one = (__bf16)1.0f;
  const bf16x8 ones8 = {one, one, one, one, one, one, one, one};

  auto ldw = [&](LDS_AS char* base, int o) -> bf16x8 {
    bf16x4 a = __builtin_amdgcn_ds_read_tr16_b64_v4bf16((LDS_AS bf16x4*)(base + wo0 + o));
    bf16x4 c = __builtin_amdgcn_ds_read_tr16_b64_v4bf16((LDS_AS bf16x4*)(base + wo1 + o));
    return __builtin_shufflevector(a, c, 0, 1, 2, 3, 4, 5, 6, 7);
  };

  bf16x8 xf[2][TM], wf[2][TN], cf[2][2];
  auto ldfrags = [&](auto KT) {
    constexpr int kt = decltype(KT)::value, p = kt & 1;
    LDS_AS char* base = smem + (kt % NST) * STB;
#pragma unroll
    for (int mi = 0; mi < TM; ++mi) xf[p][mi] = *(const LDS_AS bf16x8*)(base + (mi < HM ? xoA + mi * 2048 : xoB + (mi - HM) * 2048));
#pragma unroll
    for (int ni = 0; ni < TN; ++ni) wf[p][ni] = ldw(base, nb_of(ni) * 256);
    if (LN) { cf[p][0] = ldw(base, cnb0 * 256); cf[p][1] = ldw(base, cnb1 * 256); }
  };
  auto arrive = [&](auto KT) {
    constexpr int kt = decltype(KT)::value;
    constexpr int last = (kt + NST - 2 < NK - 1) ? kt + NST - 2 : NK - 1;
    constexpr int rem = (kt == 0 ? ((NST - 1 < NK - 1) ? NST - 1 : NK - 1) : last - kt);
    if (NFULL == 8 || full) wait_vm<rem * LPW>(); else wait_vm<rem * (LPW - 1)>();
    bar_lgkm();
  };
  arrive(IC<0>{});
  ldfrags(IC<0>{});
  auto kstep = [&](auto KT) {
    constexpr int kt = decltype(KT)::value, p = kt & 1;
    GST(1 + kt);
    if constexpr (kt + 1 < NK) {
      arrive(IC<kt + 1>{});
      if (kt + NST < NK) issue(kt + NST);
      ldfrags(IC<kt + 1>{});
    }
#pragma unroll
    for (int mi = 0; mi < TM; ++mi)
#pragma unroll
      for (int ni = 0; ni < TN; ++ni) {
        if (MODE == 0 && ni == 2) acc[mi][ni] = MFMA16(xf[p][mi], wf[p][ni], acc[mi][ni]);
        else                      acc[mi][ni] = MFMA16(wf[p][ni], xf[p][mi], acc[mi][ni]);
      }
    if (LN) {
      cs0 = MFMA16(cf[p][0], ones8, cs0);
      cs1 = MFMA16(cf[p][1], ones8, cs1);
      if (SELF) {
        if (wn) {
#pragma unroll
          for (int mi = 0; mi < TM; ++mi) racc[mi] = MFMA16(xf[p][mi], xf[p][mi], racc[mi]);
        } else {
#pragma unroll
          for (int mi = 0; mi < TM; ++mi) racc[mi] = MFMA16(ones8, xf[p][mi], racc[mi]);
        }
      }
    }
  };
  static_for<0, NK>(kstep);

  GST(1 + NK);
  if (MODE == 1 || MODE == 3) {
#pragma unroll
    for (int mi = 0; mi < HM; ++mi)
#pragma unroll
      for (int ni = 0; ni < TN; ++ni) PIN(resv[mi][ni]);
  }
  if (MODE == 2 || MODE == 3) {
#pragma unroll
    for (int ni = 0; ni < TN; ++ni) PIN(biasv[ni]);
  }
  if (LN && !SELF) {
    if (wave < 2) {
#pragma unroll
      for (int i = 0; i < 8; ++i) PIN(pst[i]);
    }
  }
  unsigned pfv = 0;
  {
    const int line = j * 512 + tid;
    if (line < pf_lines) pfv = touch(pf + (size_t)line * 128);
  }

  constexpr int NX4 = HM * TN;
  constexpr int CS_OFF = 8 * NX4 * 1024;
  constexpr int ST_OFF = CS_OFF + 1024;
  bar_lgkm();
  {
    LDS_AS f32x4* wr = (LDS_AS f32x4*)(smem + wave * (NX4 * 1024) + lane * 16);
#pragma unroll
    for (int mi = 0; mi < HM; ++mi)
#pragma unroll
      for (int ni = 0; ni < TN; ++ni) wr[(mi * TN + ni) * 64] = acc[HM + mi][ni];
    if (LN) {
      if (l15 == 0) {
        *(LDS_AS f32x4*)(smem + CS_OFF + (kg * 8 + cnb0) * 64 + g * 16) = cs0;
        *(LDS_AS f32x4*)(smem + CS_OFF + (kg * 8 + cnb1) * 64 + g * 16) = cs1;
      }
      if (SELF) {
        if (g == (wn ? (l15 >> 2) : 0)) {
#pragma unroll
          for (int mi = 0; mi < TM; ++mi) {
            const int r = wn ? (l15 & 3) : 0;
            const float v = r == 0 ? racc[mi][0] : r == 1 ? racc[mi][1] : r == 2 ? racc[mi][2] : racc[mi][3];
            const int tl = wm * TM * 16 + (mi < HM ? kg * HM + mi : (1 - kg) * HM + (mi - HM)) * 16 + l15;
            *(LDS_AS float*)(smem + ST_OFF + ((kg * 2 + wn) * BM + tl) * 4) = v;
          }
        }
      } else if (wave < 2) {
        float sm = 0.f, sq = 0.f;
#pragma unroll
        for (int i = 0; i < 8; ++i) { sm += pst[i][0] + pst[i][2]; sq += pst[i][1] + pst[i][3]; }
        const float m = sm * (1.0f / K);
        const float v = __builtin_fmaxf(sq * (1.0f / K) - m * m, 0.f);
        *(LDS_AS f32x2*)(smem + ST_OFF + tid * 8) = f32x2{m, __builtin_amdgcn_rsqf(v + 1e-5f)};
      }
    }
  }
  bar_lgkm();
  f32x4 fa[HM][TN];
  {
    const LDS_AS f32x4* rd = (const LDS_AS f32x4*)(smem + (wave ^ 4) * (NX4 * 1024) + lane * 16);
#pragma unroll
    for (int mi = 0; mi < HM; ++mi)
#pragma unroll
      for (int ni = 0; ni < TN; ++ni) fa[mi][ni] = acc[mi][ni] + rd[(mi * TN + ni) * 64];
  }
  auto tok_stats = [&](int tl) -> f32x2 {
    if (SELF) {
      const LDS_AS float* p = (const LDS_AS float*)(smem + ST_OFF + tl * 4);
      const float sm = p[0] + p[2 * BM], sq = p[BM] + p[3 * BM];
      const float m = sm * (1.0f / K);
      const float v = __builtin_fmaxf(sq * (1.0f / K) - m * m, 0.f);
      return f32x2{m, __builtin_amdgcn_rsqf(v + 1e-5f)};
    }
    return *(const LDS_AS f32x2*)(smem + ST_OFF + tl * 8);
  };
  auto col_sum4 = [&](int nb) -> f32x4 {
    return *(const LDS_AS f32x4*)(smem + CS_OFF + nb * 64 + g * 16) + *(const LDS_AS f32x4*)(smem + CS_OFF + (8 + nb) * 64 + g * 16);
  };

  if (MODE == 0) {
    const int head = nt * 2 + wn;
    const float qscale = 0.25f * 1.4426950408889634f;
    const f32x4 cq = col_sum4(nb_of(0)), ck = col_sum4(nb_of(1));
    const float cv = *(const LDS_AS float*)(smem + CS_OFF + nb_of(2) * 64 + l15 * 4) +
                     *(const LDS_AS float*)(smem + CS_OFF + (8 + nb_of(2)) * 64 + l15 * 4);
#pragma unroll
    for (int mi = 0; mi < HM; ++mi) {
      const int tok = tok0 + mi * 16 + l15;
      const f32x2 st = tok_stats(tl0 + mi * 16 + l15);
      {
        const float sc = st[1] * qscale;
        float v[4];
#pragma unroll
        for (int r = 0; r < 4; ++r) v[r] = sc * __builtin_fmaf(-st[0], cq[r], fa[mi][0][r]);
        *(u32x2*)(out0 + ((size_t)head * SEQ + tok) * 16 + 4 * g) = pack4(v[0], v[1], v[2], v[3]);
      }
      {
        float v[4];
#pragma unroll
        for (int r = 0; r < 4; ++r) v[r] = st[1] * __builtin_fmaf(-st[0], ck[r], fa[mi][1][r]);
        *(u32x2*)(out1 + (size_t)head * 32768 + (tok >> 5) * 512 + (g >> 1) * 256 + (tok & 31) * 8 + (g & 1) * 4) =
            pack4(v[0], v[1], v[2], v[3]);
      }
      {
        float v[4];
#pragma unroll
        for (int r = 0; r < 4; ++r) {
          const f32x2 sr = tok_stats(tl0 + mi * 16 + 4 * g + r);
          v[r] = sr[1] * __builtin_fmaf(-sr[0], cv, fa[mi][2][r]);
        }
        const int ks16 = (tok0 + mi * 16) >> 4;
        *(u32x2*)(out2 + (size_t)head * 32768 + ks16 * 256 + (g & 1) * 128 + l15 * 8 + (g >> 1) * 4) =
            pack4(v[0], v[1], v[2], v[3]);
      }
    }
  } else if (MODE == 2) {
#pragma unroll
    for (int mi = 0; mi < HM; ++mi) {
      const int tok = tok0 + mi * 16 + l15;
      const f32x2 st = tok_stats(tl0 + mi * 16 + l15);
      const float nrm = -st[0] * st[1];
#pragma unroll
      for (int ni = 0; ni < TN; ++ni) {
        const f32x4 cc = col_sum4(nb_of(ni));
        const float bb[4] = {bf_lo(biasv[ni][0]), bf_hi(biasv[ni][0]), bf_lo(biasv[ni][1]), bf_hi(biasv[ni][1])};
        float v[4];
#pragma unroll
        for (int r = 0; r < 4; ++r)
          v[r] = gelu_tanh(__builtin_fmaf(st[1], fa[mi][ni][r], __builtin_fmaf(nrm, cc[r], bb[r])));
        *(u32x2*)(out0 + (size_t)tok * FF + colbase(nb_of(ni)) + 4 * g) = pack4(v[0], v[1], v[2], v[3]);
      }
    }
  } else {
#pragma unroll
    for (int mi = 0; mi < HM; ++mi) {
      const int tok = tok0 + mi * 16 + l15;
      float sm = 0.f, sq = 0.f;
#pragma unroll
      for (int ni = 0; ni < TN; ++ni) {
        float rr[4] = {bf_lo(resv[mi][ni][0]), bf_hi(resv[mi][ni][0]), bf_lo(resv[mi][ni][1]), bf_hi(resv[mi][ni][1])};
        if (MODE == 3) {
          rr[0] += bf_lo(biasv[ni][0]); rr[1] += bf_hi(biasv[ni][0]); rr[2] += bf_lo(biasv[ni][1]); rr[3] += bf_hi(biasv[ni][1]);
        }
        const u32x2 pk = pack4(rr[0] + fa[mi][ni][0], rr[1] + fa[mi][ni][1], rr[2] + fa[mi][ni][2], rr[3] + fa[mi][ni][3]);
        *(u32x2*)(out0 + (size_t)tok * DM + colbase(nb_of(ni)) + 4 * g) = pk;
        const float w[4] = {bf_lo(pk[0]), bf_hi(pk[0]), bf_lo(pk[1]), bf_hi(pk[1])};
#pragma unroll
        for (int r = 0; r < 4; ++r) { sm += w[r]; sq = __builtin_fmaf(w[r], w[r], sq); }
      }
      sm = sum_g(sm); sq = sum_g(sq);
      if (g == 0) *(f32x2*)(stats + (size_t)tok * 32 + (nt * 2 + wn) * 2) = f32x2{sm, sq};
    }
  }
  GST(2 + NK);
  asm volatile("s_waitcnt vmcnt(0)" ::"v"(pfv) : "memory");
}

constexpr int A_KOFF = 0, A_VOFF = 65536, A_ONES = 131072, A_QOFF = 135168, A_COMB = 143360, A_LDS = 143360 + 8 * 64 * 40;

#define MFMA32(a, b, c) __builtin_amdgcn_mfma_f32_32x32x16_bf16(a, b, c, 0, 0, 0)
__device__ __forceinline__ bf16x8 exp_pack8(const f32x16& s, int o) {
  bf16x8 p;
#pragma unroll
  for (int r = 0; r < 8; r += 2) {
    f32x2 e = {__builtin_amdgcn_exp2f(s[o + r]), __builtin_amdgcn_exp2f(s[o + r + 1])};
    bf16x2 c = __builtin_convertvector(e, bf16x2);
    p[r] = c[0]; p[r + 1] = c[1];
  }
  return p;
}
#define LDF(p) (*(const LDS_AS bf16x8*)(p))
#define ASTEP(SC, SN, VA, VB, VNA, VNB, KO, VO)                              \
  do {                                                                       \
    SN = MFMA32(kf, qf, negm); SBAR();                                       \
    kf = LDF(kp + (KO)); VNA = LDF(vp + (VO)); VNB = LDF(vp + (VO) + 512); SBAR(); \
    { bf16x8 pa = exp_pack8(SC, 0); SBAR(); o = MFMA32(VA, pa, o); SBAR(); } \
    { bf16x8 pb = exp_pack8(SC, 8); SBAR(); o = MFMA32(VB, pb, o); SBAR(); } \
  } while (0)

__device__ __forceinline__ float max16(const f32x16 s) {
  float a = __builtin_fmaxf(__builtin_fmaxf(s[0], s[1]), s[2]);
  float c = __builtin_fmaxf(__builtin_fmaxf(s[3], s[4]), s[5]);
  a = __builtin_fmaxf(__builtin_fmaxf(a, s[6]), s[7]);
  c = __builtin_fmaxf(__builtin_fmaxf(c, s[8]), s[9]);
  a = __builtin_fmaxf(__builtin_fmaxf(a, s[10]), s[11]);
  c = __builtin_fmaxf(__builtin_fmaxf(c, s[12]), s[13]);
  a = __builtin_fmaxf(__builtin_fmaxf(a, s[14]), s[15]);
  return __builtin_fmaxf(a, c);
}
__device__ __forceinline__ float xhalf_max(float m) {
  auto rr = __builtin_amdgcn_permlane32_swap(__float_as_uint(m), __float_as_uint(m), false, false);
  return __builtin_fmaxf(__uint_as_float(rr[0]), __uint_as_float(rr[1]));
}

__global__ __launch_bounds__(1024, 4) void attn_kernel(const bf16_t* __restrict__ Q, const bf16_t* __restrict__ Kf,
                                                       const bf16_t* __restrict__ Vf, bf16_t* __restrict__ O,
                                                       const char* __restrict__ pf0, int pf0_lines,
                                                       const char* __restrict__ pf1, int pf1_lines) {
  extern __shared__ __attribute__((aligned(16))) char smem_g[];
  LDS_AS char* smem = (LDS_AS char*)smem_g;
  const int tid = threadIdx.x, lane = tid & 63;
  const int wave = __builtin_amdgcn_readfirstlane(tid >> 6);
  const int kh = wave >> 3, qw = wave & 7;
  const int b = blockIdx.x, xcd = b & 7, j = b >> 3;
  const int head = j, qt = xcd;
  const int r32 = lane & 31, hh = lane >> 5;
  const int q0 = qt * 256 + qw * 32;

  const unsigned lds0 = (unsigned)(uintptr_t)smem;
  {
    const bf16_t* ks = Kf + (size_t)head * 32768 + kh * 16384 + qw * 512 + lane * 8;
    const bf16_t* vs = Vf + (size_t)head * 32768 + kh * 16384 + qw * 512 + lane * 8;
    const unsigned kd = lds0 + A_KOFF + kh * 32768 + qw * 1024, vd = lds0 + A_VOFF + kh * 32768 + qw * 1024;
    glds16(Q + ((size_t)head * SEQ + q0 + r32) * 16 + hh * 8, lds0 + A_QOFF + qw * 1024);
#pragma unroll
    for (int c = 0; c < 4; ++c) {
      glds16(ks + c * 4096, kd + c * 8192);
      glds16(vs + c * 4096, vd + c * 8192);
    }
  }
  if (tid < 256) *(LDS_AS u32x4*)(smem + A_ONES + tid * 16) = u32x4{0x3F803F80u, 0x3F803F80u, 0x3F803F80u, 0x3F803F80u};

  LDS_AS char* kb = smem + A_KOFF + kh * 32768 + lane * 16;
  const bool real = r32 < 16;
  LDS_AS char* vb = real ? (smem + A_VOFF + kh * 32768 + hh * 256 + r32 * 16) : (smem + A_ONES);
  const int vstride = real ? 2048 : 0;

  wait_bar<6>();
  const bf16x8 qf = LDF(smem + A_QOFF + qw * 1024 + lane * 16);
  f32x16 negm;
  float mref;
  {
    f32x16 z = {};
    f32x16 s = MFMA32(LDF(smem + A_KOFF + lane * 16), qf, z);
    mref = xhalf_max(max16(s));
#pragma unroll
    for (int r = 0; r < 16; ++r) negm[r] = -mref;
  }
  f32x16 o = {};
  unsigned pfv = 0;
#pragma unroll 1
  for (int pass = 0; pass < 2; ++pass) {
    LDS_AS char* kp = kb;
    LDS_AS char* vp = vb;
    bf16x8 kf = LDF(kp), va0 = LDF(vp), vb0 = LDF(vp + 512), va1, vb1;
    f32x16 sA = MFMA32(kf, qf, negm), sB;
    kf = LDF(kp + 1024);
    SBAR();
#pragma unroll 1
    for (int t = 0; t < 32; t += 2) {
      if (t == 6 && pass == 0) {
        wait_bar<0>();
        const int line = j * 1024 + tid;
        if (line < pf0_lines) pfv = touch(pf0 + (size_t)line * 128);
        else if (line - pf0_lines < pf1_lines) pfv = touch(pf1 + (size_t)(line - pf0_lines) * 128);
      }
      ASTEP(sA, sB, va0, vb0, va1, vb1, 2048, 1024);
      ASTEP(sB, sA, va1, vb1, va0, vb0, 3072, 2048);
      kp += 2048; vp += vstride;
    }
    if (__builtin_expect(pass == 1 || !__any(!(o[8] < 1.2676506e30f)), 1)) break;
    float m = -INFINITY;
    f32x16 z = {};
    for (int t = 0; t < 32; ++t) {
      f32x16 sc = MFMA32(LDF(kb + t * 1024), qf, z);
      m = __builtin_fmaxf(m, max16(sc));
    }
    mref = xhalf_max(m);
#pragma unroll
    for (int r = 0; r < 16; ++r) negm[r] = -mref;
    o = z;
  }

  LDS_AS float* cb = (LDS_AS float*)(smem + A_COMB + qw * (64 * 40) + lane * 4);
  if (kh) {
#pragma unroll
    for (int r = 0; r < 9; ++r) cb[r * 64] = o[r];
    cb[9 * 64] = mref;
  }
  bar_lgkm();
  if (!kh) {
    const float m2 = cb[9 * 64];
    const float mm = __builtin_fmaxf(mref, m2);
    const float f1 = __builtin_amdgcn_exp2f(mref - mm), f2 = __builtin_amdgcn_exp2f(m2 - mm);
    const float l = o[8] * f1 + cb[8 * 64] * f2;
    const float inv = __builtin_amdgcn_rcpf(l);
    const float a1 = f1 * inv, a2 = f2 * inv;
    float v[8];
#pragma unroll
    for (int r = 0; r < 8; ++r) v[r] = o[r] * a1 + cb[r * 64] * a2;
    bf16_t* op = O + (size_t)(q0 + r32) * DM + head * 16 + 4 * hh;
    *(u32x2*)(op) = pack4(v[0], v[1], v[2], v[3]);
    *(u32x2*)(op + 8) = pack4(v[4], v[5], v[6], v[7]);
  }
  asm volatile("s_waitcnt vmcnt(0)" ::"v"(pfv) : "memory");
}

constexpr int QKV_NST = 5, WO_NST = 8, F1_NST = 5, F2_NST = 8;
#define QKV0_K gemm_kernel<128, 96, 2, 2, 512, 1536, QKV_NST, 0, 1>
#define QKV1_K gemm_kernel<128, 96, 2, 2, 512, 1536, QKV_NST, 0, 0>
#define WO_K gemm_kernel<64, 64, 2, 2, 512, 512, WO_NST, 1, 0>
#define F1_K gemm_kernel<128, 112, 4, 1, 512, 1792, F1_NST, 2, 0>
#define F2_K gemm_kernel<64, 64, 2, 2, 1792, 512, F2_NST, 3, 0>
constexpr int QKV_LDS = QKV_NST * (128 + 96) * 128, WO_LDS = WO_NST * 128 * 128, F1_LDS = F1_NST * (128 + 112) * 128,
              F2_LDS = F2_NST * 128 * 128;

extern "C" void kernel_launch(void* const* d_in, const int* in_sizes, int n_in, void* d_out, int out_size, void* d_ws,
                              size_t ws_size, hipStream_t stream) {
  const bf16_t* x = (const bf16_t*)d_in[0];
  const bf16_t* wqkv = (const bf16_t*)d_in[1];
  const bf16_t* wo = (const bf16_t*)d_in[2];
  const bf16_t* w1 = (const bf16_t*)d_in[4];
  const bf16_t* b1 = (const bf16_t*)d_in[5];
  const bf16_t* w2 = (const bf16_t*)d_in[6];
  const bf16_t* b2 = (const bf16_t*)d_in[7];
  char* ws = (char*)d_ws;
  bf16_t* Qb = (bf16_t*)(ws + (0 << 20));
  bf16_t* Kb = (bf16_t*)(ws + (2 << 20));
  bf16_t* Vb = (bf16_t*)(ws + (4 << 20));
  bf16_t* Ob = (bf16_t*)(ws + (6 << 20));
  bf16_t* X1 = (bf16_t*)(ws + (8 << 20));
  bf16_t* Hb = (bf16_t*)(ws + (10 << 20));
  bf16_t* X2 = (bf16_t*)(ws + (18 << 20));
  float* St1 = (float*)(ws + (20 << 20));
  float* St2 = (float*)(ws + (21 << 20));
  bf16_t* out = (bf16_t*)d_out;

  hipFuncSetAttribute((const void*)QKV0_K, hipFuncAttributeMaxDynamicSharedMemorySize, QKV_LDS);
  hipFuncSetAttribute((const void*)QKV1_K, hipFuncAttributeMaxDynamicSharedMemorySize, QKV_LDS);
  hipFuncSetAttribute((const void*)WO_K, hipFuncAttributeMaxDynamicSharedMemorySize, WO_LDS);
  hipFuncSetAttribute((const void*)F1_K, hipFuncAttributeMaxDynamicSharedMemorySize, F1_LDS);
  hipFuncSetAttribute((const void*)F2_K, hipFuncAttributeMaxDynamicSharedMemorySize, F2_LDS);
  hipFuncSetAttribute((const void*)attn_kernel, hipFuncAttributeMaxDynamicSharedMemorySize, A_LDS);

  for (int layer = 0; layer < 2; ++layer) {
    const bf16_t* xin = layer == 0 ? x : X2;
    bf16_t* xout = layer == 0 ? X2 : out;
    const char* none = (const char*)x;
    if (layer == 0) QKV0_K<<<256, 512, QKV_LDS, stream>>>(xin, wqkv, nullptr, nullptr, Qb, Kb, Vb, nullptr, none, 0);
    else            QKV1_K<<<256, 512, QKV_LDS, stream>>>(xin, wqkv, nullptr, nullptr, Qb, Kb, Vb, St2, none, 0);
    attn_kernel<<<256, 1024, A_LDS, stream>>>(Qb, Kb, Vb, Ob, (const char*)wo, DM * DM * 2 / 128, (const char*)w1, DM * FF * 2 / 128);
    WO_K<<<256, 512, WO_LDS, stream>>>(Ob, wo, nullptr, xin, X1, nullptr, nullptr, St1, none, 0);
    F1_K<<<256, 512, F1_LDS, stream>>>(X1, w1, b1, nullptr, Hb, nullptr, nullptr, St1, (const char*)w2, FF * DM * 2 / 128);
    F2_K<<<256, 512, F2_LDS, stream>>>(Hb, w2, b2, X1, xout, nullptr, nullptr, St2, (const char*)wqkv,
                                       layer == 0 ? DM * 3 * DM * 2 / 128 : 0);
  }
}
```

```cpp
#include <hip/hip_runtime.h>
#include <hip/hip_bf16.h>
#include <stdint.h>

typedef __bf16 bf16_t;
typedef __bf16 bf16x2 __attribute__((ext_vector_type(2)));
typedef __bf16 bf16x4 __attribute__((ext_vector_type(4)));
typedef __bf16 bf16x8 __attribute__((ext_vector_type(8)));
typedef float f32x2 __attribute__((ext_vector_type(2)));
typedef float f32x4 __attribute__((ext_vector_type(4)));
typedef float f32x16 __attribute__((ext_vector_type(16)));
typedef unsigned u32x2 __attribute__((ext_vector_type(2)));
typedef unsigned u32x4 __attribute__((ext_vector_type(4)));

#define LDS_AS __attribute__((address_space(3)))
#define GLB_AS __attribute__((address_space(1)))

constexpr int SEQ = 2048, DM = 512, NH = 32, DH = 16, FF = 1792;

__device__ __forceinline__ void glds16(const void* g, unsigned lds_base) {
  unsigned keep;
  asm volatile("s_mov_b32 %0, m0\n\ts_mov_b32 m0, %2\n\ts_nop 0\n\tglobal_load_lds_dwordx4 %1, off\n\ts_mov_b32 m0, %0"
               : "=&s"(keep) : "v"(g), "s"(lds_base) : "memory");
}
template <int IMM>
__device__ __forceinline__ void glds16s(unsigned voff, const void* sbase, unsigned lds_base) {
  asm volatile("s_add_i32 m0, %2, %3\n\ts_nop 0\n\tglobal_load_lds_dwordx4 %0, %1"
               :: "v"(voff), "s"(sbase), "s"(lds_base), "i"(IMM) : "memory", "scc");
}
__device__ __forceinline__ void touch(const void* p, unsigned lds_dummy) {
  unsigned keep;
  asm volatile("s_mov_b32 %0, m0\n\ts_mov_b32 m0, %2\n\ts_nop 0\n\tglobal_load_lds_dword %1, off\n\ts_mov_b32 m0, %0"
               : "=&s"(keep) : "v"(p), "s"(lds_dummy) : "memory");
}
#define ST8(ptr, val) (*(u32x2*)(ptr) = (val))
#define SBAR() __builtin_amdgcn_sched_barrier(0)
#define PIN(x) asm volatile("" : "+v"(x))
__device__ __forceinline__ float rbf(float v) { return (float)(__bf16)v; }
__device__ __forceinline__ u32x2 pack4(float a, float b, float c, float d) {
  f32x2 lo = {a, b}, hi = {c, d};
  bf16x2 l = __builtin_convertvector(lo, bf16x2), h = __builtin_convertvector(hi, bf16x2);
  u32x2 r = {__builtin_bit_cast(unsigned, l), __builtin_bit_cast(unsigned, h)};
  return r;
}
__device__ __forceinline__ float bf_lo(unsigned u) { return __builtin_bit_cast(float, u << 16); }
__device__ __forceinline__ float bf_hi(unsigned u) { return __builtin_bit_cast(float, u & 0xffff0000u); }

#define GST(i)
#define GST_DECL
#define GST_FLUSH
template <int N>
__device__ __forceinline__ void wait_bar() {
  asm volatile("s_waitcnt vmcnt(%0) lgkmcnt(0)\n\ts_barrier" ::"i"(N) : "memory");
}

template <int I> struct IC { static constexpr int value = I; };
template <int I, int N, class F>
__device__ __forceinline__ void static_for(F& f) {
  if constexpr (I < N) { f(IC<I>{}); static_for<I + 1, N>(f); }
}

__device__ __forceinline__ float gelu_tanh(float x) {
  const float c1 = -2.0f * 0.7978845608028654f * 1.4426950408889634f;
  const float c2 = c1 * 0.044715f;
  float t = x * __builtin_fmaf(x * x, c2, c1);
  float e = __builtin_amdgcn_exp2f(t);
  return x * __builtin_amdgcn_rcpf(1.0f + e);
}

template <int N>
__device__ __forceinline__ void wait_vm() { asm volatile("s_waitcnt vmcnt(%0)" ::"i"(N) : "memory"); }
__device__ __forceinline__ void bar_lgkm() { asm volatile("s_waitcnt lgkmcnt(0)\n\ts_barrier" ::: "memory"); }
#define MFMA16(a, b, c) __builtin_amdgcn_mfma_f32_16x16x32_bf16(a, b, c, 0, 0, 0)
__device__ __forceinline__ float sum_g(float v) {
  auto a = __builtin_amdgcn_permlane16_swap(__float_as_uint(v), __float_as_uint(v), false, false);
  v = __uint_as_float(a[0]) + __uint_as_float(a[1]);
  auto b = __builtin_amdgcn_permlane32_swap(__float_as_uint(v), __float_as_uint(v), false, false);
  return __uint_as_float(b[0]) + __uint_as_float(b[1]);
}

template <int BM, int BN, int WVM, int WVN, int K, int LDW, int NST, int MODE, int SELF>
__global__ __launch_bounds__(768, 3) void gemm_kernel(const bf16_t* __restrict__ X, const bf16_t* __restrict__ W,
                                                      const bf16_t* __restrict__ bias, const bf16_t* __restrict__ res,
                                                      bf16_t* __restrict__ out0, bf16_t* __restrict__ out1,
                                                      bf16_t* __restrict__ out2, float* __restrict__ stats,
                                                      const char* __restrict__ pf, int pf_lines) {
  extern __shared__ __attribute__((aligned(16))) char smem_g[];
  LDS_AS char* smem = (LDS_AS char*)smem_g;
  constexpr int TM = BM / WVM / 16, TN = BN / WVN / 16, NB = BN / 16, HM = TM / 2;
  constexpr int XB = BM * 128, WB = BN * 128, STB = XB + WB;
  constexpr int NXI = BM / 8, NI = (BM + BN) / 8, LPL = (NI + 3) / 4, NK = K / 64;
  constexpr int NFULL = NI - 4 * (LPL - 1);
#define DEP_BIG 2
#define DEP_SMALL 3
  constexpr int DEP = (BM == 128) ? DEP_BIG : DEP_SMALL;
  static_assert(NST >= DEP + 1, "ring too small");
  constexpr bool LN = (MODE == 0 || MODE == 2);
  constexpr int NX4 = HM * TN;
  constexpr int EX_OFF = NST * STB;
  constexpr int CS_OFF = EX_OFF;
  constexpr int ST_OFF = CS_OFF + 512;
  constexpr int PF_OFF = ST_OFF + 1024;
  static_assert(8 * NX4 * 1024 <= ((NK - 1) % NST) * STB, "exchange area overlaps the last stage");

  const int tid = threadIdx.x, lane = tid & 63;
  const int wave = __builtin_amdgcn_readfirstlane(tid >> 6);
  const int b = blockIdx.x, xcd = b & 7, j = b >> 3;
  int mt, nt;
  if (BM == 128) { mt = xcd * 2 + (j & 1); nt = j >> 1; }
  else           { mt = xcd * 4 + (j & 3); nt = j >> 2; }
  GST_DECL; GST(0);
  const int m0 = mt * BM;
  const int l15 = lane & 15, g = lane >> 4;
  const int q_ = (lane & 15) >> 2, p_ = lane & 3;
  const __bf16 one = (__bf16)1.0f;
  const bf16x8 ones8 = {one, one, one, one, one, one, one, one};

  auto colbase = [&](int nb) -> int {
    if (MODE == 0) return (nb >> 1) * 512 + (nt * 2 + (nb & 1)) * 16;
    return nt * BN + nb * 16;
  };
  auto tr8 = [&](LDS_AS char* p0, LDS_AS char* p1) -> bf16x8 {
    bf16x4 a = __builtin_amdgcn_ds_read_tr16_b64_v4bf16((LDS_AS bf16x4*)p0);
    bf16x4 c = __builtin_amdgcn_ds_read_tr16_b64_v4bf16((LDS_AS bf16x4*)p1);
    return __builtin_shufflevector(a, c, 0, 1, 2, 3, 4, 5, 6, 7);
  };

  if (wave >= 8) {
    const int lw = wave - 8;
    constexpr int XS = NXI / 4;
    unsigned voff[LPL];
    {
      const int row = 8 * lw + (lane >> 3);
      voff[0] = (unsigned)(row * K + (((lane & 7) ^ ((row >> 1) & 7)) * 8)) * 2u;
#pragma unroll
      for (int i = 1; i < XS; ++i) voff[i] = voff[0] + i * 32 * K * 2;
#pragma unroll
      for (int i = XS; i < LPL; ++i) {
        const int sidx = 4 * (lw + 4 * (i - XS)) + (lane >> 4);
        const int kb = sidx / NB, nb = sidx % NB;
        const int krow = kb * 8 + (((lane >> 1) & 7) ^ ((kb & 1) << 2));
        voff[i] = (unsigned)(krow * LDW + colbase(nb) + (lane & 1) * 8) * 2u;
      }
    }
    const char* xs_ = (const char*)(X + (size_t)m0 * K);
    const char* ws_ = (const char*)W;
    const unsigned lds0 = (unsigned)(uintptr_t)smem + lw * 1024;
    const bool full = lw < NFULL;
    auto issue = [&](auto ST) {
      constexpr int st = decltype(ST)::value;
      auto one_ = [&](auto I) {
        constexpr int i = decltype(I)::value;
        if (i < LPL - 1 || NFULL == 4 || full)
          glds16s<(st % NST) * STB + i * 4096>(voff[i], i < XS ? xs_ + st * 128 : ws_ + (size_t)st * (64 * LDW * 2), lds0);
      };
      static_for<0, LPL>(one_);
    };
    f32x4 pst[8];
    if (LN && !SELF) {
      if (lw < 2) {
#pragma unroll
        for (int i = 0; i < 8; ++i) pst[i] = *(const f32x4*)(stats + (size_t)(m0 + lw * 64 + lane) * 32 + i * 4);
      }
    }
    static_for<0, (DEP < NK ? DEP : NK)>(issue);

    const int cnb0 = lw, cnb1 = (lw + 4 < NB) ? lw + 4 : NB - 1;
    const int wo0 = XB + g * NB * 256 + (q_ + 4 * (g & 1)) * 32 + p_ * 8;
    const int wo1 = XB + g * NB * 256 + (q_ + 4 * (1 - (g & 1))) * 32 + p_ * 8;
    const int xo = (lw * 32 + l15) * 128 + ((g ^ (l15 >> 1)) * 16);
    f32x4 cs0 = {0.f, 0.f, 0.f, 0.f}, cs1 = {0.f, 0.f, 0.f, 0.f};
    f32x4 rs[2], rq[2];
    rs[0] = rs[1] = rq[0] = rq[1] = f32x4{0.f, 0.f, 0.f, 0.f};
    auto lstep = [&](auto KT) {
      constexpr int kt = decltype(KT)::value;
      constexpr int last = (kt + DEP - 1 < NK - 1) ? kt + DEP - 1 : NK - 1;
      if (NFULL == 4 || full) wait_vm<(last - kt) * LPL>(); else wait_vm<(last - kt) * (LPL - 1)>();
      bar_lgkm();
      if constexpr (kt + DEP < NK) issue(IC<kt + DEP>{});
      if (LN) {
        LDS_AS char* base = smem + (kt % NST) * STB;
#pragma unroll
        for (int ks = 0; ks < 2; ++ks) {
          const int o0 = (ks * 4 * NB + cnb0) * 256, o1 = (ks * 4 * NB + cnb1) * 256;
          cs0 = MFMA16(tr8(base + wo0 + o0, base + wo1 + o0), ones8, cs0);
          cs1 = MFMA16(tr8(base + wo0 + o1, base + wo1 + o1), ones8, cs1);
          if (SELF) {
#pragma unroll
            for (int mi = 0; mi < 2; ++mi) {
              const bf16x8 xf = *(const LDS_AS bf16x8*)(base + ((xo ^ (ks * 64)) + mi * 2048));
              rs[mi] = MFMA16(ones8, xf, rs[mi]);
              rq[mi] = MFMA16(xf, xf, rq[mi]);
            }
          }
        }
      }
    };
    static_for<0, NK>(lstep);
    if (LN) {
      if (l15 == 0) {
        *(LDS_AS f32x4*)(smem + CS_OFF + cnb0 * 64 + g * 16) = cs0;
        *(LDS_AS f32x4*)(smem + CS_OFF + cnb1 * 64 + g * 16) = cs1;
      }
      if (SELF) {
        if (g == (l15 >> 2)) {
          const int r = l15 & 3;
#pragma unroll
          for (int mi = 0; mi < 2; ++mi) {
            const float sq = r == 0 ? rq[mi][0] : r == 1 ? rq[mi][1] : r == 2 ? rq[mi][2] : rq[mi][3];
            const float m = rs[mi][0] * (1.0f / K);
            const float v = __builtin_fmaxf(sq * (1.0f / K) - m * m, 0.f);
            *(LDS_AS f32x2*)(smem + ST_OFF + (lw * 32 + mi * 16 + l15) * 8) = f32x2{m, __builtin_amdgcn_rsqf(v + 1e-5f)};
          }
        }
      } else if (lw < 2) {
        float sm = 0.f, sq = 0.f;
#pragma unroll
        for (int i = 0; i < 8; ++i) { PIN(pst[i]); sm += pst[i][0] + pst[i][2]; sq += pst[i][1] + pst[i][3]; }
        const float m = sm * (1.0f / K);
        const float v = __builtin_fmaxf(sq * (1.0f / K) - m * m, 0.f);
        *(LDS_AS f32x2*)(smem + ST_OFF + (lw * 64 + lane) * 8) = f32x2{m, __builtin_amdgcn_rsqf(v + 1e-5f)};
      }
    }
#pragma unroll
    for (int i = 0; i < 2; ++i) {
      const int line = j * 512 + i * 256 + lw * 64 + lane;
      if (line < pf_lines) touch(pf + (size_t)line * 128, (unsigned)(uintptr_t)smem + PF_OFF);
    }
    bar_lgkm();
    asm volatile("s_waitcnt vmcnt(0)" ::: "memory");
    return;
  }

  const int kg = wave >> 2, w4 = wave & 3;
  const int wm = w4 / WVN, wn = w4 % WVN;
  auto nb_of = [&](int ni) -> int {
    if (MODE == 0) return 2 * ni + wn;
    return wn * TN + ni;
  };
  const int tl0 = wm * TM * 16 + kg * HM * 16;
  const int tok0 = m0 + tl0;

  u32x2 resv[HM][TN];
  u32x2 biasv[TN];
  if (MODE == 1 || MODE == 3) {
#pragma unroll
    for (int mi = 0; mi < HM; ++mi)
#pragma unroll
      for (int ni = 0; ni < TN; ++ni)
        resv[mi][ni] = *(const u32x2*)(res + (size_t)(tok0 + mi * 16 + l15) * DM + colbase(nb_of(ni)) + 4 * g);
  }
  if (MODE == 2 || MODE == 3) {
#pragma unroll
    for (int ni = 0; ni < TN; ++ni) biasv[ni] = *(const u32x2*)(bias + colbase(nb_of(ni)) + 4 * g);
  }

  const int xo_ = ((wm * TM * 16 + l15) * 128 + ((g ^ (l15 >> 1)) * 16)) ^ (kg * 64);
  const int xoA = xo_ + kg * HM * 2048, xoB = xo_ + (1 - kg) * HM * 2048;
  const int wo0 = XB + (kg * 4 + g) * NB * 256 + (q_ + 4 * (g & 1)) * 32 + p_ * 8 + nb_of(0) * 256;
  const int wo1 = XB + (kg * 4 + g) * NB * 256 + (q_ + 4 * (1 - (g & 1))) * 32 + p_ * 8 + nb_of(0) * 256;
  constexpr int NSTEP = (MODE == 0) ? 512 : 256;

  f32x4 acc[TM][TN];
#pragma unroll
  for (int mi = 0; mi < TM; ++mi)
#pragma unroll
    for (int ni = 0; ni < TN; ++ni) acc[mi][ni] = f32x4{0.f, 0.f, 0.f, 0.f};

  bf16x8 xf[2][TM], wf[2][TN];
  auto ldfrags = [&](auto KT) {
    constexpr int kt = decltype(KT)::value, p = kt & 1;
    LDS_AS char* base = smem + (kt % NST) * STB;
#pragma unroll
    for (int mi = 0; mi < TM; ++mi) xf[p][mi] = *(const LDS_AS bf16x8*)(base + (mi < HM ? xoA + mi * 2048 : xoB + (mi - HM) * 2048));
#pragma unroll
    for (int ni = 0; ni < TN; ++ni) wf[p][ni] = tr8(base + wo0 + ni * NSTEP, base + wo1 + ni * NSTEP);
  };
  GST(1);
  bar_lgkm();
  GST(2);
  ldfrags(IC<0>{});
  auto kstep = [&](auto KT) {
    constexpr int kt = decltype(KT)::value, p = kt & 1;
    if constexpr (kt + 1 < NK) {
      bar_lgkm();
      ldfrags(IC<kt + 1>{});
    }
#pragma unroll
    for (int mi = 0; mi < TM; ++mi)
#pragma unroll
      for (int ni = 0; ni < TN; ++ni) {
        if (MODE == 0 && ni == 2) acc[mi][ni] = MFMA16(xf[p][mi], wf[p][ni], acc[mi][ni]);
        else                      acc[mi][ni] = MFMA16(wf[p][ni], xf[p][mi], acc[mi][ni]);
      }
  };
  static_for<0, NK>(kstep);

  GST(3);
  {
    LDS_AS f32x4* wr = (LDS_AS f32x4*)(smem + wave * (NX4 * 1024) + lane * 16);
#pragma unroll
    for (int mi = 0; mi < HM; ++mi)
#pragma unroll
      for (int ni = 0; ni < TN; ++ni) wr[(mi * TN + ni) * 64] = acc[HM + mi][ni];
  }
  GST(4);
  bar_lgkm();
  GST(5);
  f32x4 fa[HM][TN];
  {
    const LDS_AS f32x4* rd = (const LDS_AS f32x4*)(smem + (wave ^ 4) * (NX4 * 1024) + lane * 16);
#pragma unroll
    for (int mi = 0; mi < HM; ++mi)
#pragma unroll
      for (int ni = 0; ni < TN; ++ni) fa[mi][ni] = acc[mi][ni] + rd[(mi * TN + ni) * 64];
  }
  auto tok_stats = [&](int tl) -> f32x2 { return *(const LDS_AS f32x2*)(smem + ST_OFF + tl * 8); };
  auto col_sum4 = [&](int nb) -> f32x4 { return *(const LDS_AS f32x4*)(smem + CS_OFF + nb * 64 + g * 16); };

  if (MODE == 0) {
    const int head = nt * 2 + wn;
    const float qscale = 0.25f * 1.4426950408889634f;
    const f32x4 cq = col_sum4(nb_of(0)), ck = col_sum4(nb_of(1));
    const float cv = *(const LDS_AS float*)(smem + CS_OFF + nb_of(2) * 64 + l15 * 4);
#pragma unroll
    for (int mi = 0; mi < HM; ++mi) {
      const int tok = tok0 + mi * 16 + l15;
      const f32x2 st = tok_stats(tl0 + mi * 16 + l15);
      {
        const float sc = st[1] * qscale;
        float v[4];
#pragma unroll
        for (int r = 0; r < 4; ++r) v[r] = sc * __builtin_fmaf(-st[0], cq[r], fa[mi][0][r]);
        ST8(out0 + ((size_t)head * SEQ + tok) * 16 + 4 * g, pack4(v[0], v[1], v[2], v[3]));
      }
      {
        float v[4];
#pragma unroll
        for (int r = 0; r < 4; ++r) v[r] = st[1] * __builtin_fmaf(-st[0], ck[r], fa[mi][1][r]);
        ST8(out1 + (size_t)head * 32768 + (tok >> 5) * 512 + (g >> 1) * 256 + (tok & 31) * 8 + (g & 1) * 4,
            pack4(v[0], v[1], v[2], v[3]));
      }
      {
        float v[4];
#pragma unroll
        for (int r = 0; r < 4; ++r) {
          const f32x2 sr = tok_stats(tl0 + mi * 16 + 4 * g + r);
          v[r] = sr[1] * __builtin_fmaf(-sr[0], cv, fa[mi][2][r]);
        }
        const int ks16 = (tok0 + mi * 16) >> 4;
        ST8(out2 + (size_t)head * 32768 + ks16 * 256 + (g & 1) * 128 + l15 * 8 + (g >> 1) * 4,
            pack4(v[0], v[1], v[2], v[3]));
      }
    }
  } else if (MODE == 2) {
#pragma unroll
    for (int mi = 0; mi < HM; ++mi) {
      const int tok = tok0 + mi * 16 + l15;
      const f32x2 st = tok_stats(tl0 + mi * 16 + l15);
      const float nrm = -st[0] * st[1];
#pragma unroll
      for (int ni = 0; ni < TN; ++ni) {
        const f32x4 cc = col_sum4(nb_of(ni));
        const float bb[4] = {bf_lo(biasv[ni][0]), bf_hi(biasv[ni][0]), bf_lo(biasv[ni][1]), bf_hi(biasv[ni][1])};
        float v[4];
#pragma unroll
        for (int r = 0; r < 4; ++r)
          v[r] = gelu_tanh(__builtin_fmaf(st[1], fa[mi][ni][r], __builtin_fmaf(nrm, cc[r], bb[r])));
        ST8(out0 + (size_t)tok * FF + colbase(nb_of(ni)) + 4 * g, pack4(v[0], v[1], v[2], v[3]));
      }
    }
  } else {
#pragma unroll
    for (int mi = 0; mi < HM; ++mi) {
      const int tok = tok0 + mi * 16 + l15;
      float sm = 0.f, sq = 0.f;
#pragma unroll
      for (int ni = 0; ni < TN; ++ni) {
        float rr[4] = {bf_lo(resv[mi][ni][0]), bf_hi(resv[mi][ni][0]), bf_lo(resv[mi][ni][1]), bf_hi(resv[mi][ni][1])};
        if (MODE == 3) {
          rr[0] += bf_lo(biasv[ni][0]); rr[1] += bf_hi(biasv[ni][0]); rr[2] += bf_lo(biasv[ni][1]); rr[3] += bf_hi(biasv[ni][1]);
        }
        const u32x2 pk = pack4(rr[0] + fa[mi][ni][0], rr[1] + fa[mi][ni][1], rr[2] + fa[mi][ni][2], rr[3] + fa[mi][ni][3]);
        ST8(out0 + (size_t)tok * DM + colbase(nb_of(ni)) + 4 * g, pk);
        const float w[4] = {bf_lo(pk[0]), bf_hi(pk[0]), bf_lo(pk[1]), bf_hi(pk[1])};
#pragma unroll
        for (int r = 0; r < 4; ++r) { sm += w[r]; sq = __builtin_fmaf(w[r], w[r], sq); }
      }
      sm = sum_g(sm); sq = sum_g(sq);
      if (g == 0) *(f32x2*)(stats + (size_t)tok * 32 + (nt * 2 + wn) * 2) = f32x2{sm, sq};
    }
  }
  GST(6);
  GST(7);
  GST_FLUSH;
}

constexpr int A_KOFF = 0, A_VOFF = 65536, A_ONES = 131072, A_QOFF = 135168, A_COMB = 143360, A_LDS = 143360 + 8 * 64 * 40;

#define MFMA32(a, b, c) __builtin_amdgcn_mfma_f32_32x32x16_bf16(a, b, c, 0, 0, 0)
__device__ __forceinline__ bf16x8 exp_pack8(const f32x16& s, int o) {
  bf16x8 p;
#pragma unroll
  for (int r = 0; r < 8; r += 2) {
    f32x2 e = {__builtin_amdgcn_exp2f(s[o + r]), __builtin_amdgcn_exp2f(s[o + r + 1])};
    bf16x2 c = __builtin_convertvector(e, bf16x2);
    p[r] = c[0]; p[r + 1] = c[1];
  }
  return p;
}
#define LDF(p) (*(const LDS_AS bf16x8*)(p))
#define ASTEP(SC, SN, VA, VB, VNA, VNB, KO, VO)                              \
  do {                                                                       \
    SN = MFMA32(kf, qf, negm); SBAR();                                       \
    kf = LDF(kp + (KO)); VNA = LDF(vp + (VO)); VNB = LDF(vp + (VO) + 512); SBAR(); \
    { bf16x8 pa = exp_pack8(SC, 0); SBAR(); o = MFMA32(VA, pa, o); SBAR(); } \
    { bf16x8 pb = exp_pack8(SC, 8); SBAR(); o = MFMA32(VB, pb, o); SBAR(); } \
  } while (0)

__device__ __forceinline__ float max16(const f32x16 s) {
  float a = __builtin_fmaxf(__builtin_fmaxf(s[0], s[1]), s[2]);
  float c = __builtin_fmaxf(__builtin_fmaxf(s[3], s[4]), s[5]);
  a = __builtin_fmaxf(__builtin_fmaxf(a, s[6]), s[7]);
  c = __builtin_fmaxf(__builtin_fmaxf(c, s[8]), s[9]);
  a = __builtin_fmaxf(__builtin_fmaxf(a, s[10]), s[11]);
  c = __builtin_fmaxf(__builtin_fmaxf(c, s[12]), s[13]);
  a = __builtin_fmaxf(__builtin_fmaxf(a, s[14]), s[15]);
  return __builtin_fmaxf(a, c);
}
__device__ __forceinline__ float xhalf_max(float m) {
  auto rr = __builtin_amdgcn_permlane32_swap(__float_as_uint(m), __float_as_uint(m), false, false);
  return __builtin_fmaxf(__uint_as_float(rr[0]), __uint_as_float(rr[1]));
}

__global__ __launch_bounds__(1024, 4) void attn_kernel(const bf16_t* __restrict__ Q, const bf16_t* __restrict__ Kf,
                                                       const bf16_t* __restrict__ Vf, bf16_t* __restrict__ O,
                                                       const char* __restrict__ pf0, int pf0_lines,
                                                       const char* __restrict__ pf1, int pf1_lines) {
  extern __shared__ __attribute__((aligned(16))) char smem_g[];
  LDS_AS char* smem = (LDS_AS char*)smem_g;
  const int tid = threadIdx.x, lane = tid & 63;
  const int wave = __builtin_amdgcn_readfirstlane(tid >> 6);
  const int kh = wave >> 3, qw = wave & 7;
  const int b = blockIdx.x, xcd = b & 7, j = b >> 3;
  const int head = xcd * 4 + (j & 3), qt = j >> 2;
  const int r32 = lane & 31, hh = lane >> 5;
  const int q0 = qt * 256 + qw * 32;

  const unsigned lds0 = (unsigned)(uintptr_t)smem;
    const bf16_t* ks = Kf + (size_t)head * 32768 + kh * 16384 + qw * 512 + lane * 8;
    const bf16_t* vs = Vf + (size_t)head * 32768 + kh * 16384 + qw * 512 + lane * 8;
    const unsigned kd = lds0 + A_KOFF + kh * 32768 + qw * 1024, vd = lds0 + A_VOFF + kh * 32768 + qw * 1024;
    glds16(Q + ((size_t)head * SEQ + q0 + r32) * 16 + hh * 8, lds0 + A_QOFF + qw * 1024);
    glds16(ks, kd);
    glds16(vs, vd);

  if (tid < 256) {
    const unsigned v = (tid & 31) == 0 ? 0x3F803F80u : 0u;
    *(LDS_AS u32x4*)(smem + A_ONES + tid * 16) = u32x4{v, v, v, v};
  }

  LDS_AS char* kb = smem + A_KOFF + kh * 32768 + lane * 16;
  const bool real = r32 < 16;
  LDS_AS char* vb = real ? (smem + A_VOFF + kh * 32768 + hh * 256 + r32 * 16) : (smem + A_ONES + (r32 == 16 ? 0 : 16));
  const int vstride = real ? 2048 : 0;

  wait_bar<0>();
  const bf16x8 qf = LDF(smem + A_QOFF + qw * 1024 + lane * 16);
  f32x16 negm;
  float mref;
  {
    f32x16 z = {};
    f32x16 s = MFMA32(LDF(smem + A_KOFF + lane * 16), qf, z);
    mref = xhalf_max(max16(s));
#pragma unroll
    for (int r = 0; r < 16; ++r) negm[r] = -mref;
  }
  f32x16 o = {};
#pragma unroll 1
  for (int pass = 0; pass < 2; ++pass) {
    LDS_AS char* kp = kb;
    LDS_AS char* vp = vb;
    bf16x8 kf = LDF(kp), va0 = LDF(vp), vb0 = LDF(vp + 512), va1, vb1;
    f32x16 sA = MFMA32(kf, qf, negm), sB;
    kf = LDF(kp + 1024);
    SBAR();
#pragma unroll 1
    for (int t = 0; t < 32; t += 2) {
      if (pass == 0) {
        if (t < 6) {
          const int c = (t >> 1) + 1;
          glds16(ks + c * 4096, kd + c * 8192);
          glds16(vs + c * 4096, vd + c * 8192);
        } else if (t == 6) {
          wait_bar<4>();
        } else if (t == 14) {
          wait_bar<0>();
          const int line = j * 1024 + tid;
          const char* pp = line < pf0_lines ? pf0 + (size_t)line * 128 : pf1 + (size_t)(line - pf0_lines) * 128;
          if (line < pf0_lines + pf1_lines) touch(pp, lds0 + A_ONES + 3072);
        }
      }
      ASTEP(sA, sB, va0, vb0, va1, vb1, 2048, 1024);
      ASTEP(sB, sA, va1, vb1, va0, vb0, 3072, 2048);
      kp += 2048; vp += vstride;
    }
    if (__builtin_expect(pass == 1 || !__any(!(o[8] < 1.2676506e30f)), 1)) break;
    float m = -INFINITY;
    f32x16 z = {};
    for (int t = 0; t < 32; ++t) {
      f32x16 sc = MFMA32(LDF(kb + t * 1024), qf, z);
      m = __builtin_fmaxf(m, max16(sc));
    }
    mref = xhalf_max(m);
#pragma unroll
    for (int r = 0; r < 16; ++r) negm[r] = -mref;
    o = z;
  }

  {
    auto rr = __builtin_amdgcn_permlane32_swap(__float_as_uint(o[8]), __float_as_uint(o[8]), false, false);
    o[8] = __uint_as_float(rr[0]);
  }
  LDS_AS float* cb = (LDS_AS float*)(smem + A_COMB + qw * (64 * 40) + lane * 4);
  if (kh) {
#pragma unroll
    for (int r = 0; r < 9; ++r) cb[r * 64] = o[r];
    cb[9 * 64] = mref;
  }
  bar_lgkm();
  if (!kh) {
    const float m2 = cb[9 * 64];
    const float mm = __builtin_fmaxf(mref, m2);
    const float f1 = __builtin_amdgcn_exp2f(mref - mm), f2 = __builtin_amdgcn_exp2f(m2 - mm);
    const float l = o[8] * f1 + cb[8 * 64] * f2;
    const float inv = __builtin_amdgcn_rcpf(l);
    const float a1 = f1 * inv, a2 = f2 * inv;
    float v[8];
#pragma unroll
    for (int r = 0; r < 8; ++r) v[r] = o[r] * a1 + cb[r * 64] * a2;
    bf16_t* op = O + (size_t)(q0 + r32) * DM + head * 16 + 4 * hh;
    *(u32x2*)(op) = pack4(v[0], v[1], v[2], v[3]);
    *(u32x2*)(op + 8) = pack4(v[4], v[5], v[6], v[7]);
  }
  asm volatile("s_waitcnt vmcnt(0)" ::: "memory");
}

constexpr int QKV_NST = 5, WO_NST = 6, F1_NST = 5, F2_NST = 6;
#define QKV0_K gemm_kernel<128, 96, 2, 2, 512, 1536, QKV_NST, 0, 1>
#define QKV1_K gemm_kernel<128, 96, 2, 2, 512, 1536, QKV_NST, 0, 0>
#define WO_K gemm_kernel<64, 64, 2, 2, 512, 512, WO_NST, 1, 0>
#define F1_K gemm_kernel<128, 112, 4, 1, 512, 1792, F1_NST, 2, 0>
#define F2_K gemm_kernel<64, 64, 2, 2, 1792, 512, F2_NST, 3, 0>
constexpr int QKV_LDS = QKV_NST * (128 + 96) * 128 + 2048, WO_LDS = WO_NST * 128 * 128 + 2048,
              F1_LDS = F1_NST * (128 + 112) * 128 + 2048, F2_LDS = F2_NST * 128 * 128 + 2048;

extern "C" void kernel_launch(void* const* d_in, const int* in_sizes, int n_in, void* d_out, int out_size, void* d_ws,
                              size_t ws_size, hipStream_t stream) {
  const bf16_t* x = (const bf16_t*)d_in[0];
  const bf16_t* wqkv = (const bf16_t*)d_in[1];
  const bf16_t* wo = (const bf16_t*)d_in[2];
  const bf16_t* w1 = (const bf16_t*)d_in[4];
  const bf16_t* b1 = (const bf16_t*)d_in[5];
  const bf16_t* w2 = (const bf16_t*)d_in[6];
  const bf16_t* b2 = (const bf16_t*)d_in[7];
  char* ws = (char*)d_ws;
  bf16_t* Qb = (bf16_t*)(ws + (0 << 20));
  bf16_t* Kb = (bf16_t*)(ws + (2 << 20));
  bf16_t* Vb = (bf16_t*)(ws + (4 << 20));
  bf16_t* Ob = (bf16_t*)(ws + (6 << 20));
  bf16_t* X1 = (bf16_t*)(ws + (8 << 20));
  bf16_t* Hb = (bf16_t*)(ws + (10 << 20));
  bf16_t* X2 = (bf16_t*)(ws + (18 << 20));
  float* St1 = (float*)(ws + (20 << 20));
  float* St2 = (float*)(ws + (21 << 20));
  bf16_t* out = (bf16_t*)d_out;

  hipFuncSetAttribute((const void*)QKV0_K, hipFuncAttributeMaxDynamicSharedMemorySize, QKV_LDS);
  hipFuncSetAttribute((const void*)QKV1_K, hipFuncAttributeMaxDynamicSharedMemorySize, QKV_LDS);
  hipFuncSetAttribute((const void*)WO_K, hipFuncAttributeMaxDynamicSharedMemorySize, WO_LDS);
  hipFuncSetAttribute((const void*)F1_K, hipFuncAttributeMaxDynamicSharedMemorySize, F1_LDS);
  hipFuncSetAttribute((const void*)F2_K, hipFuncAttributeMaxDynamicSharedMemorySize, F2_LDS);
  hipFuncSetAttribute((const void*)attn_kernel, hipFuncAttributeMaxDynamicSharedMemorySize, A_LDS);

  for (int layer = 0; layer < 2; ++layer) {
    const bf16_t* xin = layer == 0 ? x : X2;
    bf16_t* xout = layer == 0 ? X2 : out;
    const char* none = (const char*)x;
    if (layer == 0) QKV0_K<<<256, 768, QKV_LDS, stream>>>(xin, wqkv, nullptr, nullptr, Qb, Kb, Vb, nullptr, none, 0);
    else            QKV1_K<<<256, 768, QKV_LDS, stream>>>(xin, wqkv, nullptr, nullptr, Qb, Kb, Vb, St2, none, 0);
    attn_kernel<<<256, 1024, A_LDS, stream>>>(Qb, Kb, Vb, Ob, (const char*)wo, DM * DM * 2 / 128, (const char*)w1, DM * FF * 2 / 128);
    WO_K<<<256, 768, WO_LDS, stream>>>(Ob, wo, nullptr, xin, X1, nullptr, nullptr, St1, none, 0);
    F1_K<<<256, 768, F1_LDS, stream>>>(X1, w1, b1, nullptr, Hb, nullptr, nullptr, St1, (const char*)w2, FF * DM * 2 / 128);
    F2_K<<<256, 768, F2_LDS, stream>>>(Hb, w2, b2, X1, xout, nullptr, nullptr, St2, (const char*)wqkv,
                                       layer == 0 ? DM * 3 * DM * 2 / 128 : 0);
  }
}
```

```cpp
#include <hip/hip_runtime.h>
#include <hip/hip_bf16.h>
#include <stdint.h>

typedef __bf16 bf16_t;
typedef __bf16 bf16x2 __attribute__((ext_vector_type(2)));
typedef __bf16 bf16x4 __attribute__((ext_vector_type(4)));
typedef __bf16 bf16x8 __attribute__((ext_vector_type(8)));
typedef float f32x2 __attribute__((ext_vector_type(2)));
typedef float f32x4 __attribute__((ext_vector_type(4)));
typedef float f32x16 __attribute__((ext_vector_type(16)));
typedef unsigned u32x2 __attribute__((ext_vector_type(2)));
typedef unsigned u32x4 __attribute__((ext_vector_type(4)));

#define LDS_AS __attribute__((address_space(3)))
#define GLB_AS __attribute__((address_space(1)))

constexpr int SEQ = 2048, DM = 512, NH = 32, DH = 16, FF = 1792;

__device__ __forceinline__ void glds16(const void* g, unsigned lds_base) {
  unsigned keep;
  asm volatile("s_mov_b32 %0, m0\n\ts_mov_b32 m0, %2\n\ts_nop 0\n\tglobal_load_lds_dwordx4 %1, off\n\ts_mov_b32 m0, %0"
               : "=&s"(keep) : "v"(g), "s"(lds_base) : "memory");
}
template <int IMM>
__device__ __forceinline__ void glds16s(unsigned voff, const void* sbase, unsigned lds_base) {
  asm volatile("s_add_i32 m0, %2, %3\n\ts_nop 0\n\tglobal_load_lds_dwordx4 %0, %1"
               :: "v"(voff), "s"(sbase), "s"(lds_base), "i"(IMM) : "memory", "scc");
}
__device__ __forceinline__ void touch(const void* p, unsigned lds_dummy) {
  unsigned keep;
  asm volatile("s_mov_b32 %0, m0\n\ts_mov_b32 m0, %2\n\ts_nop 0\n\tglobal_load_lds_dword %1, off\n\ts_mov_b32 m0, %0"
               : "=&s"(keep) : "v"(p), "s"(lds_dummy) : "memory");
}
#define ST8(ptr, val) (*(u32x2*)(ptr) = (val))
#define SBAR() __builtin_amdgcn_sched_barrier(0)
#define PIN(x) asm volatile("" : "+v"(x))
__device__ __forceinline__ float rbf(float v) { return (float)(__bf16)v; }
__device__ __forceinline__ u32x2 pack4(float a, float b, float c, float d) {
  f32x2 lo = {a, b}, hi = {c, d};
  bf16x2 l = __builtin_convertvector(lo, bf16x2), h = __builtin_convertvector(hi, bf16x2);
  u32x2 r = {__builtin_bit_cast(unsigned, l), __builtin_bit_cast(unsigned, h)};
  return r;
}
__device__ __forceinline__ float bf_lo(unsigned u) { return __builtin_bit_cast(float, u << 16); }
__device__ __forceinline__ float bf_hi(unsigned u) { return __builtin_bit_cast(float, u & 0xffff0000u); }

#define GST(i)
#define GST_DECL
#define GST_FLUSH
template <int N>
__device__ __forceinline__ void wait_bar() {
  asm volatile("s_waitcnt vmcnt(%0) lgkmcnt(0)\n\ts_barrier" ::"i"(N) : "memory");
}

template <int I> struct IC { static constexpr int value = I; };
template <int I, int N, class F>
__device__ __forceinline__ void static_for(F& f) {
  if constexpr (I < N) { f(IC<I>{}); static_for<I + 1, N>(f); }
}

__device__ __forceinline__ float gelu_tanh(float x) {
  const float c1 = -2.0f * 0.7978845608028654f * 1.4426950408889634f;
  const float c2 = c1 * 0.044715f;
  float t = x * __builtin_fmaf(x * x, c2, c1);
  float e = __builtin_amdgcn_exp2f(t);
  return x * __builtin_amdgcn_rcpf(1.0f + e);
}

template <int N>
__device__ __forceinline__ void wait_vm() { asm volatile("s_waitcnt vmcnt(%0)" ::"i"(N) : "memory"); }
__device__ __forceinline__ void bar_lgkm() { asm volatile("s_waitcnt lgkmcnt(0)\n\ts_barrier" ::: "memory"); }
#define MFMA16(a, b, c) __builtin_amdgcn_mfma_f32_16x16x32_bf16(a, b, c, 0, 0, 0)
__device__ __forceinline__ float sum_g(float v) {
  auto a = __builtin_amdgcn_permlane16_swap(__float_as_uint(v), __float_as_uint(v), false, false);
  v = __uint_as_float(a[0]) + __uint_as_float(a[1]);
  auto b = __builtin_amdgcn_permlane32_swap(__float_as_uint(v), __float_as_uint(v), false, false);
  return __uint_as_float(b[0]) + __uint_as_float(b[1]);
}

template <int BM, int BN, int WVM, int WVN, int K, int LDW, int NST, int MODE, int SELF>
__global__ __launch_bounds__(768, 3) void gemm_kernel(const bf16_t* __restrict__ X, const bf16_t* __restrict__ W,
                                                      const bf16_t* __restrict__ bias, const bf16_t* __restrict__ res,
                                                      bf16_t* __restrict__ out0, bf16_t* __restrict__ out1,
                                                      bf16_t* __restrict__ out2, float* __restrict__ stats,
                                                      const char* __restrict__ pf, int pf_lines) {
  extern __shared__ __attribute__((aligned(16))) char smem_g[];
  LDS_AS char* smem = (LDS_AS char*)smem_g;
  constexpr int TM = BM / WVM / 16, TN = BN / WVN / 16, NB = BN / 16, HM = TM / 2;
  constexpr int XB = BM * 128, WB = BN * 128, STB = XB + WB;
  constexpr int NXI = BM / 8, NI = (BM + BN) / 8, LPL = (NI + 3) / 4, NK = K / 64;
  constexpr int NFULL = NI - 4 * (LPL - 1);
#define DEP_BIG 2
#define DEP_SMALL 3
  constexpr int DEP = (BM == 128) ? DEP_BIG : DEP_SMALL;
  static_assert(NST >= DEP + 1, "ring too small");
  constexpr bool LN = (MODE == 0 || MODE == 2);
  constexpr int PF_AT = (NK - 4);
  constexpr int NX4 = HM * TN;
  constexpr int EX_OFF = NST * STB;
  constexpr int CS_OFF = EX_OFF;
  constexpr int ST_OFF = CS_OFF + 512;
  constexpr int PF_OFF = ST_OFF + 1024;
  constexpr int BI_OFF = PF_OFF + 256;
  static_assert(8 * NX4 * 1024 <= ((NK - 1) % NST) * STB, "exchange area overlaps the last stage");

  const int tid = threadIdx.x, lane = tid & 63;
  const int wave = __builtin_amdgcn_readfirstlane(tid >> 6);
  const int b = blockIdx.x, xcd = b & 7, j = b >> 3;
  int mt, nt;
  if (BM == 128) { mt = xcd * 2 + (j & 1); nt = j >> 1; }
  else           { mt = xcd * 4 + (j & 3); nt = j >> 2; }
  GST_DECL; GST(0);
  const int m0 = mt * BM;
  const int l15 = lane & 15, g = lane >> 4;
  const int q_ = (lane & 15) >> 2, p_ = lane & 3;
  const __bf16 one = (__bf16)1.0f;
  const bf16x8 ones8 = {one, one, one, one, one, one, one, one};

  auto colbase = [&](int nb) -> int {
    if (MODE == 0) return (nb >> 1) * 512 + (nt * 2 + (nb & 1)) * 16;
    return nt * BN + nb * 16;
  };
  auto tr8 = [&](LDS_AS char* p0, LDS_AS char* p1) -> bf16x8 {
    bf16x4 a = __builtin_amdgcn_ds_read_tr16_b64_v4bf16((LDS_AS bf16x4*)p0);
    bf16x4 c = __builtin_amdgcn_ds_read_tr16_b64_v4bf16((LDS_AS bf16x4*)p1);
    return __builtin_shufflevector(a, c, 0, 1, 2, 3, 4, 5, 6, 7);
  };

  if (wave >= 8) {
    const int lw = wave - 8;
    constexpr int XS = NXI / 4;
    unsigned voff[LPL];
    {
      const int row = 8 * lw + (lane >> 3);
      voff[0] = (unsigned)(row * K + (((lane & 7) ^ ((row >> 1) & 7)) * 8)) * 2u;
#pragma unroll
      for (int i = 1; i < XS; ++i) voff[i] = voff[0] + i * 32 * K * 2;
#pragma unroll
      for (int i = XS; i < LPL; ++i) {
        const int sidx = 4 * (lw + 4 * (i - XS)) + (lane >> 4);
        const int kb = sidx / NB, nb = sidx % NB;
        const int krow = kb * 8 + (((lane >> 1) & 7) ^ ((kb & 1) << 2));
        voff[i] = (unsigned)(krow * LDW + colbase(nb) + (lane & 1) * 8) * 2u;
      }
    }
    const char* xs_ = (const char*)(X + (size_t)m0 * K);
    const char* ws_ = (const char*)W;
    const unsigned lds0 = (unsigned)(uintptr_t)smem + lw * 1024;
    const bool full = lw < NFULL;
    auto issue = [&](auto ST) {
      constexpr int st = decltype(ST)::value;
      auto one_ = [&](auto I) {
        constexpr int i = decltype(I)::value;
        if (i < LPL - 1 || NFULL == 4 || full)
          glds16s<(st % NST) * STB + i * 4096>(voff[i], i < XS ? xs_ + st * 128 : ws_ + (size_t)st * (64 * LDW * 2), lds0);
      };
      static_for<0, LPL>(one_);
    };
    static_for<0, (DEP < NK ? DEP : NK)>(issue);

    const int cnb0 = lw, cnb1 = (lw + 4 < NB) ? lw + 4 : NB - 1;
    const int wo0 = XB + g * NB * 256 + (q_ + 4 * (g & 1)) * 32 + p_ * 8;
    const int wo1 = XB + g * NB * 256 + (q_ + 4 * (1 - (g & 1))) * 32 + p_ * 8;
    const int xo = (lw * 32 + l15) * 128 + ((g ^ (l15 >> 1)) * 16);
    f32x4 cs0 = {0.f, 0.f, 0.f, 0.f}, cs1 = {0.f, 0.f, 0.f, 0.f};
    f32x4 rs[2], rq[2];
    rs[0] = rs[1] = rq[0] = rq[1] = f32x4{0.f, 0.f, 0.f, 0.f};
    auto lstep = [&](auto KT) {
      constexpr int kt = decltype(KT)::value;
      constexpr int last = (kt + DEP - 1 < NK - 1) ? kt + DEP - 1 : NK - 1;
      if (NFULL == 4 || full) wait_vm<(last - kt) * LPL>(); else wait_vm<(last - kt) * (LPL - 1)>();
      bar_lgkm();
      if constexpr (kt + DEP < NK) issue(IC<kt + DEP>{});
      if (LN) {
        LDS_AS char* base = smem + (kt % NST) * STB;
#pragma unroll
        for (int ks = 0; ks < 2; ++ks) {
          const int o0 = (ks * 4 * NB + cnb0) * 256, o1 = (ks * 4 * NB + cnb1) * 256;
          cs0 = MFMA16(tr8(base + wo0 + o0, base + wo1 + o0), ones8, cs0);
          cs1 = MFMA16(tr8(base + wo0 + o1, base + wo1 + o1), ones8, cs1);
          if (SELF) {
#pragma unroll
            for (int mi = 0; mi < 2; ++mi) {
              const bf16x8 xf = *(const LDS_AS bf16x8*)(base + ((xo ^ (ks * 64)) + mi * 2048));
              rs[mi] = MFMA16(ones8, xf, rs[mi]);
              rq[mi] = MFMA16(xf, xf, rq[mi]);
            }
          }
        }
      }
    };
    static_for<0, NK>(lstep);
    if (LN) {
      if (l15 == 0) {
        *(LDS_AS f32x4*)(smem + CS_OFF + cnb0 * 64 + g * 16) = cs0;
        *(LDS_AS f32x4*)(smem + CS_OFF + cnb1 * 64 + g * 16) = cs1;
      }
      if (SELF) {
        if (g == (l15 >> 2)) {
          const int r = l15 & 3;
#pragma unroll
          for (int mi = 0; mi < 2; ++mi) {
            const float sq = r == 0 ? rq[mi][0] : r == 1 ? rq[mi][1] : r == 2 ? rq[mi][2] : rq[mi][3];
            const float m = rs[mi][0] * (1.0f / K);
            const float v = __builtin_fmaxf(sq * (1.0f / K) - m * m, 0.f);
            *(LDS_AS f32x2*)(smem + ST_OFF + (lw * 32 + mi * 16 + l15) * 8) = f32x2{m, __builtin_amdgcn_rsqf(v + 1e-5f)};
          }
        }
      }
    }
    bar_lgkm();
    return;
  }

  const int kg = wave >> 2, w4 = wave & 3;
  const int wm = w4 / WVN, wn = w4 % WVN;
  auto nb_of = [&](int ni) -> int {
    if (MODE == 0) return 2 * ni + wn;
    return wn * TN + ni;
  };
  const int tl0 = wm * TM * 16 + kg * HM * 16;
  const int tok0 = m0 + tl0;

  if (LN && !SELF) {
    if (wave < 2) {
      float sm = 0.f, sq = 0.f;
#pragma unroll
      for (int i = 0; i < 8; ++i) {
        const f32x4 t = *(const f32x4*)(stats + (size_t)(m0 + tid) * 32 + i * 4);
        sm += t[0] + t[2]; sq += t[1] + t[3];
      }
      const float m = sm * (1.0f / K);
      const float v = __builtin_fmaxf(sq * (1.0f / K) - m * m, 0.f);
      *(LDS_AS f32x2*)(smem + ST_OFF + tid * 8) = f32x2{m, __builtin_amdgcn_rsqf(v + 1e-5f)};
    }
  }
  if (MODE == 2) {
    if (wave == 2 && lane < BN / 4) *(LDS_AS u32x2*)(smem + BI_OFF + lane * 8) = *(const u32x2*)(bias + nt * BN + lane * 4);
  }
  u32x2 resv[HM][TN];
  u32x2 biasv[TN];
  if (MODE == 1 || MODE == 3) {
#pragma unroll
    for (int mi = 0; mi < HM; ++mi)
#pragma unroll
      for (int ni = 0; ni < TN; ++ni)
        resv[mi][ni] = *(const u32x2*)(res + (size_t)(tok0 + mi * 16 + l15) * DM + colbase(nb_of(ni)) + 4 * g);
  }
  if (MODE == 3) {
#pragma unroll
    for (int ni = 0; ni < TN; ++ni) biasv[ni] = *(const u32x2*)(bias + colbase(nb_of(ni)) + 4 * g);
  }

  const int xo_ = ((wm * TM * 16 + l15) * 128 + ((g ^ (l15 >> 1)) * 16)) ^ (kg * 64);
  const int xoA = xo_ + kg * HM * 2048, xoB = xo_ + (1 - kg) * HM * 2048;
  const int wo0 = XB + (kg * 4 + g) * NB * 256 + (q_ + 4 * (g & 1)) * 32 + p_ * 8 + nb_of(0) * 256;
  const int wo1 = XB + (kg * 4 + g) * NB * 256 + (q_ + 4 * (1 - (g & 1))) * 32 + p_ * 8 + nb_of(0) * 256;
  constexpr int NSTEP = (MODE == 0) ? 512 : 256;

  f32x4 acc[TM][TN];
#pragma unroll
  for (int mi = 0; mi < TM; ++mi)
#pragma unroll
    for (int ni = 0; ni < TN; ++ni) acc[mi][ni] = f32x4{0.f, 0.f, 0.f, 0.f};

  bf16x8 xf[2][TM], wf[2][TN];
  auto ldfrags = [&](auto KT) {
    constexpr int kt = decltype(KT)::value, p = kt & 1;
    LDS_AS char* base = smem + (kt % NST) * STB;
#pragma unroll
    for (int mi = 0; mi < TM; ++mi) xf[p][mi] = *(const LDS_AS bf16x8*)(base + (mi < HM ? xoA + mi * 2048 : xoB + (mi - HM) * 2048));
#pragma unroll
    for (int ni = 0; ni < TN; ++ni) wf[p][ni] = tr8(base + wo0 + ni * NSTEP, base + wo1 + ni * NSTEP);
  };
  GST(1);
  bar_lgkm();
  GST(2);
  ldfrags(IC<0>{});
  auto kstep = [&](auto KT) {
    constexpr int kt = decltype(KT)::value, p = kt & 1;
    if constexpr (kt + 1 < NK) {
      bar_lgkm();
      ldfrags(IC<kt + 1>{});
    }
    if constexpr (kt == PF_AT) {
      const int line = j * 512 + tid;
      if (line < pf_lines) touch(pf + (size_t)line * 128, (unsigned)(uintptr_t)smem + PF_OFF);
    }
    SBAR();
#pragma unroll
    for (int mj = 0; mj < TM; ++mj)
#pragma unroll
      for (int ni = 0; ni < TN; ++ni) {
        const int mi = (mj + HM) % TM;
        if (MODE == 0 && ni == 2) acc[mi][ni] = MFMA16(xf[p][mi], wf[p][ni], acc[mi][ni]);
        else                      acc[mi][ni] = MFMA16(wf[p][ni], xf[p][mi], acc[mi][ni]);
      }
    SBAR();
  };
  static_for<0, NK>(kstep);

  GST(3);
  {
    LDS_AS f32x4* wr = (LDS_AS f32x4*)(smem + wave * (NX4 * 1024) + lane * 16);
#pragma unroll
    for (int mi = 0; mi < HM; ++mi)
#pragma unroll
      for (int ni = 0; ni < TN; ++ni) wr[(mi * TN + ni) * 64] = acc[HM + mi][ni];
  }
  GST(4);
  bar_lgkm();
  GST(5);
  f32x4 fa[HM][TN];
  {
    const LDS_AS f32x4* rd = (const LDS_AS f32x4*)(smem + (wave ^ 4) * (NX4 * 1024) + lane * 16);
#pragma unroll
    for (int mi = 0; mi < HM; ++mi)
#pragma unroll
      for (int ni = 0; ni < TN; ++ni) fa[mi][ni] = acc[mi][ni] + rd[(mi * TN + ni) * 64];
  }
  auto tok_stats = [&](int tl) -> f32x2 { return *(const LDS_AS f32x2*)(smem + ST_OFF + tl * 8); };
  auto col_sum4 = [&](int nb) -> f32x4 { return *(const LDS_AS f32x4*)(smem + CS_OFF + nb * 64 + g * 16); };

  if (MODE == 0) {
    const int head = nt * 2 + wn;
    const float qscale = 0.25f * 1.4426950408889634f;
    const f32x4 cq = col_sum4(nb_of(0)), ck = col_sum4(nb_of(1));
    const float cv = *(const LDS_AS float*)(smem + CS_OFF + nb_of(2) * 64 + l15 * 4);
#pragma unroll
    for (int mi = 0; mi < HM; ++mi) {
      const int tok = tok0 + mi * 16 + l15;
      const f32x2 st = tok_stats(tl0 + mi * 16 + l15);
      {
        const float sc = st[1] * qscale;
        float v[4];
#pragma unroll
        for (int r = 0; r < 4; ++r) v[r] = sc * __builtin_fmaf(-st[0], cq[r], fa[mi][0][r]);
        ST8(out0 + ((size_t)head * SEQ + tok) * 16 + 4 * g, pack4(v[0], v[1], v[2], v[3]));
      }
      {
        float v[4];
#pragma unroll
        for (int r = 0; r < 4; ++r) v[r] = st[1] * __builtin_fmaf(-st[0], ck[r], fa[mi][1][r]);
        ST8(out1 + (size_t)head * 32768 + (tok >> 5) * 512 + (g >> 1) * 256 + (tok & 31) * 8 + (g & 1) * 4,
            pack4(v[0], v[1], v[2], v[3]));
      }
      {
        float v[4];
#pragma unroll
        for (int r = 0; r < 4; ++r) {
          const f32x2 sr = tok_stats(tl0 + mi * 16 + 4 * g + r);
          v[r] = sr[1] * __builtin_fmaf(-sr[0], cv, fa[mi][2][r]);
        }
        const int ks16 = (tok0 + mi * 16) >> 4;
        ST8(out2 + (size_t)head * 32768 + ks16 * 256 + (g & 1) * 128 + l15 * 8 + (g >> 1) * 4,
            pack4(v[0], v[1], v[2], v[3]));
      }
    }
  } else if (MODE == 2) {
#pragma unroll
    for (int mi = 0; mi < HM; ++mi) {
      const int tok = tok0 + mi * 16 + l15;
      const f32x2 st = tok_stats(tl0 + mi * 16 + l15);
      const float nrm = -st[0] * st[1];
#pragma unroll
      for (int ni = 0; ni < TN; ++ni) {
        const f32x4 cc = col_sum4(nb_of(ni));
        const u32x2 bv = *(const LDS_AS u32x2*)(smem + BI_OFF + ni * 32 + g * 8);
        const float bb[4] = {bf_lo(bv[0]), bf_hi(bv[0]), bf_lo(bv[1]), bf_hi(bv[1])};
        float v[4];
#pragma unroll
        for (int r = 0; r < 4; ++r)
          v[r] = gelu_tanh(__builtin_fmaf(st[1], fa[mi][ni][r], __builtin_fmaf(nrm, cc[r], bb[r])));
        ST8(out0 + (size_t)tok * FF + colbase(nb_of(ni)) + 4 * g, pack4(v[0], v[1], v[2], v[3]));
      }
    }
  } else {
#pragma unroll
    for (int mi = 0; mi < HM; ++mi) {
      const int tok = tok0 + mi * 16 + l15;
      float sm = 0.f, sq = 0.f;
#pragma unroll
      for (int ni = 0; ni < TN; ++ni) {
        float rr[4] = {bf_lo(resv[mi][ni][0]), bf_hi(resv[mi][ni][0]), bf_lo(resv[mi][ni][1]), bf_hi(resv[mi][ni][1])};
        if (MODE == 3) {
          rr[0] += bf_lo(biasv[ni][0]); rr[1] += bf_hi(biasv[ni][0]); rr[2] += bf_lo(biasv[ni][1]); rr[3] += bf_hi(biasv[ni][1]);
        }
        const u32x2 pk = pack4(rr[0] + fa[mi][ni][0], rr[1] + fa[mi][ni][1], rr[2] + fa[mi][ni][2], rr[3] + fa[mi][ni][3]);
        ST8(out0 + (size_t)tok * DM + colbase(nb_of(ni)) + 4 * g, pk);
        const float w[4] = {bf_lo(pk[0]), bf_hi(pk[0]), bf_lo(pk[1]), bf_hi(pk[1])};
#pragma unroll
        for (int r = 0; r < 4; ++r) { sm += w[r]; sq = __builtin_fmaf(w[r], w[r], sq); }
      }
      sm = sum_g(sm); sq = sum_g(sq);
      if (g == 0) *(f32x2*)(stats + (size_t)tok * 32 + (nt * 2 + wn) * 2) = f32x2{sm, sq};
    }
  }
  GST(6);
  asm volatile("s_waitcnt vmcnt(0)" ::: "memory");
  GST(7);
  GST_FLUSH;
}

constexpr int A_KOFF = 0, A_VOFF = 65536, A_ONES = 131072, A_QOFF = 135168, A_COMB = 143360, A_LDS = 143360 + 8 * 64 * 40;

#define MFMA32(a, b, c) __builtin_amdgcn_mfma_f32_32x32x16_bf16(a, b, c, 0, 0, 0)
__device__ __forceinline__ bf16x8 exp_pack8(const f32x16& s, int o) {
  bf16x8 p;
#pragma unroll
  for (int r = 0; r < 8; r += 2) {
    f32x2 e = {__builtin_amdgcn_exp2f(s[o + r]), __builtin_amdgcn_exp2f(s[o + r + 1])};
    bf16x2 c = __builtin_convertvector(e, bf16x2);
    p[r] = c[0]; p[r + 1] = c[1];
  }
  return p;
}
#define LDF(p) (*(const LDS_AS bf16x8*)(p))
#define ASTEP(SC, SN, VA, VB, VNA, VNB, KO, VO)                              \
  do {                                                                       \
    SN = MFMA32(kf, qf, negm); SBAR();                                       \
    kf = LDF(kp + (KO)); VNA = LDF(vp + (VO)); VNB = LDF(vp + (VO) + 512); SBAR(); \
    { bf16x8 pa = exp_pack8(SC, 0); SBAR(); o = MFMA32(VA, pa, o); SBAR(); } \
    { bf16x8 pb = exp_pack8(SC, 8); SBAR(); o = MFMA32(VB, pb, o); SBAR(); } \
  } while (0)

__device__ __forceinline__ float max16(const f32x16 s) {
  float a = __builtin_fmaxf(__builtin_fmaxf(s[0], s[1]), s[2]);
  float c = __builtin_fmaxf(__builtin_fmaxf(s[3], s[4]), s[5]);
  a = __builtin_fmaxf(__builtin_fmaxf(a, s[6]), s[7]);
  c = __builtin_fmaxf(__builtin_fmaxf(c, s[8]), s[9]);
  a = __builtin_fmaxf(__builtin_fmaxf(a, s[10]), s[11]);
  c = __builtin_fmaxf(__builtin_fmaxf(c, s[12]), s[13]);
  a = __builtin_fmaxf(__builtin_fmaxf(a, s[14]), s[15]);
  return __builtin_fmaxf(a, c);
}
__device__ __forceinline__ float xhalf_max(float m) {
  auto rr = __builtin_amdgcn_permlane32_swap(__float_as_uint(m), __float_as_uint(m), false, false);
  return __builtin_fmaxf(__uint_as_float(rr[0]), __uint_as_float(rr[1]));
}

__global__ __launch_bounds__(1024, 4) void attn_kernel(const bf16_t* __restrict__ Q, const bf16_t* __restrict__ Kf,
                                                       const bf16_t* __restrict__ Vf, bf16_t* __restrict__ O,
                                                       const char* __restrict__ pf0, int pf0_lines,
                                                       const char* __restrict__ pf1, int pf1_lines) {
  extern __shared__ __attribute__((aligned(16))) char smem_g[];
  LDS_AS char* smem = (LDS_AS char*)smem_g;
  const int tid = threadIdx.x, lane = tid & 63;
  const int wave = __builtin_amdgcn_readfirstlane(tid >> 6);
  const int kh = wave >> 3, qw = wave & 7;
  const int b = blockIdx.x, xcd = b & 7, j = b >> 3;
  const int head = xcd * 4 + (j & 3), qt = j >> 2;
  const int r32 = lane & 31, hh = lane >> 5;
  const int q0 = qt * 256 + qw * 32;

  const unsigned lds0 = (unsigned)(uintptr_t)smem;
    const bf16_t* ks = Kf + (size_t)head * 32768 + kh * 16384 + qw * 512 + lane * 8;
    const bf16_t* vs = Vf + (size_t)head * 32768 + kh * 16384 + qw * 512 + lane * 8;
    const unsigned kd = lds0 + A_KOFF + kh * 32768 + qw * 1024, vd = lds0 + A_VOFF + kh * 32768 + qw * 1024;
    glds16(Q + ((size_t)head * SEQ + q0 + r32) * 16 + hh * 8, lds0 + A_QOFF + qw * 1024);
    glds16(ks, kd);
    glds16(vs, vd);

  if (tid < 256) {
    const unsigned v = (tid & 31) == 0 ? 0x3F803F80u : 0u;
    *(LDS_AS u32x4*)(smem + A_ONES + tid * 16) = u32x4{v, v, v, v};
  }

  LDS_AS char* kb = smem + A_KOFF + kh * 32768 + lane * 16;
  const bool real = r32 < 16;
  LDS_AS char* vb = real ? (smem + A_VOFF + kh * 32768 + hh * 256 + r32 * 16) : (smem + A_ONES + (r32 == 16 ? 0 : 16));
  const int vstride = real ? 2048 : 0;

  wait_bar<0>();
  const bf16x8 qf = LDF(smem + A_QOFF + qw * 1024 + lane * 16);
  f32x16 negm;
  float mref;
  {
    f32x16 z = {};
    f32x16 s = MFMA32(LDF(smem + A_KOFF + lane * 16), qf, z);
    mref = xhalf_max(max16(s));
#pragma unroll
    for (int r = 0; r < 16; ++r) negm[r] = -mref;
  }
  f32x16 o = {};
#pragma unroll 1
  for (int pass = 0; pass < 2; ++pass) {
    LDS_AS char* kp = kb;
    LDS_AS char* vp = vb;
    bf16x8 kf = LDF(kp), va0 = LDF(vp), vb0 = LDF(vp + 512), va1, vb1;
    f32x16 sA = MFMA32(kf, qf, negm), sB;
    kf = LDF(kp + 1024);
    SBAR();
#pragma unroll 1
    for (int t = 0; t < 32; t += 2) {
      if (pass == 0) {
        if (t < 6) {
          if ((wave >> 2) == (t >> 1)) {
            const int c = (t >> 1) + 1, w2 = (wave & 3) * 2;
            const bf16_t* k2 = Kf + (size_t)head * 32768 + c * 4096 + w2 * 512 + lane * 8;
            const bf16_t* v2 = Vf + (size_t)head * 32768 + c * 4096 + w2 * 512 + lane * 8;
            const unsigned d2 = lds0 + c * 8192 + w2 * 1024;
#pragma unroll
            for (int hf = 0; hf < 2; ++hf)
#pragma unroll
              for (int i = 0; i < 2; ++i) {
                glds16(k2 + hf * 16384 + i * 512, d2 + A_KOFF + hf * 32768 + i * 1024);
                glds16(v2 + hf * 16384 + i * 512, d2 + A_VOFF + hf * 32768 + i * 1024);
              }
          }
        } else if (t == 6) {
          wait_bar<0>();
        } else if (t == 8) {
          const int line = j * 1024 + tid;
          const char* pp = line < pf0_lines ? pf0 + (size_t)line * 128 : pf1 + (size_t)(line - pf0_lines) * 128;
          if (line < pf0_lines + pf1_lines) touch(pp, lds0 + A_ONES + 3072);
        }
      }
#define ATT_PRIO 1
      {
        const int pr = ((t >> ATT_PRIO) + (wave >> 2)) & 3;
        if (pr == 0) __builtin_amdgcn_s_setprio(0);
        else if (pr == 1) __builtin_amdgcn_s_setprio(1);
        else if (pr == 2) __builtin_amdgcn_s_setprio(2);
        else __builtin_amdgcn_s_setprio(3);
      }
      ASTEP(sA, sB, va0, vb0, va1, vb1, 2048, 1024);
      ASTEP(sB, sA, va1, vb1, va0, vb0, 3072, 2048);
      kp += 2048; vp += vstride;
    }
    if (__builtin_expect(pass == 1 || !__any(!(o[8] < 1.2676506e30f)), 1)) break;
    float m = -INFINITY;
    f32x16 z = {};
    for (int t = 0; t < 32; ++t) {
      f32x16 sc = MFMA32(LDF(kb + t * 1024), qf, z);
      m = __builtin_fmaxf(m, max16(sc));
    }
    mref = xhalf_max(m);
#pragma unroll
    for (int r = 0; r < 16; ++r) negm[r] = -mref;
    o = z;
  }

  {
    auto rr = __builtin_amdgcn_permlane32_swap(__float_as_uint(o[8]), __float_as_uint(o[8]), false, false);
    o[8] = __uint_as_float(rr[0]);
  }
  LDS_AS float* cb = (LDS_AS float*)(smem + A_COMB + qw * (64 * 40) + lane * 4);
  if (kh) {
#pragma unroll
    for (int r = 0; r < 9; ++r) cb[r * 64] = o[r];
    cb[9 * 64] = mref;
  }
  bar_lgkm();
  if (!kh) {
    const float m2 = cb[9 * 64];
    const float mm = __builtin_fmaxf(mref, m2);
    const float f1 = __builtin_amdgcn_exp2f(mref - mm), f2 = __builtin_amdgcn_exp2f(m2 - mm);
    const float l = o[8] * f1 + cb[8 * 64] * f2;
    const float inv = __builtin_amdgcn_rcpf(l);
    const float a1 = f1 * inv, a2 = f2 * inv;
    float v[8];
#pragma unroll
    for (int r = 0; r < 8; ++r) v[r] = o[r] * a1 + cb[r * 64] * a2;
    bf16_t* op = O + (size_t)(q0 + r32) * DM + head * 16 + 4 * hh;
    *(u32x2*)(op) = pack4(v[0], v[1], v[2], v[3]);
    *(u32x2*)(op + 8) = pack4(v[4], v[5], v[6], v[7]);
  }
  asm volatile("s_waitcnt vmcnt(0)" ::: "memory");
}

constexpr int QKV_NST = 5, WO_NST = 6, F1_NST = 5, F2_NST = 6;
#define QKV0_K gemm_kernel<128, 96, 2, 2, 512, 1536, QKV_NST, 0, 1>
#define QKV1_K gemm_kernel<128, 96, 2, 2, 512, 1536, QKV_NST, 0, 0>
#define WO_K gemm_kernel<64, 64, 2, 2, 512, 512, WO_NST, 1, 0>
#define F1_K gemm_kernel<128, 112, 4, 1, 512, 1792, F1_NST, 2, 0>
#define F2_K gemm_kernel<64, 64, 2, 2, 1792, 512, F2_NST, 3, 0>
constexpr int QKV_LDS = QKV_NST * (128 + 96) * 128 + 2048, WO_LDS = WO_NST * 128 * 128 + 2048,
              F1_LDS = F1_NST * (128 + 112) * 128 + 2048, F2_LDS = F2_NST * 128 * 128 + 2048;

extern "C" void kernel_launch(void* const* d_in, const int* in_sizes, int n_in, void* d_out, int out_size, void* d_ws,
                              size_t ws_size, hipStream_t stream) {
  const bf16_t* x = (const bf16_t*)d_in[0];
  const bf16_t* wqkv = (const bf16_t*)d_in[1];
  const bf16_t* wo = (const bf16_t*)d_in[2];
  const bf16_t* w1 = (const bf16_t*)d_in[4];
  const bf16_t* b1 = (const bf16_t*)d_in[5];
  const bf16_t* w2 = (const bf16_t*)d_in[6];
  const bf16_t* b2 = (const bf16_t*)d_in[7];
  char* ws = (char*)d_ws;
  bf16_t* Qb = (bf16_t*)(ws + (0 << 20));
  bf16_t* Kb = (bf16_t*)(ws + (2 << 20));
  bf16_t* Vb = (bf16_t*)(ws + (4 << 20));
  bf16_t* Ob = (bf16_t*)(ws + (6 << 20));
  bf16_t* X1 = (bf16_t*)(ws + (8 << 20));
  bf16_t* Hb = (bf16_t*)(ws + (10 << 20));
  bf16_t* X2 = (bf16_t*)(ws + (18 << 20));
  float* St1 = (float*)(ws + (20 << 20));
  float* St2 = (float*)(ws + (21 << 20));
  bf16_t* out = (bf16_t*)d_out;

  hipFuncSetAttribute((const void*)QKV0_K, hipFuncAttributeMaxDynamicSharedMemorySize, QKV_LDS);
  hipFuncSetAttribute((const void*)QKV1_K, hipFuncAttributeMaxDynamicSharedMemorySize, QKV_LDS);
  hipFuncSetAttribute((const void*)WO_K, hipFuncAttributeMaxDynamicSharedMemorySize, WO_LDS);
  hipFuncSetAttribute((const void*)F1_K, hipFuncAttributeMaxDynamicSharedMemorySize, F1_LDS);
  hipFuncSetAttribute((const void*)F2_K, hipFuncAttributeMaxDynamicSharedMemorySize, F2_LDS);
  hipFuncSetAttribute((const void*)attn_kernel, hipFuncAttributeMaxDynamicSharedMemorySize, A_LDS);

  for (int layer = 0; layer < 2; ++layer) {
    const bf16_t* xin = layer == 0 ? x : X2;
    bf16_t* xout = layer == 0 ? X2 : out;
    const char* none = (const char*)x;
    if (layer == 0) QKV0_K<<<256, 768, QKV_LDS, stream>>>(xin, wqkv, nullptr, nullptr, Qb, Kb, Vb, nullptr, none, 0);
    else            QKV1_K<<<256, 768, QKV_LDS, stream>>>(xin, wqkv, nullptr, nullptr, Qb, Kb, Vb, St2, none, 0);
    attn_kernel<<<256, 1024, A_LDS, stream>>>(Qb, Kb, Vb, Ob, (const char*)wo, DM * DM * 2 / 128, (const char*)w1, DM * FF * 2 / 128);
    WO_K<<<256, 768, WO_LDS, stream>>>(Ob, wo, nullptr, xin, X1, nullptr, nullptr, St1, none, 0);
    F1_K<<<256, 768, F1_LDS, stream>>>(X1, w1, b1, nullptr, Hb, nullptr, nullptr, St1, (const char*)w2, FF * DM * 2 / 128);
    F2_K<<<256, 768, F2_LDS, stream>>>(Hb, w2, b2, X1, xout, nullptr, nullptr, St2, (const char*)wqkv,
                                       layer == 0 ? DM * 3 * DM * 2 / 128 : 0);
  }
}
```

```cpp
#include <hip/hip_runtime.h>
#include <hip/hip_bf16.h>
#include <stdint.h>

typedef __bf16 bf16_t;
typedef __bf16 bf16x2 __attribute__((ext_vector_type(2)));
typedef __bf16 bf16x4 __attribute__((ext_vector_type(4)));
typedef __bf16 bf16x8 __attribute__((ext_vector_type(8)));
typedef float f32x2 __attribute__((ext_vector_type(2)));
typedef float f32x4 __attribute__((ext_vector_type(4)));
typedef float f32x16 __attribute__((ext_vector_type(16)));
typedef unsigned u32x2 __attribute__((ext_vector_type(2)));
typedef unsigned u32x4 __attribute__((ext_vector_type(4)));

#define LDS_AS __attribute__((address_space(3)))
#define GLB_AS __attribute__((address_space(1)))

constexpr int SEQ = 2048, DM = 512, NH = 32, DH = 16, FF = 1792;

__device__ __forceinline__ void glds16(const void* g, unsigned lds_base) {
  unsigned keep;
  asm volatile("s_mov_b32 %0, m0\n\ts_mov_b32 m0, %2\n\ts_nop 0\n\tglobal_load_lds_dwordx4 %1, off\n\ts_mov_b32 m0, %0"
               : "=&s"(keep) : "v"(g), "s"(lds_base) : "memory");
}
template <int IMM>
__device__ __forceinline__ void glds16s(unsigned voff, const void* sbase, unsigned lds_base) {
  asm volatile("s_add_i32 m0, %2, %3\n\ts_nop 0\n\tglobal_load_lds_dwordx4 %0, %1"
               :: "v"(voff), "s"(sbase), "s"(lds_base), "i"(IMM) : "memory", "scc");
}
__device__ __forceinline__ void touch(const void* p, unsigned lds_dummy) {
  unsigned keep;
  asm volatile("s_mov_b32 %0, m0\n\ts_mov_b32 m0, %2\n\ts_nop 0\n\tglobal_load_lds_dword %1, off\n\ts_mov_b32 m0, %0"
               : "=&s"(keep) : "v"(p), "s"(lds_dummy) : "memory");
}
#define ST8(ptr, val) (*(u32x2*)(ptr) = (val))
#define SBAR() __builtin_amdgcn_sched_barrier(0)
#define PIN(x) asm volatile("" : "+v"(x))
__device__ __forceinline__ float rbf(float v) { return (float)(__bf16)v; }
__device__ __forceinline__ u32x2 pack4(float a, float b, float c, float d) {
  f32x2 lo = {a, b}, hi = {c, d};
  bf16x2 l = __builtin_convertvector(lo, bf16x2), h = __builtin_convertvector(hi, bf16x2);
  u32x2 r = {__builtin_bit_cast(unsigned, l), __builtin_bit_cast(unsigned, h)};
  return r;
}
__device__ __forceinline__ float bf_lo(unsigned u) { return __builtin_bit_cast(float, u << 16); }
__device__ __forceinline__ float bf_hi(unsigned u) { return __builtin_bit_cast(float, u & 0xffff0000u); }

#define GST(i)
#define GST_DECL
#define GST_FLUSH
template <int N>
__device__ __forceinline__ void wait_bar() {
  asm volatile("s_waitcnt vmcnt(%0) lgkmcnt(0)\n\ts_barrier" ::"i"(N) : "memory");
}

template <int I> struct IC { static constexpr int value = I; };
template <int I, int N, class F>
__device__ __forceinline__ void static_for(F& f) {
  if constexpr (I < N) { f(IC<I>{}); static_for<I + 1, N>(f); }
}

__device__ __forceinline__ float gelu_tanh(float x) {
  const float c1 = -2.0f * 0.7978845608028654f * 1.4426950408889634f;
  const float c2 = c1 * 0.044715f;
  float t = x * __builtin_fmaf(x * x, c2, c1);
  float e = __builtin_amdgcn_exp2f(t);
  return x * __builtin_amdgcn_rcpf(1.0f + e);
}

template <int N>
__device__ __forceinline__ void wait_vm() { asm volatile("s_waitcnt vmcnt(%0)" ::"i"(N) : "memory"); }
__device__ __forceinline__ void bar_lgkm() { asm volatile("s_waitcnt lgkmcnt(0)\n\ts_barrier" ::: "memory"); }
#define MFMA16(a, b, c) __builtin_amdgcn_mfma_f32_16x16x32_bf16(a, b, c, 0, 0, 0)
__device__ __forceinline__ float sum_g(float v) {
  auto a = __builtin_amdgcn_permlane16_swap(__float_as_uint(v), __float_as_uint(v), false, false);
  v = __uint_as_float(a[0]) + __uint_as_float(a[1]);
  auto b = __builtin_amdgcn_permlane32_swap(__float_as_uint(v), __float_as_uint(v), false, false);
  return __uint_as_float(b[0]) + __uint_as_float(b[1]);
}

template <int BM, int BN, int WVM, int WVN, int K, int LDW, int NST, int MODE, int SELF, int NL>
__global__ __launch_bounds__(512 + 64 * NL, 2 + NL / 4) void gemm_kernel(const bf16_t* __restrict__ X, const bf16_t* __restrict__ W,
                                                      const bf16_t* __restrict__ bias, const bf16_t* __restrict__ res,
                                                      bf16_t* __restrict__ out0, bf16_t* __restrict__ out1,
                                                      bf16_t* __restrict__ out2, float* __restrict__ stats,
                                                      const char* __restrict__ pf, int pf_lines) {
  extern __shared__ __attribute__((aligned(16))) char smem_g[];
  LDS_AS char* smem = (LDS_AS char*)smem_g;
  constexpr int TM = BM / WVM / 16, TN = BN / WVN / 16, NB = BN / 16, HM = TM / 2;
  constexpr int XB = BM * 128, WB = BN * 128, STB = XB + WB;
  constexpr int NXI = BM / 8, NI = (BM + BN) / 8, LPL = (NI + NL - 1) / NL, NK = K / 64;
  constexpr int NFULL = NI - NL * (LPL - 1);
#define DEP_BIG 2
#define DEP_SMALL 3
  constexpr int DEP = (BM == 128) ? DEP_BIG : DEP_SMALL;
  static_assert(NST >= DEP + 1, "ring too small");
  constexpr bool LN = (MODE == 0 || MODE == 2);
  constexpr int PF_AT = (NK - 4);
  constexpr int NX4 = HM * TN;
  constexpr int EX_OFF = NST * STB;
  constexpr int CS_OFF = EX_OFF;
  constexpr int ST_OFF = CS_OFF + 512;
  constexpr int PF_OFF = ST_OFF + 1024;
  constexpr int BI_OFF = PF_OFF + 256;
  static_assert(8 * NX4 * 1024 <= ((NK - 1) % NST) * STB, "exchange area overlaps the last stage");

  const int tid = threadIdx.x, lane = tid & 63;
  const int wave = __builtin_amdgcn_readfirstlane(tid >> 6);
  const int b = blockIdx.x, xcd = b & 7, j = b >> 3;
  int mt, nt;
  if (BM == 128) { mt = (xcd >> 1) * 4 + (j & 3); nt = (xcd & 1) * 8 + (j >> 2); }
  else           { mt = (xcd >> 1) * 8 + (j & 7); nt = (xcd & 1) * 4 + (j >> 3); }
  GST_DECL; GST(0);
  const int m0 = mt * BM;
  const int l15 = lane & 15, g = lane >> 4;
  const int q_ = (lane & 15) >> 2, p_ = lane & 3;
  const __bf16 one = (__bf16)1.0f;
  const bf16x8 ones8 = {one, one, one, one, one, one, one, one};

  auto colbase = [&](int nb) -> int {
    if (MODE == 0) return (nb >> 1) * 512 + (nt * 2 + (nb & 1)) * 16;
    return nt * BN + nb * 16;
  };
  auto tr8 = [&](LDS_AS char* p0, LDS_AS char* p1) -> bf16x8 {
    bf16x4 a = __builtin_amdgcn_ds_read_tr16_b64_v4bf16((LDS_AS bf16x4*)p0);
    bf16x4 c = __builtin_amdgcn_ds_read_tr16_b64_v4bf16((LDS_AS bf16x4*)p1);
    return __builtin_shufflevector(a, c, 0, 1, 2, 3, 4, 5, 6, 7);
  };

  if (wave >= 8) {
    const int lw = wave - 8;
    constexpr int XS = NXI / NL;
    unsigned voff[LPL];
    {
      const int row = 8 * lw + (lane >> 3);
      voff[0] = (unsigned)(row * K + (((lane & 7) ^ ((row >> 1) & 7)) * 8)) * 2u;
#pragma unroll
      for (int i = 1; i < XS; ++i) voff[i] = voff[0] + i * (8 * NL) * K * 2;
#pragma unroll
      for (int i = XS; i < LPL; ++i) {
        const int sidx = 4 * (lw + NL * (i - XS)) + (lane >> 4);
        const int kb = sidx / NB, nb = sidx % NB;
        const int krow = kb * 8 + (((lane >> 1) & 7) ^ ((kb & 1) << 2));
        voff[i] = (unsigned)(krow * LDW + colbase(nb) + (lane & 1) * 8) * 2u;
      }
    }
    const char* xs_ = (const char*)(X + (size_t)m0 * K);
    const char* ws_ = (const char*)W;
    const unsigned lds0 = (unsigned)(uintptr_t)smem + lw * 1024;
    const bool full = lw < NFULL;
    auto issue = [&](auto ST) {
      constexpr int st = decltype(ST)::value;
      auto one_ = [&](auto I) {
        constexpr int i = decltype(I)::value;
        if (i < LPL - 1 || NFULL == NL || full)
          glds16s<(st % NST) * STB + i * (1024 * NL)>(voff[i], i < XS ? xs_ + st * 128 : ws_ + (size_t)st * (64 * LDW * 2), lds0);
      };
      static_for<0, LPL>(one_);
    };
    static_for<0, (DEP < NK ? DEP : NK)>(issue);

    const int cnb0 = lw, cnb1 = (lw + 4 < NB) ? lw + 4 : NB - 1;
    const int wo0 = XB + g * NB * 256 + (q_ + 4 * (g & 1)) * 32 + p_ * 8;
    const int wo1 = XB + g * NB * 256 + (q_ + 4 * (1 - (g & 1))) * 32 + p_ * 8;
    const int xo = (lw * 32 + l15) * 128 + ((g ^ (l15 >> 1)) * 16);
    f32x4 cs0 = {0.f, 0.f, 0.f, 0.f}, cs1 = {0.f, 0.f, 0.f, 0.f};
    f32x4 rs[2], rq[2];
    rs[0] = rs[1] = rq[0] = rq[1] = f32x4{0.f, 0.f, 0.f, 0.f};
    auto lstep = [&](auto KT) {
      constexpr int kt = decltype(KT)::value;
      constexpr int last = (kt + DEP - 1 < NK - 1) ? kt + DEP - 1 : NK - 1;
      if (NFULL == NL || full) wait_vm<(last - kt) * LPL>(); else wait_vm<(last - kt) * (LPL - 1)>();
      bar_lgkm();
      if constexpr (kt + DEP < NK) issue(IC<kt + DEP>{});
      if (LN) {
        LDS_AS char* base = smem + (kt % NST) * STB;
#pragma unroll
        for (int ks = 0; ks < 2; ++ks) {
          const int o0 = (ks * 4 * NB + cnb0) * 256, o1 = (ks * 4 * NB + cnb1) * 256;
          cs0 = MFMA16(tr8(base + wo0 + o0, base + wo1 + o0), ones8, cs0);
          cs1 = MFMA16(tr8(base + wo0 + o1, base + wo1 + o1), ones8, cs1);
          if (SELF) {
#pragma unroll
            for (int mi = 0; mi < 2; ++mi) {
              const bf16x8 xf = *(const LDS_AS bf16x8*)(base + ((xo ^ (ks * 64)) + mi * 2048));
              rs[mi] = MFMA16(ones8, xf, rs[mi]);
              rq[mi] = MFMA16(xf, xf, rq[mi]);
            }
          }
        }
      }
    };
    static_for<0, NK>(lstep);
    if (LN) {
      if (l15 == 0) {
        *(LDS_AS f32x4*)(smem + CS_OFF + cnb0 * 64 + g * 16) = cs0;
        *(LDS_AS f32x4*)(smem + CS_OFF + cnb1 * 64 + g * 16) = cs1;
      }
      if (SELF) {
        if (g == (l15 >> 2)) {
          const int r = l15 & 3;
#pragma unroll
          for (int mi = 0; mi < 2; ++mi) {
            const float sq = r == 0 ? rq[mi][0] : r == 1 ? rq[mi][1] : r == 2 ? rq[mi][2] : rq[mi][3];
            const float m = rs[mi][0] * (1.0f / K);
            const float v = __builtin_fmaxf(sq * (1.0f / K) - m * m, 0.f);
            *(LDS_AS f32x2*)(smem + ST_OFF + (lw * 32 + mi * 16 + l15) * 8) = f32x2{m, __builtin_amdgcn_rsqf(v + 1e-5f)};
          }
        }
      }
    }
    bar_lgkm();
    return;
  }

  const int kg = wave >> 2, w4 = wave & 3;
  const int wm = w4 / WVN, wn = w4 % WVN;
  auto nb_of = [&](int ni) -> int {
    if (MODE == 0) return 2 * ni + wn;
    return wn * TN + ni;
  };
  const int tl0 = wm * TM * 16 + kg * HM * 16;
  const int tok0 = m0 + tl0;

  if (LN && !SELF) {
    if (wave < 2) {
      float sm = 0.f, sq = 0.f;
#pragma unroll
      for (int i = 0; i < 8; ++i) {
        const f32x4 t = *(const f32x4*)(stats + (size_t)(m0 + tid) * 32 + i * 4);
        sm += t[0] + t[2]; sq += t[1] + t[3];
      }
      const float m = sm * (1.0f / K);
      const float v = __builtin_fmaxf(sq * (1.0f / K) - m * m, 0.f);
      *(LDS_AS f32x2*)(smem + ST_OFF + tid * 8) = f32x2{m, __builtin_amdgcn_rsqf(v + 1e-5f)};
    }
  }
  if (MODE == 2) {
    if (wave == 2 && lane < BN / 4) *(LDS_AS u32x2*)(smem + BI_OFF + lane * 8) = *(const u32x2*)(bias + nt * BN + lane * 4);
  }
  u32x2 resv[HM][TN];
  u32x2 biasv[TN];
  if (MODE == 1 || MODE == 3) {
#pragma unroll
    for (int mi = 0; mi < HM; ++mi)
#pragma unroll
      for (int ni = 0; ni < TN; ++ni)
        resv[mi][ni] = *(const u32x2*)(res + (size_t)(tok0 + mi * 16 + l15) * DM + colbase(nb_of(ni)) + 4 * g);
  }
  if (MODE == 3) {
#pragma unroll
    for (int ni = 0; ni < TN; ++ni) biasv[ni] = *(const u32x2*)(bias + colbase(nb_of(ni)) + 4 * g);
  }

  const int xo_ = ((wm * TM * 16 + l15) * 128 + ((g ^ (l15 >> 1)) * 16)) ^ (kg * 64);
  const int xoA = xo_ + kg * HM * 2048, xoB = xo_ + (1 - kg) * HM * 2048;
  const int wo0 = XB + (kg * 4 + g) * NB * 256 + (q_ + 4 * (g & 1)) * 32 + p_ * 8 + nb_of(0) * 256;
  const int wo1 = XB + (kg * 4 + g) * NB * 256 + (q_ + 4 * (1 - (g & 1))) * 32 + p_ * 8 + nb_of(0) * 256;
  constexpr int NSTEP = (MODE == 0) ? 512 : 256;

  f32x4 acc[TM][TN];
#pragma unroll
  for (int mi = 0; mi < TM; ++mi)
#pragma unroll
    for (int ni = 0; ni < TN; ++ni) acc[mi][ni] = f32x4{0.f, 0.f, 0.f, 0.f};

  bf16x8 xf[2][TM], wf[2][TN];
  auto ldfrags = [&](auto KT) {
    constexpr int kt = decltype(KT)::value, p = kt & 1;
    LDS_AS char* base = smem + (kt % NST) * STB;
#pragma unroll
    for (int mi = 0; mi < TM; ++mi) xf[p][mi] = *(const LDS_AS bf16x8*)(base + (mi < HM ? xoA + mi * 2048 : xoB + (mi - HM) * 2048));
#pragma unroll
    for (int ni = 0; ni < TN; ++ni) wf[p][ni] = tr8(base + wo0 + ni * NSTEP, base + wo1 + ni * NSTEP);
  };
  GST(1);
  bar_lgkm();
  GST(2);
  ldfrags(IC<0>{});
  auto kstep = [&](auto KT) {
    constexpr int kt = decltype(KT)::value, p = kt & 1;
    if constexpr (kt + 1 < NK) {
      bar_lgkm();
      ldfrags(IC<kt + 1>{});
    }
    if constexpr (kt == PF_AT) {
      const int line = j * 512 + tid;
      if (line < pf_lines) touch(pf + (size_t)line * 128, (unsigned)(uintptr_t)smem + PF_OFF);
    }
    SBAR();
#pragma unroll
    for (int mj = 0; mj < TM; ++mj)
#pragma unroll
      for (int ni = 0; ni < TN; ++ni) {
        const int mi = (mj + HM) % TM;
        if (MODE == 0 && ni == 2) acc[mi][ni] = MFMA16(xf[p][mi], wf[p][ni], acc[mi][ni]);
        else                      acc[mi][ni] = MFMA16(wf[p][ni], xf[p][mi], acc[mi][ni]);
      }
    SBAR();
  };
  static_for<0, NK>(kstep);

  GST(3);
  {
    LDS_AS f32x4* wr = (LDS_AS f32x4*)(smem + wave * (NX4 * 1024) + lane * 16);
#pragma unroll
    for (int mi = 0; mi < HM; ++mi)
#pragma unroll
      for (int ni = 0; ni < TN; ++ni) wr[(mi * TN + ni) * 64] = acc[HM + mi][ni];
  }
  GST(4);
  bar_lgkm();
  GST(5);
  f32x4 fa[HM][TN];
  {
    const LDS_AS f32x4* rd = (const LDS_AS f32x4*)(smem + (wave ^ 4) * (NX4 * 1024) + lane * 16);
#pragma unroll
    for (int mi = 0; mi < HM; ++mi)
#pragma unroll
      for (int ni = 0; ni < TN; ++ni) fa[mi][ni] = acc[mi][ni] + rd[(mi * TN + ni) * 64];
  }
  auto tok_stats = [&](int tl) -> f32x2 { return *(const LDS_AS f32x2*)(smem + ST_OFF + tl * 8); };
  auto col_sum4 = [&](int nb) -> f32x4 { return *(const LDS_AS f32x4*)(smem + CS_OFF + nb * 64 + g * 16); };

  if (MODE == 0) {
    const int head = nt * 2 + wn;
    const float qscale = 0.25f * 1.4426950408889634f;
    const f32x4 cq = col_sum4(nb_of(0)), ck = col_sum4(nb_of(1));
    const float cv = *(const LDS_AS float*)(smem + CS_OFF + nb_of(2) * 64 + l15 * 4);
#pragma unroll
    for (int mi = 0; mi < HM; ++mi) {
      const int tok = tok0 + mi * 16 + l15;
      const f32x2 st = tok_stats(tl0 + mi * 16 + l15);
      {
        const float sc = st[1] * qscale;
        float v[4];
#pragma unroll
        for (int r = 0; r < 4; ++r) v[r] = sc * __builtin_fmaf(-st[0], cq[r], fa[mi][0][r]);
        ST8(out0 + ((size_t)head * SEQ + tok) * 16 + 4 * g, pack4(v[0], v[1], v[2], v[3]));
      }
      {
        float v[4];
#pragma unroll
        for (int r = 0; r < 4; ++r) v[r] = st[1] * __builtin_fmaf(-st[0], ck[r], fa[mi][1][r]);
        ST8(out1 + (size_t)head * 32768 + (tok >> 5) * 512 + (g >> 1) * 256 + (tok & 31) * 8 + (g & 1) * 4,
            pack4(v[0], v[1], v[2], v[3]));
      }
      {
        float v[4];
#pragma unroll
        for (int r = 0; r < 4; ++r) {
          const f32x2 sr = tok_stats(tl0 + mi * 16 + 4 * g + r);
          v[r] = sr[1] * __builtin_fmaf(-sr[0], cv, fa[mi][2][r]);
        }
        const int ks16 = (tok0 + mi * 16) >> 4;
        ST8(out2 + (size_t)head * 32768 + ks16 * 256 + (g & 1) * 128 + l15 * 8 + (g >> 1) * 4,
            pack4(v[0], v[1], v[2], v[3]));
      }
    }
  } else if (MODE == 2) {
#pragma unroll
    for (int mi = 0; mi < HM; ++mi) {
      const int tok = tok0 + mi * 16 + l15;
      const f32x2 st = tok_stats(tl0 + mi * 16 + l15);
      const float nrm = -st[0] * st[1];
#pragma unroll
      for (int ni = 0; ni < TN; ++ni) {
        const f32x4 cc = col_sum4(nb_of(ni));
        const u32x2 bv = *(const LDS_AS u32x2*)(smem + BI_OFF + ni * 32 + g * 8);
        const float bb[4] = {bf_lo(bv[0]), bf_hi(bv[0]), bf_lo(bv[1]), bf_hi(bv[1])};
        float v[4];
#pragma unroll
        for (int r = 0; r < 4; ++r)
          v[r] = gelu_tanh(__builtin_fmaf(st[1], fa[mi][ni][r], __builtin_fmaf(nrm, cc[r], bb[r])));
        ST8(out0 + (size_t)tok * FF + colbase(nb_of(ni)) + 4 * g, pack4(v[0], v[1], v[2], v[3]));
      }
    }
  } else {
#pragma unroll
    for (int mi = 0; mi < HM; ++mi) {
      const int tok = tok0 + mi * 16 + l15;
      float sm = 0.f, sq = 0.f;
#pragma unroll
      for (int ni = 0; ni < TN; ++ni) {
        float rr[4] = {bf_lo(resv[mi][ni][0]), bf_hi(resv[mi][ni][0]), bf_lo(resv[mi][ni][1]), bf_hi(resv[mi][ni][1])};
        if (MODE == 3) {
          rr[0] += bf_lo(biasv[ni][0]); rr[1] += bf_hi(biasv[ni][0]); rr[2] += bf_lo(biasv[ni][1]); rr[3] += bf_hi(biasv[ni][1]);
        }
        const u32x2 pk = pack4(rr[0] + fa[mi][ni][0], rr[1] + fa[mi][ni][1], rr[2] + fa[mi][ni][2], rr[3] + fa[mi][ni][3]);
        ST8(out0 + (size_t)tok * DM + colbase(nb_of(ni)) + 4 * g, pk);
        const float w[4] = {bf_lo(pk[0]), bf_hi(pk[0]), bf_lo(pk[1]), bf_hi(pk[1])};
#pragma unroll
        for (int r = 0; r < 4; ++r) { sm += w[r]; sq = __builtin_fmaf(w[r], w[r], sq); }
      }
      sm = sum_g(sm); sq = sum_g(sq);
      if (g == 0) *(f32x2*)(stats + (size_t)tok * 32 + (nt * 2 + wn) * 2) = f32x2{sm, sq};
    }
  }
  GST(6);
  asm volatile("s_waitcnt vmcnt(0)" ::: "memory");
  GST(7);
  GST_FLUSH;
}

constexpr int A_KOFF = 0, A_VOFF = 65536, A_ONES = 131072, A_QOFF = 139264, A_COMB = A_QOFF, A_LDS = A_COMB + 8 * 64 * 40;

#define MFMA32(a, b, c) __builtin_amdgcn_mfma_f32_32x32x16_bf16(a, b, c, 0, 0, 0)
__device__ __forceinline__ bf16x8 exp_pack8(const f32x16& s, int o) {
  bf16x8 p;
#pragma unroll
  for (int r = 0; r < 8; r += 2) {
    f32x2 e = {__builtin_amdgcn_exp2f(s[o + r]), __builtin_amdgcn_exp2f(s[o + r + 1])};
    bf16x2 c = __builtin_convertvector(e, bf16x2);
    p[r] = c[0]; p[r + 1] = c[1];
  }
  return p;
}
#define LDF(p) (*(const LDS_AS bf16x8*)(p))
#define ASTEP(SC, SN, VA, VB, VNA, VNB, KO, VO)                              \
  do {                                                                       \
    SN = MFMA32(kf, qf, negm); SBAR();                                       \
    kf = LDF(kp + (KO)); VNA = LDF(vp + (VO)); VNB = LDF(vp + (VO) + 512); SBAR(); \
    { bf16x8 pa = exp_pack8(SC, 0); SBAR(); o = MFMA32(VA, pa, o); SBAR(); } \
    { bf16x8 pb = exp_pack8(SC, 8); SBAR(); o = MFMA32(VB, pb, o); SBAR(); } \
  } while (0)

__device__ __forceinline__ float max16(const f32x16 s) {
  float a = __builtin_fmaxf(__builtin_fmaxf(s[0], s[1]), s[2]);
  float c = __builtin_fmaxf(__builtin_fmaxf(s[3], s[4]), s[5]);
  a = __builtin_fmaxf(__builtin_fmaxf(a, s[6]), s[7]);
  c = __builtin_fmaxf(__builtin_fmaxf(c, s[8]), s[9]);
  a = __builtin_fmaxf(__builtin_fmaxf(a, s[10]), s[11]);
  c = __builtin_fmaxf(__builtin_fmaxf(c, s[12]), s[13]);
  a = __builtin_fmaxf(__builtin_fmaxf(a, s[14]), s[15]);
  return __builtin_fmaxf(a, c);
}
__device__ __forceinline__ float xhalf_max(float m) {
  auto rr = __builtin_amdgcn_permlane32_swap(__float_as_uint(m), __float_as_uint(m), false, false);
  return __builtin_fmaxf(__uint_as_float(rr[0]), __uint_as_float(rr[1]));
}

__global__ __launch_bounds__(1024, 4) void attn_kernel(const bf16_t* __restrict__ Q, const bf16_t* __restrict__ Kf,
                                                       const bf16_t* __restrict__ Vf, bf16_t* __restrict__ O,
                                                       const char* __restrict__ pf0, int pf0_lines,
                                                       const char* __restrict__ pf1, int pf1_lines) {
  extern __shared__ __attribute__((aligned(16))) char smem_g[];
  LDS_AS char* smem = (LDS_AS char*)smem_g;
  const int tid = threadIdx.x, lane = tid & 63;
  const int wave = __builtin_amdgcn_readfirstlane(tid >> 6);
  const int kh = wave >> 3, qw = wave & 7;
  const int b = blockIdx.x, xcd = b & 7, j = b >> 3;
  const int head = xcd * 4 + (j & 3), qt = j >> 2;
  const int r32 = lane & 31, hh = lane >> 5;
  const int q0 = qt * 256 + qw * 32;

  const unsigned lds0 = (unsigned)(uintptr_t)smem;
    const bf16_t* ks = Kf + (size_t)head * 32768 + kh * 16384 + qw * 512 + lane * 8;
    const bf16_t* vs = Vf + (size_t)head * 32768 + kh * 16384 + qw * 512 + lane * 8;
    const unsigned kd = lds0 + A_KOFF + kh * 32768 + qw * 1024, vd = lds0 + A_VOFF + kh * 32768 + qw * 1024;
    glds16(Q + ((size_t)head * SEQ + q0 + r32) * 16 + hh * 8, lds0 + A_QOFF + qw * 1024);
    glds16(ks, kd);
    glds16(vs, vd);

  if (tid < 448) {
    const unsigned v = (tid & 31) == 0 ? 0x3F803F80u : 0u;
    *(LDS_AS u32x4*)(smem + A_ONES + tid * 16) = u32x4{v, v, v, v};
  }

  LDS_AS char* kb = smem + A_KOFF + kh * 32768 + lane * 16;
  const bool real = r32 < 16;
  LDS_AS char* vb = real ? (smem + A_VOFF + kh * 32768 + hh * 256 + r32 * 16) : (smem + A_ONES + (r32 == 16 ? 0 : 16));
  const int vstride = real ? 2048 : 0;

  wait_bar<0>();
  const bf16x8 qf = LDF(smem + A_QOFF + qw * 1024 + lane * 16);
  f32x16 negm;
  float mref;
  {
    f32x16 z = {};
    f32x16 s = MFMA32(LDF(smem + A_KOFF + lane * 16), qf, z);
    mref = xhalf_max(max16(s));
#pragma unroll
    for (int r = 0; r < 16; ++r) negm[r] = -mref;
  }
  f32x16 o = {};
  {
    LDS_AS char* kp = kb;
    LDS_AS char* vp = vb;
    bf16x8 kf = LDF(kp), va0 = LDF(vp), vb0 = LDF(vp + 512), va1, vb1;
    f32x16 sA = MFMA32(kf, qf, negm), sB;
    kf = LDF(kp + 1024);
    SBAR();
#pragma unroll 1
    for (int t = 0; t < 8; t += 2) {
      if (t < 6) {
        if ((wave >> 2) == (t >> 1)) {
          const int c = (t >> 1) + 1, w2 = (wave & 3) * 2;
          const bf16_t* k2 = Kf + (size_t)head * 32768 + c * 4096 + w2 * 512 + lane * 8;
          const bf16_t* v2 = Vf + (size_t)head * 32768 + c * 4096 + w2 * 512 + lane * 8;
          const unsigned d2 = lds0 + c * 8192 + w2 * 1024;
#pragma unroll
          for (int hf = 0; hf < 2; ++hf)
#pragma unroll
            for (int i = 0; i < 2; ++i) {
              glds16(k2 + hf * 16384 + i * 512, d2 + A_KOFF + hf * 32768 + i * 1024);
              glds16(v2 + hf * 16384 + i * 512, d2 + A_VOFF + hf * 32768 + i * 1024);
            }
        }
      } else {
        wait_bar<0>();
      }
      ASTEP(sA, sB, va0, vb0, va1, vb1, 2048, 1024);
      ASTEP(sB, sA, va1, vb1, va0, vb0, 3072, 2048);
      kp += 2048; vp += vstride;
    }
    {
      const int line = j * 1024 + tid;
      const char* pp = line < pf0_lines ? pf0 + (size_t)line * 128 : pf1 + (size_t)(line - pf0_lines) * 128;
      if (line < pf0_lines + pf1_lines) touch(pp, lds0 + A_ONES + 7168);
    }
#pragma unroll 1
    for (int it = 0; it < 6; ++it) {
      {
        const int pr = (it + (wave >> 2)) & 3;
        if (pr == 0) __builtin_amdgcn_s_setprio(0);
        else if (pr == 1) __builtin_amdgcn_s_setprio(1);
        else if (pr == 2) __builtin_amdgcn_s_setprio(2);
        else __builtin_amdgcn_s_setprio(3);
      }
      ASTEP(sA, sB, va0, vb0, va1, vb1, 2048, 1024);
      ASTEP(sB, sA, va1, vb1, va0, vb0, 3072, 2048);
      ASTEP(sA, sB, va0, vb0, va1, vb1, 4096, 3072);
      ASTEP(sB, sA, va1, vb1, va0, vb0, 5120, 4096);
      kp += 4096; vp += 2 * vstride;
    }
    __builtin_amdgcn_s_setprio(0);
  }
  if (__builtin_expect(__any(!(o[8] < 1.2676506e30f)), 0)) {
    float m = -INFINITY;
    f32x16 z = {};
#pragma unroll 1
    for (int t = 0; t < 32; ++t) {
      f32x16 sc = MFMA32(LDF(kb + t * 1024), qf, z);
      m = __builtin_fmaxf(m, max16(sc));
    }
    mref = xhalf_max(m);
#pragma unroll
    for (int r = 0; r < 16; ++r) negm[r] = -mref;
    o = z;
#pragma unroll 1
    for (int t = 0; t < 32; ++t) {
      const f32x16 sc = MFMA32(LDF(kb + t * 1024), qf, negm);
      LDS_AS char* vq = vb + (vstride >> 1) * t;
      o = MFMA32(LDF(vq), exp_pack8(sc, 0), o);
      o = MFMA32(LDF(vq + 512), exp_pack8(sc, 8), o);
    }
  }

  {
    auto rr = __builtin_amdgcn_permlane32_swap(__float_as_uint(o[8]), __float_as_uint(o[8]), false, false);
    o[8] = __uint_as_float(rr[0]);
  }
  LDS_AS float* cb = (LDS_AS float*)(smem + A_COMB + qw * (64 * 40) + lane * 4);
  if (kh) {
#pragma unroll
    for (int r = 0; r < 9; ++r) cb[r * 64] = o[r];
    cb[9 * 64] = mref;
  }
  bar_lgkm();
  if (!kh) {
    const float m2 = cb[9 * 64];
    const float mm = __builtin_fmaxf(mref, m2);
    const float f1 = __builtin_amdgcn_exp2f(mref - mm), f2 = __builtin_amdgcn_exp2f(m2 - mm);
    const float l = o[8] * f1 + cb[8 * 64] * f2;
    const float inv = __builtin_amdgcn_rcpf(l);
    const float a1 = f1 * inv, a2 = f2 * inv;
    float v[8];
#pragma unroll
    for (int r = 0; r < 8; ++r) v[r] = o[r] * a1 + cb[r * 64] * a2;
    bf16_t* op = O + (size_t)(q0 + r32) * DM + head * 16 + 4 * hh;
    *(u32x2*)(op) = pack4(v[0], v[1], v[2], v[3]);
    *(u32x2*)(op + 8) = pack4(v[4], v[5], v[6], v[7]);
  }
  asm volatile("s_waitcnt vmcnt(0)" ::: "memory");
}

constexpr int QKV_NST = 5, WO_NST = 6, F1_NST = 5, F2_NST = 6;
#define NL_SMALL 4
#define QKV0_K gemm_kernel<128, 96, 2, 2, 512, 1536, QKV_NST, 0, 1, 4>
#define QKV1_K gemm_kernel<128, 96, 2, 2, 512, 1536, QKV_NST, 0, 0, 4>
#define WO_K gemm_kernel<64, 64, 2, 2, 512, 512, WO_NST, 1, 0, NL_SMALL>
#define F1_K gemm_kernel<128, 112, 4, 1, 512, 1792, F1_NST, 2, 0, 4>
#define F2_K gemm_kernel<64, 64, 2, 2, 1792, 512, F2_NST, 3, 0, NL_SMALL>
constexpr int QKV_LDS = QKV_NST * (128 + 96) * 128 + 2048, WO_LDS = WO_NST * 128 * 128 + 2048,
              F1_LDS = F1_NST * (128 + 112) * 128 + 2048, F2_LDS = F2_NST * 128 * 128 + 2048;

extern "C" void kernel_launch(void* const* d_in, const int* in_sizes, int n_in, void* d_out, int out_size, void* d_ws,
                              size_t ws_size, hipStream_t stream) {
  const bf16_t* x = (const bf16_t*)d_in[0];
  const bf16_t* wqkv = (const bf16_t*)d_in[1];
  const bf16_t* wo = (const bf16_t*)d_in[2];
  const bf16_t* w1 = (const bf16_t*)d_in[4];
  const bf16_t* b1 = (const bf16_t*)d_in[5];
  const bf16_t* w2 = (const bf16_t*)d_in[6];
  const bf16_t* b2 = (const bf16_t*)d_in[7];
  char* ws = (char*)d_ws;
  bf16_t* Qb = (bf16_t*)(ws + (0 << 20));
  bf16_t* Kb = (bf16_t*)(ws + (2 << 20));
  bf16_t* Vb = (bf16_t*)(ws + (4 << 20));
  bf16_t* Ob = (bf16_t*)(ws + (6 << 20));
  bf16_t* X1 = (bf16_t*)(ws + (8 << 20));
  bf16_t* Hb = (bf16_t*)(ws + (10 << 20));
  bf16_t* X2 = (bf16_t*)(ws + (18 << 20));
  float* St1 = (float*)(ws + (20 << 20));
  float* St2 = (float*)(ws + (21 << 20));
  bf16_t* out = (bf16_t*)d_out;

  hipFuncSetAttribute((const void*)QKV0_K, hipFuncAttributeMaxDynamicSharedMemorySize, QKV_LDS);
  hipFuncSetAttribute((const void*)QKV1_K, hipFuncAttributeMaxDynamicSharedMemorySize, QKV_LDS);
  hipFuncSetAttribute((const void*)WO_K, hipFuncAttributeMaxDynamicSharedMemorySize, WO_LDS);
  hipFuncSetAttribute((const void*)F1_K, hipFuncAttributeMaxDynamicSharedMemorySize, F1_LDS);
  hipFuncSetAttribute((const void*)F2_K, hipFuncAttributeMaxDynamicSharedMemorySize, F2_LDS);
  hipFuncSetAttribute((const void*)attn_kernel, hipFuncAttributeMaxDynamicSharedMemorySize, A_LDS);

  for (int layer = 0; layer < 2; ++layer) {
    const bf16_t* xin = layer == 0 ? x : X2;
    bf16_t* xout = layer == 0 ? X2 : out;
    const char* none = (const char*)x;
    if (layer == 0) QKV0_K<<<256, 768, QKV_LDS, stream>>>(xin, wqkv, nullptr, nullptr, Qb, Kb, Vb, nullptr, none, 0);
    else            QKV1_K<<<256, 768, QKV_LDS, stream>>>(xin, wqkv, nullptr, nullptr, Qb, Kb, Vb, St2, none, 0);
    attn_kernel<<<256, 1024, A_LDS, stream>>>(Qb, Kb, Vb, Ob, (const char*)wo, DM * DM * 2 / 128, (const char*)w1, DM * FF * 2 / 128);
    WO_K<<<256, 512 + 64 * NL_SMALL, WO_LDS, stream>>>(Ob, wo, nullptr, xin, X1, nullptr, nullptr, St1, none, 0);
    F1_K<<<256, 768, F1_LDS, stream>>>(X1, w1, b1, nullptr, Hb, nullptr, nullptr, St1, (const char*)w2, FF * DM * 2 / 128);
    F2_K<<<256, 512 + 64 * NL_SMALL, F2_LDS, stream>>>(Hb, w2, b2, X1, xout, nullptr, nullptr, St2, (const char*)wqkv,
                                       layer == 0 ? DM * 3 * DM * 2 / 128 : 0);
  }
}
```

```cpp
#include <hip/hip_runtime.h>
#include <hip/hip_bf16.h>
#include <stdint.h>

typedef __bf16 bf16_t;
typedef __bf16 bf16x2 __attribute__((ext_vector_type(2)));
typedef __bf16 bf16x4 __attribute__((ext_vector_type(4)));
typedef __bf16 bf16x8 __attribute__((ext_vector_type(8)));
typedef float f32x2 __attribute__((ext_vector_type(2)));
typedef float f32x4 __attribute__((ext_vector_type(4)));
typedef float f32x16 __attribute__((ext_vector_type(16)));
typedef unsigned u32x2 __attribute__((ext_vector_type(2)));
typedef unsigned u32x4 __attribute__((ext_vector_type(4)));

#define LDS_AS __attribute__((address_space(3)))
#define GLB_AS __attribute__((address_space(1)))

constexpr int SEQ = 2048, DM = 512, NH = 32, DH = 16, FF = 1792;

__device__ __forceinline__ void glds16(const void* g, unsigned lds_base) {
  unsigned keep;
  asm volatile("s_mov_b32 %0, m0\n\ts_mov_b32 m0, %2\n\ts_nop 0\n\tglobal_load_lds_dwordx4 %1, off\n\ts_mov_b32 m0, %0"
               : "=&s"(keep) : "v"(g), "s"(lds_base) : "memory");
}
template <int IMM>
__device__ __forceinline__ void glds16s(unsigned voff, const void* sbase, unsigned lds_base) {
  asm volatile("s_add_i32 m0, %2, %3\n\ts_nop 0\n\tglobal_load_lds_dwordx4 %0, %1"
               :: "v"(voff), "s"(sbase), "s"(lds_base), "i"(IMM) : "memory", "scc");
}
__device__ __forceinline__ void touch(const void* p, unsigned lds_dummy) {
  unsigned keep;
  asm volatile("s_mov_b32 %0, m0\n\ts_mov_b32 m0, %2\n\ts_nop 0\n\tglobal_load_lds_dword %1, off\n\ts_mov_b32 m0, %0"
               : "=&s"(keep) : "v"(p), "s"(lds_dummy) : "memory");
}
#define ST8(ptr, val) (*(u32x2*)(ptr) = (val))
#define SBAR() __builtin_amdgcn_sched_barrier(0)
#define PIN(x) asm volatile("" : "+v"(x))
__device__ __forceinline__ int tile_off(int tok, int col, int nc16) { return ((tok >> 4) * nc16 + (col >> 4)) * 256; }
__device__ __forceinline__ float rbf(float v) { return (float)(__bf16)v; }
__device__ __forceinline__ u32x2 pack4(float a, float b, float c, float d) {
  f32x2 lo = {a, b}, hi = {c, d};
  bf16x2 l = __builtin_convertvector(lo, bf16x2), h = __builtin_convertvector(hi, bf16x2);
  u32x2 r = {__builtin_bit_cast(unsigned, l), __builtin_bit_cast(unsigned, h)};
  return r;
}
__device__ __forceinline__ float bf_lo(unsigned u) { return __builtin_bit_cast(float, u << 16); }
__device__ __forceinline__ float bf_hi(unsigned u) { return __builtin_bit_cast(float, u & 0xffff0000u); }

#define GST(i)
#define GST_DECL
#define GST_FLUSH
template <int N>
__device__ __forceinline__ void wait_bar() {
  asm volatile("s_waitcnt vmcnt(%0) lgkmcnt(0)\n\ts_barrier" ::"i"(N) : "memory");
}

template <int I> struct IC { static constexpr int value = I; };
template <int I, int N, class F>
__device__ __forceinline__ void static_for(F& f) {
  if constexpr (I < N) { f(IC<I>{}); static_for<I + 1, N>(f); }
}

__device__ __forceinline__ float gelu_tanh(float x) {
  const float c1 = -2.0f * 0.7978845608028654f * 1.4426950408889634f;
  const float c2 = c1 * 0.044715f;
  float t = x * __builtin_fmaf(x * x, c2, c1);
  float e = __builtin_amdgcn_exp2f(t);
  return x * __builtin_amdgcn_rcpf(1.0f + e);
}

template <int N>
__device__ __forceinline__ void wait_vm() { asm volatile("s_waitcnt vmcnt(%0)" ::"i"(N) : "memory"); }
__device__ __forceinline__ void bar_lgkm() { asm volatile("s_waitcnt lgkmcnt(0)\n\ts_barrier" ::: "memory"); }
#define MFMA16(a, b, c) __builtin_amdgcn_mfma_f32_16x16x32_bf16(a, b, c, 0, 0, 0)
__device__ __forceinline__ float sum_g(float v) {
  auto a = __builtin_amdgcn_permlane16_swap(__float_as_uint(v), __float_as_uint(v), false, false);
  v = __uint_as_float(a[0]) + __uint_as_float(a[1]);
  auto b = __builtin_amdgcn_permlane32_swap(__float_as_uint(v), __float_as_uint(v), false, false);
  return __uint_as_float(b[0]) + __uint_as_float(b[1]);
}

template <int BM, int BN, int WVM, int WVN, int K, int LDW, int NST, int MODE, int SELF, int NL, int XT>
__global__ __launch_bounds__(512 + 64 * NL, 2 + NL / 4) void gemm_kernel(const bf16_t* __restrict__ X, const bf16_t* __restrict__ W,
                                                      const bf16_t* __restrict__ bias, const bf16_t* __restrict__ res,
                                                      bf16_t* __restrict__ out0, bf16_t* __restrict__ out1,
                                                      bf16_t* __restrict__ out2, float* __restrict__ stats,
                                                      const char* __restrict__ pf, int pf_lines, int flags) {
  extern __shared__ __attribute__((aligned(16))) char smem_g[];
  LDS_AS char* smem = (LDS_AS char*)smem_g;
  constexpr int TM = BM / WVM / 16, TN = BN / WVN / 16, NB = BN / 16, HM = TM / 2;
  constexpr int XB = BM * 128, WB = BN * 128, STB = XB + WB;
  constexpr int NXI = BM / 8, NI = (BM + BN) / 8, LPL = (NI + NL - 1) / NL, NK = K / 64;
  constexpr int NFULL = NI - NL * (LPL - 1);
#define DEP_BIG 2
#define DEP_SMALL 3
  constexpr int DEP = (BM == 128) ? DEP_BIG : DEP_SMALL;
  static_assert(NST >= DEP + 1, "ring too small");
  constexpr bool LN = (MODE == 0 || MODE == 2);
  constexpr int PF_AT = (NK - 4);
  constexpr int NX4 = HM * TN;
  constexpr int EX_OFF = NST * STB;
  constexpr int CS_OFF = EX_OFF;
  constexpr int ST_OFF = CS_OFF + 512;
  constexpr int PF_OFF = ST_OFF + 1024;
  constexpr int BI_OFF = PF_OFF + 256;
  static_assert(8 * NX4 * 1024 <= ((NK - 1) % NST) * STB, "exchange area overlaps the last stage");

  const int tid = threadIdx.x, lane = tid & 63;
  const int wave = __builtin_amdgcn_readfirstlane(tid >> 6);
  const int b = blockIdx.x, xcd = b & 7, j = b >> 3;
  int mt, nt;
  if (BM == 128) { mt = xcd * 2 + (j & 1); nt = j >> 1; }
  else           { mt = xcd * 4 + (j & 3); nt = j >> 2; }
  GST_DECL; GST(0);
  const int m0 = mt * BM;
  const int l15 = lane & 15, g = lane >> 4;
  const int q_ = (lane & 15) >> 2, p_ = lane & 3;
  const __bf16 one = (__bf16)1.0f;
  const bf16x8 ones8 = {one, one, one, one, one, one, one, one};

  auto colbase = [&](int nb) -> int {
    if (MODE == 0) return (nb >> 1) * 512 + (nt * 2 + (nb & 1)) * 16;
    return nt * BN + nb * 16;
  };
  auto tr8 = [&](LDS_AS char* p0, LDS_AS char* p1) -> bf16x8 {
    bf16x4 a = __builtin_amdgcn_ds_read_tr16_b64_v4bf16((LDS_AS bf16x4*)p0);
    bf16x4 c = __builtin_amdgcn_ds_read_tr16_b64_v4bf16((LDS_AS bf16x4*)p1);
    return __builtin_shufflevector(a, c, 0, 1, 2, 3, 4, 5, 6, 7);
  };

  if (wave >= 8) {
    const int lw = wave - 8;
    constexpr int XS = NXI / NL;
    unsigned voff[LPL];
    {
      const int row = 8 * lw + (lane >> 3);
      const int ch = (lane & 7) ^ ((row >> 1) & 7);
      if (XT) voff[0] = (unsigned)((row >> 4) * (K / 16) * 512 + (ch >> 1) * 512 + (row & 15) * 32 + (ch & 1) * 16);
      else    voff[0] = (unsigned)(row * K + ch * 8) * 2u;
#pragma unroll
      for (int i = 1; i < XS; ++i) voff[i] = voff[0] + i * (8 * NL) * K * 2;
#pragma unroll
      for (int i = XS; i < LPL; ++i) {
        const int sidx = 4 * (lw + NL * (i - XS)) + (lane >> 4);
        const int kb = sidx / NB, nb = sidx % NB;
        const int krow = kb * 8 + (((lane >> 1) & 7) ^ ((kb & 1) << 2));
        voff[i] = (unsigned)(krow * LDW + colbase(nb) + (lane & 1) * 8) * 2u;
      }
    }
    const char* xs_ = (const char*)(X + (size_t)m0 * K);
    const char* ws_ = (const char*)W;
    const unsigned lds0 = (unsigned)(uintptr_t)smem + lw * 1024;
    const bool full = lw < NFULL;
    auto issue = [&](auto ST) {
      constexpr int st = decltype(ST)::value;
      auto one_ = [&](auto I) {
        constexpr int i = decltype(I)::value;
        if (i < LPL - 1 || NFULL == NL || full)
          glds16s<(st % NST) * STB + i * (1024 * NL)>(voff[i], i < XS ? xs_ + st * (XT ? 2048 : 128) : ws_ + (size_t)st * (64 * LDW * 2), lds0);
      };
      static_for<0, LPL>(one_);
    };
    static_for<0, (DEP < NK ? DEP : NK)>(issue);

    const int cnb0 = lw, cnb1 = (lw + 4 < NB) ? lw + 4 : NB - 1;
    const int wo0 = XB + g * NB * 256 + (q_ + 4 * (g & 1)) * 32 + p_ * 8;
    const int wo1 = XB + g * NB * 256 + (q_ + 4 * (1 - (g & 1))) * 32 + p_ * 8;
    const int xo = (lw * 32 + l15) * 128 + ((g ^ (l15 >> 1)) * 16);
    f32x4 cs0 = {0.f, 0.f, 0.f, 0.f}, cs1 = {0.f, 0.f, 0.f, 0.f};
    f32x4 rs[2], rq[2];
    rs[0] = rs[1] = rq[0] = rq[1] = f32x4{0.f, 0.f, 0.f, 0.f};
    auto lstep = [&](auto KT) {
      constexpr int kt = decltype(KT)::value;
      constexpr int last = (kt + DEP - 1 < NK - 1) ? kt + DEP - 1 : NK - 1;
      if (NFULL == NL || full) wait_vm<(last - kt) * LPL>(); else wait_vm<(last - kt) * (LPL - 1)>();
      bar_lgkm();
      if constexpr (kt + DEP < NK) issue(IC<kt + DEP>{});
      if (LN) {
        LDS_AS char* base = smem + (kt % NST) * STB;
#pragma unroll
        for (int ks = 0; ks < 2; ++ks) {
          const int o0 = (ks * 4 * NB + cnb0) * 256, o1 = (ks * 4 * NB + cnb1) * 256;
          cs0 = MFMA16(tr8(base + wo0 + o0, base + wo1 + o0), ones8, cs0);
          cs1 = MFMA16(tr8(base + wo0 + o1, base + wo1 + o1), ones8, cs1);
          if (SELF) {
#pragma unroll
            for (int mi = 0; mi < 2; ++mi) {
              const bf16x8 xf = *(const LDS_AS bf16x8*)(base + ((xo ^ (ks * 64)) + mi * 2048));
              rs[mi] = MFMA16(ones8, xf, rs[mi]);
              rq[mi] = MFMA16(xf, xf, rq[mi]);
            }
          }
        }
      }
    };
    static_for<0, NK>(lstep);
    if (LN) {
      if (l15 == 0) {
        *(LDS_AS f32x4*)(smem + CS_OFF + cnb0 * 64 + g * 16) = cs0;
        *(LDS_AS f32x4*)(smem + CS_OFF + cnb1 * 64 + g * 16) = cs1;
      }
      if (SELF) {
        if (g == (l15 >> 2)) {
          const int r = l15 & 3;
#pragma unroll
          for (int mi = 0; mi < 2; ++mi) {
            const float sq = r == 0 ? rq[mi][0] : r == 1 ? rq[mi][1] : r == 2 ? rq[mi][2] : rq[mi][3];
            const float m = rs[mi][0] * (1.0f / K);
            const float v = __builtin_fmaxf(sq * (1.0f / K) - m * m, 0.f);
            *(LDS_AS f32x2*)(smem + ST_OFF + (lw * 32 + mi * 16 + l15) * 8) = f32x2{m, __builtin_amdgcn_rsqf(v + 1e-5f)};
          }
        }
      }
    }
    bar_lgkm();
    return;
  }

  const int kg = wave >> 2, w4 = wave & 3;
  const int wm = w4 / WVN, wn = w4 % WVN;
  auto nb_of = [&](int ni) -> int {
    if (MODE == 0) return 2 * ni + wn;
    return wn * TN + ni;
  };
  const int tl0 = wm * TM * 16 + kg * HM * 16;
  const int tok0 = m0 + tl0;

  if (LN && !SELF) {
    if (wave < 2) {
      float sm = 0.f, sq = 0.f;
#pragma unroll
      for (int i = 0; i < 8; ++i) {
        const f32x4 t = *(const f32x4*)(stats + (size_t)(m0 + tid) * 32 + i * 4);
        sm += t[0] + t[2]; sq += t[1] + t[3];
      }
      const float m = sm * (1.0f / K);
      const float v = __builtin_fmaxf(sq * (1.0f / K) - m * m, 0.f);
      *(LDS_AS f32x2*)(smem + ST_OFF + tid * 8) = f32x2{m, __builtin_amdgcn_rsqf(v + 1e-5f)};
    }
  }
  if (MODE == 2) {
    if (wave == 2 && lane < BN / 4) *(LDS_AS u32x2*)(smem + BI_OFF + lane * 8) = *(const u32x2*)(bias + nt * BN + lane * 4);
  }
  u32x2 resv[HM][TN];
  u32x2 biasv[TN];
  if (MODE == 1 || MODE == 3) {
#pragma unroll
    for (int mi = 0; mi < HM; ++mi)
#pragma unroll
      for (int ni = 0; ni < TN; ++ni)
        resv[mi][ni] = *(const u32x2*)(res + ((flags & 1) ? tile_off(tok0 + mi * 16, colbase(nb_of(ni)), DM / 16) + l15 * 16 + 4 * g
                                                             : (tok0 + mi * 16 + l15) * DM + colbase(nb_of(ni)) + 4 * g));
  }
  if (MODE == 3) {
#pragma unroll
    for (int ni = 0; ni < TN; ++ni) biasv[ni] = *(const u32x2*)(bias + colbase(nb_of(ni)) + 4 * g);
  }

  const int xo_ = ((wm * TM * 16 + l15) * 128 + ((g ^ (l15 >> 1)) * 16)) ^ (kg * 64);
  const int xoA = xo_ + kg * HM * 2048, xoB = xo_ + (1 - kg) * HM * 2048;
  const int wo0 = XB + (kg * 4 + g) * NB * 256 + (q_ + 4 * (g & 1)) * 32 + p_ * 8 + nb_of(0) * 256;
  const int wo1 = XB + (kg * 4 + g) * NB * 256 + (q_ + 4 * (1 - (g & 1))) * 32 + p_ * 8 + nb_of(0) * 256;
  constexpr int NSTEP = (MODE == 0) ? 512 : 256;

  f32x4 acc[TM][TN];
#pragma unroll
  for (int mi = 0; mi < TM; ++mi)
#pragma unroll
    for (int ni = 0; ni < TN; ++ni) acc[mi][ni] = f32x4{0.f, 0.f, 0.f, 0.f};

  bf16x8 xf[2][TM], wf[2][TN];
  auto ldfrags = [&](auto KT) {
    constexpr int kt = decltype(KT)::value, p = kt & 1;
    LDS_AS char* base = smem + (kt % NST) * STB;
#pragma unroll
    for (int mi = 0; mi < TM; ++mi) xf[p][mi] = *(const LDS_AS bf16x8*)(base + (mi < HM ? xoA + mi * 2048 : xoB + (mi - HM) * 2048));
#pragma unroll
    for (int ni = 0; ni < TN; ++ni) wf[p][ni] = tr8(base + wo0 + ni * NSTEP, base + wo1 + ni * NSTEP);
  };
  GST(1);
  bar_lgkm();
  GST(2);
  ldfrags(IC<0>{});
#define LATE_N 1
  auto kstep = [&](auto KT) {
    constexpr int kt = decltype(KT)::value, p = kt & 1;
    constexpr bool late = (kt + 1 < NK) && (kt + 1 >= NK - LATE_N);
    if constexpr (kt + 1 < NK && !late) {
      bar_lgkm();
      ldfrags(IC<kt + 1>{});
    }
    if constexpr (kt == PF_AT) {
      const int line = j * 512 + tid;
      if (line < pf_lines) touch(pf + (size_t)line * 128, (unsigned)(uintptr_t)smem + PF_OFF);
    }
    SBAR();
#pragma unroll
    for (int mj = 0; mj < TM; ++mj)
#pragma unroll
      for (int ni = 0; ni < TN; ++ni) {
        const int mi = (mj + HM) % TM;
        if (MODE == 0 && ni == 2) acc[mi][ni] = MFMA16(xf[p][mi], wf[p][ni], acc[mi][ni]);
        else                      acc[mi][ni] = MFMA16(wf[p][ni], xf[p][mi], acc[mi][ni]);
      }
    SBAR();
    if constexpr (late) {
      bar_lgkm();
      ldfrags(IC<kt + 1>{});
      SBAR();
    }
  };
  static_for<0, NK>(kstep);

  GST(3);
  {
    LDS_AS f32x4* wr = (LDS_AS f32x4*)(smem + wave * (NX4 * 1024) + lane * 16);
#pragma unroll
    for (int mi = 0; mi < HM; ++mi)
#pragma unroll
      for (int ni = 0; ni < TN; ++ni) wr[(mi * TN + ni) * 64] = acc[HM + mi][ni];
  }
  GST(4);
  bar_lgkm();
  GST(5);
  f32x4 fa[HM][TN];
  {
    const LDS_AS f32x4* rd = (const LDS_AS f32x4*)(smem + (wave ^ 4) * (NX4 * 1024) + lane * 16);
#pragma unroll
    for (int mi = 0; mi < HM; ++mi)
#pragma unroll
      for (int ni = 0; ni < TN; ++ni) fa[mi][ni] = acc[mi][ni] + rd[(mi * TN + ni) * 64];
  }
  auto tok_stats = [&](int tl) -> f32x2 { return *(const LDS_AS f32x2*)(smem + ST_OFF + tl * 8); };
  auto col_sum4 = [&](int nb) -> f32x4 { return *(const LDS_AS f32x4*)(smem + CS_OFF + nb * 64 + g * 16); };

  if (MODE == 0) {
    const int head = nt * 2 + wn;
    const float qscale = 0.25f * 1.4426950408889634f;
    const f32x4 cq = col_sum4(nb_of(0)), ck = col_sum4(nb_of(1));
    const float cv = *(const LDS_AS float*)(smem + CS_OFF + nb_of(2) * 64 + l15 * 4);
#pragma unroll
    for (int mi = 0; mi < HM; ++mi) {
      const int tok = tok0 + mi * 16 + l15;
      const f32x2 st = tok_stats(tl0 + mi * 16 + l15);
      {
        const float sc = st[1] * qscale;
        float v[4];
#pragma unroll
        for (int r = 0; r < 4; ++r) v[r] = sc * __builtin_fmaf(-st[0], cq[r], fa[mi][0][r]);
        ST8(out0 + ((size_t)head * SEQ + tok) * 16 + 4 * g, pack4(v[0], v[1], v[2], v[3]));
      }
      {
        float v[4];
#pragma unroll
        for (int r = 0; r < 4; ++r) v[r] = st[1] * __builtin_fmaf(-st[0], ck[r], fa[mi][1][r]);
        ST8(out1 + (size_t)head * 32768 + (tok >> 5) * 512 + (g >> 1) * 256 + (tok & 31) * 8 + (g & 1) * 4,
            pack4(v[0], v[1], v[2], v[3]));
      }
      {
        float v[4];
#pragma unroll
        for (int r = 0; r < 4; ++r) {
          const f32x2 sr = tok_stats(tl0 + mi * 16 + 4 * g + r);
          v[r] = sr[1] * __builtin_fmaf(-sr[0], cv, fa[mi][2][r]);
        }
        const int ks16 = (tok0 + mi * 16) >> 4;
        ST8(out2 + (size_t)head * 32768 + ks16 * 256 + (g & 1) * 128 + l15 * 8 + (g >> 1) * 4,
            pack4(v[0], v[1], v[2], v[3]));
      }
    }
  } else if (MODE == 2) {
#pragma unroll
    for (int mi = 0; mi < HM; ++mi) {
      const int tok = tok0 + mi * 16 + l15;
      const f32x2 st = tok_stats(tl0 + mi * 16 + l15);
      const float nrm = -st[0] * st[1];
#pragma unroll
      for (int ni = 0; ni < TN; ++ni) {
        const f32x4 cc = col_sum4(nb_of(ni));
        const u32x2 bv = *(const LDS_AS u32x2*)(smem + BI_OFF + ni * 32 + g * 8);
        const float bb[4] = {bf_lo(bv[0]), bf_hi(bv[0]), bf_lo(bv[1]), bf_hi(bv[1])};
        float v[4];
#pragma unroll
        for (int r = 0; r < 4; ++r)
          v[r] = gelu_tanh(__builtin_fmaf(st[1], fa[mi][ni][r], __builtin_fmaf(nrm, cc[r], bb[r])));
        ST8(out0 + tile_off(tok0 + mi * 16, colbase(nb_of(ni)), FF / 16) + l15 * 16 + 4 * g, pack4(v[0], v[1], v[2], v[3]));
      }
    }
  } else {
#pragma unroll
    for (int mi = 0; mi < HM; ++mi) {
      const int tok = tok0 + mi * 16 + l15;
      float sm = 0.f, sq = 0.f;
#pragma unroll
      for (int ni = 0; ni < TN; ++ni) {
        float rr[4] = {bf_lo(resv[mi][ni][0]), bf_hi(resv[mi][ni][0]), bf_lo(resv[mi][ni][1]), bf_hi(resv[mi][ni][1])};
        if (MODE == 3) {
          rr[0] += bf_lo(biasv[ni][0]); rr[1] += bf_hi(biasv[ni][0]); rr[2] += bf_lo(biasv[ni][1]); rr[3] += bf_hi(biasv[ni][1]);
        }
        const u32x2 pk = pack4(rr[0] + fa[mi][ni][0], rr[1] + fa[mi][ni][1], rr[2] + fa[mi][ni][2], rr[3] + fa[mi][ni][3]);
        ST8(out0 + ((flags & 2) ? tile_off(tok0 + mi * 16, colbase(nb_of(ni)), DM / 16) + l15 * 16 + 4 * g
                                : tok * DM + colbase(nb_of(ni)) + 4 * g), pk);
        const float w[4] = {bf_lo(pk[0]), bf_hi(pk[0]), bf_lo(pk[1]), bf_hi(pk[1])};
#pragma unroll
        for (int r = 0; r < 4; ++r) { sm += w[r]; sq = __builtin_fmaf(w[r], w[r], sq); }
      }
      sm = sum_g(sm); sq = sum_g(sq);
      if (g == 0) *(f32x2*)(stats + (size_t)tok * 32 + (nt * 2 + wn) * 2) = f32x2{sm, sq};
    }
  }
  GST(6);
  asm volatile("s_waitcnt vmcnt(0)" ::: "memory");
  GST(7);
  GST_FLUSH;
}

constexpr int A_KOFF = 0, A_VOFF = 65536, A_ONES = 131072, A_QOFF = 139264, A_COMB = A_QOFF, A_LDS = A_COMB + 8 * 64 * 40;

#define MFMA32(a, b, c) __builtin_amdgcn_mfma_f32_32x32x16_bf16(a, b, c, 0, 0, 0)
__device__ __forceinline__ bf16x8 exp_pack8(const f32x16& s, int o) {
  bf16x8 p;
#pragma unroll
  for (int r = 0; r < 8; r += 2) {
    f32x2 e = {__builtin_amdgcn_exp2f(s[o + r]), __builtin_amdgcn_exp2f(s[o + r + 1])};
    bf16x2 c = __builtin_convertvector(e, bf16x2);
    p[r] = c[0]; p[r + 1] = c[1];
  }
  return p;
}
#define LDF(p) (*(const LDS_AS bf16x8*)(p))
#define ASTEP(SC, SN, VA, VB, VNA, VNB, KO, VO)                              \
  do {                                                                       \
    SN = MFMA32(kf, qf, negm); SBAR();                                       \
    kf = LDF(kp + (KO)); VNA = LDF(vp + (VO)); VNB = LDF(vp + (VO) + 512); SBAR(); \
    { bf16x8 pa = exp_pack8(SC, 0); SBAR(); o = MFMA32(VA, pa, o); SBAR(); } \
    { bf16x8 pb = exp_pack8(SC, 8); SBAR(); o = MFMA32(VB, pb, o); SBAR(); } \
  } while (0)

__device__ __forceinline__ float max16(const f32x16 s) {
  float a = __builtin_fmaxf(__builtin_fmaxf(s[0], s[1]), s[2]);
  float c = __builtin_fmaxf(__builtin_fmaxf(s[3], s[4]), s[5]);
  a = __builtin_fmaxf(__builtin_fmaxf(a, s[6]), s[7]);
  c = __builtin_fmaxf(__builtin_fmaxf(c, s[8]), s[9]);
  a = __builtin_fmaxf(__builtin_fmaxf(a, s[10]), s[11]);
  c = __builtin_fmaxf(__builtin_fmaxf(c, s[12]), s[13]);
  a = __builtin_fmaxf(__builtin_fmaxf(a, s[14]), s[15]);
  return __builtin_fmaxf(a, c);
}
__device__ __forceinline__ float xhalf_max(float m) {
  auto rr = __builtin_amdgcn_permlane32_swap(__float_as_uint(m), __float_as_uint(m), false, false);
  return __builtin_fmaxf(__uint_as_float(rr[0]), __uint_as_float(rr[1]));
}

__global__ __launch_bounds__(1024, 4) void attn_kernel(const bf16_t* __restrict__ Q, const bf16_t* __restrict__ Kf,
                                                       const bf16_t* __restrict__ Vf, bf16_t* __restrict__ O,
                                                       const char* __restrict__ pf0, int pf0_lines,
                                                       const char* __restrict__ pf1, int pf1_lines) {
  extern __shared__ __attribute__((aligned(16))) char smem_g[];
  LDS_AS char* smem = (LDS_AS char*)smem_g;
  const int tid = threadIdx.x, lane = tid & 63;
  const int wave = __builtin_amdgcn_readfirstlane(tid >> 6);
  const int kh = wave >> 3, qw = wave & 7;
  const int b = blockIdx.x, xcd = b & 7, j = b >> 3;
  const int head = xcd * 4 + (j & 3), qt = j >> 2;
  const int r32 = lane & 31, hh = lane >> 5;
  const int q0 = qt * 256 + qw * 32;

  const unsigned lds0 = (unsigned)(uintptr_t)smem;
    const bf16_t* ks = Kf + (size_t)head * 32768 + kh * 16384 + qw * 512 + lane * 8;
    const bf16_t* vs = Vf + (size_t)head * 32768 + kh * 16384 + qw * 512 + lane * 8;
    const unsigned kd = lds0 + A_KOFF + kh * 32768 + qw * 1024, vd = lds0 + A_VOFF + kh * 32768 + qw * 1024;
    glds16(Q + ((size_t)head * SEQ + q0 + r32) * 16 + hh * 8, lds0 + A_QOFF + qw * 1024);
    glds16(ks, kd);
    glds16(vs, vd);

  if (tid < 448) {
    const unsigned v = (tid & 31) == 0 ? 0x3F803F80u : 0u;
    *(LDS_AS u32x4*)(smem + A_ONES + tid * 16) = u32x4{v, v, v, v};
  }

  LDS_AS char* kb = smem + A_KOFF + kh * 32768 + lane * 16;
  const bool real = r32 < 16;
  LDS_AS char* vb = real ? (smem + A_VOFF + kh * 32768 + hh * 256 + r32 * 16) : (smem + A_ONES + (r32 == 16 ? 0 : 16));
  const int vstride = real ? 2048 : 0;

  wait_bar<0>();
  const bf16x8 qf = LDF(smem + A_QOFF + qw * 1024 + lane * 16);
  f32x16 negm;
  float mref;
  {
    f32x16 z = {};
    f32x16 s = MFMA32(LDF(smem + A_KOFF + lane * 16), qf, z);
    mref = xhalf_max(max16(s));
#pragma unroll
    for (int r = 0; r < 16; ++r) negm[r] = -mref;
  }
  f32x16 o = {};
  {
    LDS_AS char* kp = kb;
    LDS_AS char* vp = vb;
    bf16x8 kf = LDF(kp), va0 = LDF(vp), vb0 = LDF(vp + 512), va1, vb1;
    f32x16 sA = MFMA32(kf, qf, negm), sB;
    kf = LDF(kp + 1024);
    SBAR();
#pragma unroll 1
    for (int t = 0; t < 8; t += 2) {
      if (t < 6) {
        if ((wave >> 2) == (t >> 1)) {
          const int c = (t >> 1) + 1, w2 = (wave & 3) * 2;
          const bf16_t* k2 = Kf + (size_t)head * 32768 + c * 4096 + w2 * 512 + lane * 8;
          const bf16_t* v2 = Vf + (size_t)head * 32768 + c * 4096 + w2 * 512 + lane * 8;
          const unsigned d2 = lds0 + c * 8192 + w2 * 1024;
#pragma unroll
          for (int hf = 0; hf < 2; ++hf)
#pragma unroll
            for (int i = 0; i < 2; ++i) {
              glds16(k2 + hf * 16384 + i * 512, d2 + A_KOFF + hf * 32768 + i * 1024);
              glds16(v2 + hf * 16384 + i * 512, d2 + A_VOFF + hf * 32768 + i * 1024);
            }
        }
      } else {
        wait_bar<0>();
      }
      ASTEP(sA, sB, va0, vb0, va1, vb1, 2048, 1024);
      ASTEP(sB, sA, va1, vb1, va0, vb0, 3072, 2048);
      kp += 2048; vp += vstride;
    }
    {
      const int line = j * 1024 + tid;
      const char* pp = line < pf0_lines ? pf0 + (size_t)line * 128 : pf1 + (size_t)(line - pf0_lines) * 128;
      if (line < pf0_lines + pf1_lines) touch(pp, lds0 + A_ONES + 7168);
    }
#pragma unroll 1
    for (int it = 0; it < 6; ++it) {
      {
        const int pr = (it + (wave >> 2)) & 3;
        if (pr == 0) __builtin_amdgcn_s_setprio(0);
        else if (pr == 1) __builtin_amdgcn_s_setprio(1);
        else if (pr == 2) __builtin_amdgcn_s_setprio(2);
        else __builtin_amdgcn_s_setprio(3);
      }
      ASTEP(sA, sB, va0, vb0, va1, vb1, 2048, 1024);
      ASTEP(sB, sA, va1, vb1, va0, vb0, 3072, 2048);
      ASTEP(sA, sB, va0, vb0, va1, vb1, 4096, 3072);
      ASTEP(sB, sA, va1, vb1, va0, vb0, 5120, 4096);
      kp += 4096; vp += 2 * vstride;
    }
    __builtin_amdgcn_s_setprio(0);
  }
  if (__builtin_expect(__any(!(o[8] < 1.2676506e30f)), 0)) {
    float m = -INFINITY;
    f32x16 z = {};
#pragma unroll 1
    for (int t = 0; t < 32; ++t) {
      f32x16 sc = MFMA32(LDF(kb + t * 1024), qf, z);
      m = __builtin_fmaxf(m, max16(sc));
    }
    mref = xhalf_max(m);
#pragma unroll
    for (int r = 0; r < 16; ++r) negm[r] = -mref;
    o = z;
#pragma unroll 1
    for (int t = 0; t < 32; ++t) {
      const f32x16 sc = MFMA32(LDF(kb + t * 1024), qf, negm);
      LDS_AS char* vq = vb + (vstride >> 1) * t;
      o = MFMA32(LDF(vq), exp_pack8(sc, 0), o);
      o = MFMA32(LDF(vq + 512), exp_pack8(sc, 8), o);
    }
  }

  {
    auto rr = __builtin_amdgcn_permlane32_swap(__float_as_uint(o[8]), __float_as_uint(o[8]), false, false);
    o[8] = __uint_as_float(rr[0]);
  }
  LDS_AS float* cb = (LDS_AS float*)(smem + A_COMB + qw * (64 * 40) + lane * 4);
  if (kh) {
#pragma unroll
    for (int r = 0; r < 9; ++r) cb[r * 64] = o[r];
    cb[9 * 64] = mref;
  }
  bar_lgkm();
  if (!kh) {
    const float m2 = cb[9 * 64];
    const float mm = __builtin_fmaxf(mref, m2);
    const float f1 = __builtin_amdgcn_exp2f(mref - mm), f2 = __builtin_amdgcn_exp2f(m2 - mm);
    const float l = o[8] * f1 + cb[8 * 64] * f2;
    const float inv = __builtin_amdgcn_rcpf(l);
    const float a1 = f1 * inv, a2 = f2 * inv;
    float v[8];
#pragma unroll
    for (int r = 0; r < 8; ++r) v[r] = o[r] * a1 + cb[r * 64] * a2;
    bf16_t* op = O + tile_off(q0 + r32, head * 16, DM / 16) + (r32 & 15) * 16 + 4 * hh;
    *(u32x2*)(op) = pack4(v[0], v[1], v[2], v[3]);
    *(u32x2*)(op + 8) = pack4(v[4], v[5], v[6], v[7]);
  }
  asm volatile("s_waitcnt vmcnt(0)" ::: "memory");
}

constexpr int QKV_NST = 5, WO_NST = 6, F1_NST = 5, F2_NST = 6;
#define NL_SMALL 4
#define QKV0_K gemm_kernel<128, 96, 2, 2, 512, 1536, QKV_NST, 0, 1, 4, 0>
#define QKV1_K gemm_kernel<128, 96, 2, 2, 512, 1536, QKV_NST, 0, 0, 4, 1>
#define WO_K gemm_kernel<64, 64, 2, 2, 512, 512, WO_NST, 1, 0, NL_SMALL, 1>
#define F1_K gemm_kernel<128, 112, 4, 1, 512, 1792, F1_NST, 2, 0, 4, 1>
#define F2_K gemm_kernel<64, 64, 2, 2, 1792, 512, F2_NST, 3, 0, NL_SMALL, 1>
constexpr int QKV_LDS = QKV_NST * (128 + 96) * 128 + 2048, WO_LDS = WO_NST * 128 * 128 + 2048,
              F1_LDS = F1_NST * (128 + 112) * 128 + 2048, F2_LDS = F2_NST * 128 * 128 + 2048;

extern "C" void kernel_launch(void* const* d_in, const int* in_sizes, int n_in, void* d_out, int out_size, void* d_ws,
                              size_t ws_size, hipStream_t stream) {
  const bf16_t* x = (const bf16_t*)d_in[0];
  const bf16_t* wqkv = (const bf16_t*)d_in[1];
  const bf16_t* wo = (const bf16_t*)d_in[2];
  const bf16_t* w1 = (const bf16_t*)d_in[4];
  const bf16_t* b1 = (const bf16_t*)d_in[5];
  const bf16_t* w2 = (const bf16_t*)d_in[6];
  const bf16_t* b2 = (const bf16_t*)d_in[7];
  char* ws = (char*)d_ws;
  bf16_t* Qb = (bf16_t*)(ws + (0 << 20));
  bf16_t* Kb = (bf16_t*)(ws + (2 << 20));
  bf16_t* Vb = (bf16_t*)(ws + (4 << 20));
  bf16_t* Ob = (bf16_t*)(ws + (6 << 20));
  bf16_t* X1 = (bf16_t*)(ws + (8 << 20));
  bf16_t* Hb = (bf16_t*)(ws + (10 << 20));
  bf16_t* X2 = (bf16_t*)(ws + (18 << 20));
  float* St1 = (float*)(ws + (20 << 20));
  float* St2 = (float*)(ws + (21 << 20));
  bf16_t* out = (bf16_t*)d_out;

  hipFuncSetAttribute((const void*)QKV0_K, hipFuncAttributeMaxDynamicSharedMemorySize, QKV_LDS);
  hipFuncSetAttribute((const void*)QKV1_K, hipFuncAttributeMaxDynamicSharedMemorySize, QKV_LDS);
  hipFuncSetAttribute((const void*)WO_K, hipFuncAttributeMaxDynamicSharedMemorySize, WO_LDS);
  hipFuncSetAttribute((const void*)F1_K, hipFuncAttributeMaxDynamicSharedMemorySize, F1_LDS);
  hipFuncSetAttribute((const void*)F2_K, hipFuncAttributeMaxDynamicSharedMemorySize, F2_LDS);
  hipFuncSetAttribute((const void*)attn_kernel, hipFuncAttributeMaxDynamicSharedMemorySize, A_LDS);

  for (int layer = 0; layer < 2; ++layer) {
    const bf16_t* xin = layer == 0 ? x : X2;
    bf16_t* xout = layer == 0 ? X2 : out;
    const char* none = (const char*)x;
    if (layer == 0) QKV0_K<<<256, 768, QKV_LDS, stream>>>(xin, wqkv, nullptr, nullptr, Qb, Kb, Vb, nullptr, none, 0, 0);
    else            QKV1_K<<<256, 768, QKV_LDS, stream>>>(xin, wqkv, nullptr, nullptr, Qb, Kb, Vb, St2, none, 0, 0);
    attn_kernel<<<256, 1024, A_LDS, stream>>>(Qb, Kb, Vb, Ob, (const char*)wo, DM * DM * 2 / 128, (const char*)w1, DM * FF * 2 / 128);
    WO_K<<<256, 512 + 64 * NL_SMALL, WO_LDS, stream>>>(Ob, wo, nullptr, xin, X1, nullptr, nullptr, St1, none, 0, (layer ? 1 : 0) | 2);
    F1_K<<<256, 768, F1_LDS, stream>>>(X1, w1, b1, nullptr, Hb, nullptr, nullptr, St1, (const char*)w2, FF * DM * 2 / 128, 0);
    F2_K<<<256, 512 + 64 * NL_SMALL, F2_LDS, stream>>>(Hb, w2, b2, X1, xout, nullptr, nullptr, St2, (const char*)wqkv,
                                       layer == 0 ? DM * 3 * DM * 2 / 128 : 0, 1 | (layer ? 0 : 2));
  }
}
```

```cpp
#include <hip/hip_runtime.h>
#include <hip/hip_bf16.h>
#include <stdint.h>

typedef __bf16 bf16_t;
typedef __bf16 bf16x2 __attribute__((ext_vector_type(2)));
typedef __bf16 bf16x4 __attribute__((ext_vector_type(4)));
typedef __bf16 bf16x8 __attribute__((ext_vector_type(8)));
typedef float f32x2 __attribute__((ext_vector_type(2)));
typedef float f32x4 __attribute__((ext_vector_type(4)));
typedef float f32x16 __attribute__((ext_vector_type(16)));
typedef unsigned u32x2 __attribute__((ext_vector_type(2)));
typedef unsigned u32x4 __attribute__((ext_vector_type(4)));

#define LDS_AS __attribute__((address_space(3)))
#define GLB_AS __attribute__((address_space(1)))

constexpr int SEQ = 2048, DM = 512, NH = 32, DH = 16, FF = 1792;

__device__ __forceinline__ void glds16(const void* g, unsigned lds_base) {
  unsigned keep;
  asm volatile("s_mov_b32 %0, m0\n\ts_mov_b32 m0, %2\n\ts_nop 0\n\tglobal_load_lds_dwordx4 %1, off\n\ts_mov_b32 m0, %0"
               : "=&s"(keep) : "v"(g), "s"(lds_base) : "memory");
}
template <int IMM>
__device__ __forceinline__ void glds16s(unsigned voff, const void* sbase, unsigned lds_base) {
  asm volatile("s_add_i32 m0, %2, %3\n\ts_nop 0\n\tglobal_load_lds_dwordx4 %0, %1"
               :: "v"(voff), "s"(sbase), "s"(lds_base), "i"(IMM) : "memory", "scc");
}
__device__ __forceinline__ void touch(const void* p, unsigned lds_dummy) {
  unsigned keep;
  asm volatile("s_mov_b32 %0, m0\n\ts_mov_b32 m0, %2\n\ts_nop 0\n\tglobal_load_lds_dword %1, off\n\ts_mov_b32 m0, %0"
               : "=&s"(keep) : "v"(p), "s"(lds_dummy) : "memory");
}
#define ST8(ptr, val) (*(u32x2*)(ptr) = (val))
#define SBAR() __builtin_amdgcn_sched_barrier(0)
#define PIN(x) asm volatile("" : "+v"(x))
__device__ __forceinline__ int tile_off(int tok, int col, int nc16) { return ((tok >> 4) * nc16 + (col >> 4)) * 256; }
__device__ __forceinline__ float rbf(float v) { return (float)(__bf16)v; }
__device__ __forceinline__ u32x2 pack4(float a, float b, float c, float d) {
  f32x2 lo = {a, b}, hi = {c, d};
  bf16x2 l = __builtin_convertvector(lo, bf16x2), h = __builtin_convertvector(hi, bf16x2);
  u32x2 r = {__builtin_bit_cast(unsigned, l), __builtin_bit_cast(unsigned, h)};
  return r;
}
__device__ __forceinline__ float bf_lo(unsigned u) { return __builtin_bit_cast(float, u << 16); }
__device__ __forceinline__ float bf_hi(unsigned u) { return __builtin_bit_cast(float, u & 0xffff0000u); }

#define GST(i)
#define GST_DECL
#define GST_FLUSH
template <int N>
__device__ __forceinline__ void wait_bar() {
  asm volatile("s_waitcnt vmcnt(%0) lgkmcnt(0)\n\ts_barrier" ::"i"(N) : "memory");
}

template <int I> struct IC { static constexpr int value = I; };
template <int I, int N, class F>
__device__ __forceinline__ void static_for(F& f) {
  if constexpr (I < N) { f(IC<I>{}); static_for<I + 1, N>(f); }
}

__device__ __forceinline__ float gelu_tanh(float x) {
  const float c1 = -2.0f * 0.7978845608028654f * 1.4426950408889634f;
  const float c2 = c1 * 0.044715f;
  float t = x * __builtin_fmaf(x * x, c2, c1);
  float e = __builtin_amdgcn_exp2f(t);
  return x * __builtin_amdgcn_rcpf(1.0f + e);
}

template <int N>
__device__ __forceinline__ void wait_vm() { asm volatile("s_waitcnt vmcnt(%0)" ::"i"(N) : "memory"); }
__device__ __forceinline__ void bar_lgkm() { asm volatile("s_waitcnt lgkmcnt(0)\n\ts_barrier" ::: "memory"); }
#define MFMA16(a, b, c) __builtin_amdgcn_mfma_f32_16x16x32_bf16(a, b, c, 0, 0, 0)
__device__ __forceinline__ float sum_g(float v) {
  auto a = __builtin_amdgcn_permlane16_swap(__float_as_uint(v), __float_as_uint(v), false, false);
  v = __uint_as_float(a[0]) + __uint_as_float(a[1]);
  auto b = __builtin_amdgcn_permlane32_swap(__float_as_uint(v), __float_as_uint(v), false, false);
  return __uint_as_float(b[0]) + __uint_as_float(b[1]);
}

template <int BM, int BN, int WVM, int WVN, int K, int LDW, int NST, int MODE, int SELF, int NL, int XT>
__global__ __launch_bounds__(512 + 64 * NL, 2 + NL / 4) void gemm_kernel(const bf16_t* __restrict__ X, const bf16_t* __restrict__ W,
                                                      const bf16_t* __restrict__ bias, const bf16_t* __restrict__ res,
                                                      bf16_t* __restrict__ out0, bf16_t* __restrict__ out1,
                                                      bf16_t* __restrict__ out2, float* __restrict__ stats,
                                                      const char* __restrict__ pf, int pf_lines, int flags) {
  extern __shared__ __attribute__((aligned(16))) char smem_g[];
  LDS_AS char* smem = (LDS_AS char*)smem_g;
  constexpr int TM = BM / WVM / 16, TN = BN / WVN / 16, NB = BN / 16, HM = TM / 2;
  constexpr int XB = BM * 128, WB = BN * 128, STB = XB + WB;
  constexpr int NXI = BM / 8, NI = (BM + BN) / 8, LPL = (NI + NL - 1) / NL, NK = K / 64;
  constexpr int NFULL = NI - NL * (LPL - 1);
#define DEP_BIG 2
#define DEP_SMALL 3
  constexpr int DEP = (BM == 128) ? DEP_BIG : DEP_SMALL;
  static_assert(NST >= DEP + 1, "ring too small");
  constexpr bool LN = (MODE == 0 || MODE == 2);
  constexpr int PF_AT = (NK - 4);
  constexpr int NX4 = HM * TN;
  constexpr int EX_OFF = NST * STB;
  constexpr int CS_OFF = EX_OFF;
  constexpr int ST_OFF = CS_OFF + 512;
  constexpr int PF_OFF = ST_OFF + 1024;
  constexpr int BI_OFF = PF_OFF + 256;
  static_assert(8 * NX4 * 1024 <= ((NK - 1) % NST) * STB, "exchange area overlaps the last stage");

  const int tid = threadIdx.x, lane = tid & 63;
  const int wave = __builtin_amdgcn_readfirstlane(tid >> 6);
  const int b = blockIdx.x, xcd = b & 7, j = b >> 3;
  int mt, nt;
  if (BM == 128) { mt = xcd * 2 + (j & 1); nt = j >> 1; }
  else           { mt = xcd * 4 + (j & 3); nt = j >> 2; }
  GST_DECL; GST(0);
  const int m0 = mt * BM;
  const int l15 = lane & 15, g = lane >> 4;
  const int q_ = (lane & 15) >> 2, p_ = lane & 3;
  const __bf16 one = (__bf16)1.0f;
  const bf16x8 ones8 = {one, one, one, one, one, one, one, one};

  auto colbase = [&](int nb) -> int {
    if (MODE == 0) return (nb >> 1) * 512 + (nt * 2 + (nb & 1)) * 16;
    return nt * BN + nb * 16;
  };
  auto tr8 = [&](LDS_AS char* p0, LDS_AS char* p1) -> bf16x8 {
    bf16x4 a = __builtin_amdgcn_ds_read_tr16_b64_v4bf16((LDS_AS bf16x4*)p0);
    bf16x4 c = __builtin_amdgcn_ds_read_tr16_b64_v4bf16((LDS_AS bf16x4*)p1);
    return __builtin_shufflevector(a, c, 0, 1, 2, 3, 4, 5, 6, 7);
  };

  if (wave >= 8) {
    const int lw = wave - 8;
    constexpr int XS = NXI / NL;
    unsigned voff[LPL];
    {
      const int row = 8 * lw + (lane >> 3);
      const int ch = (lane & 7) ^ ((row >> 1) & 7);
      if (XT) voff[0] = (unsigned)((lw >> 1) * (K / 16) * 512 + (lw & 1) * 1024 + lane * 16);
      else    voff[0] = (unsigned)(row * K + ch * 8) * 2u;
#pragma unroll
      for (int i = 1; i < XS; ++i) voff[i] = voff[0] + i * (8 * NL) * K * 2;
#pragma unroll
      for (int i = XS; i < LPL; ++i) {
        const int sidx = 4 * (lw + NL * (i - XS)) + (lane >> 4);
        const int kb = sidx / NB, nb = sidx % NB;
        const int krow = kb * 8 + (((lane >> 1) & 7) ^ ((kb & 1) << 2));
        voff[i] = (unsigned)(krow * LDW + colbase(nb) + (lane & 1) * 8) * 2u;
      }
    }
    const char* xs_ = (const char*)(X + (size_t)m0 * K);
    const char* ws_ = (const char*)W;
    const unsigned lds0 = (unsigned)(uintptr_t)smem + lw * 1024;
    const bool full = lw < NFULL;
    auto issue = [&](auto ST) {
      constexpr int st = decltype(ST)::value;
      auto one_ = [&](auto I) {
        constexpr int i = decltype(I)::value;
        if (i < LPL - 1 || NFULL == NL || full)
          glds16s<(st % NST) * STB + i * (1024 * NL)>(voff[i], i < XS ? xs_ + st * (XT ? 2048 : 128) : ws_ + (size_t)st * (64 * LDW * 2), lds0);
      };
      static_for<0, LPL>(one_);
    };
    static_for<0, (DEP < NK ? DEP : NK)>(issue);

    const int cnb0 = lw, cnb1 = (lw + 4 < NB) ? lw + 4 : NB - 1;
    const int wo0 = XB + g * NB * 256 + (q_ + 4 * (g & 1)) * 32 + p_ * 8;
    const int wo1 = XB + g * NB * 256 + (q_ + 4 * (1 - (g & 1))) * 32 + p_ * 8;
    const int xo = (lw * 32 + l15) * 128 + ((g ^ (l15 >> 1)) * 16);
    f32x4 cs0 = {0.f, 0.f, 0.f, 0.f}, cs1 = {0.f, 0.f, 0.f, 0.f};
    f32x4 rs[2], rq[2];
    rs[0] = rs[1] = rq[0] = rq[1] = f32x4{0.f, 0.f, 0.f, 0.f};
    auto lstep = [&](auto KT) {
      constexpr int kt = decltype(KT)::value;
      constexpr int last = (kt + DEP - 1 < NK - 1) ? kt + DEP - 1 : NK - 1;
      if (NFULL == NL || full) wait_vm<(last - kt) * LPL>(); else wait_vm<(last - kt) * (LPL - 1)>();
      bar_lgkm();
      if constexpr (kt + DEP < NK) issue(IC<kt + DEP>{});
      if (LN) {
        LDS_AS char* base = smem + (kt % NST) * STB;
#pragma unroll
        for (int ks = 0; ks < 2; ++ks) {
          const int o0 = (ks * 4 * NB + cnb0) * 256, o1 = (ks * 4 * NB + cnb1) * 256;
          cs0 = MFMA16(tr8(base + wo0 + o0, base + wo1 + o0), ones8, cs0);
          cs1 = MFMA16(tr8(base + wo0 + o1, base + wo1 + o1), ones8, cs1);
          if (SELF) {
#pragma unroll
            for (int mi = 0; mi < 2; ++mi) {
              const bf16x8 xf = *(const LDS_AS bf16x8*)(base + ((xo ^ (ks * 64)) + mi * 2048));
              rs[mi] = MFMA16(ones8, xf, rs[mi]);
              rq[mi] = MFMA16(xf, xf, rq[mi]);
            }
          }
        }
      }
    };
    static_for<0, NK>(lstep);
    if (LN) {
      if (l15 == 0) {
        *(LDS_AS f32x4*)(smem + CS_OFF + cnb0 * 64 + g * 16) = cs0;
        *(LDS_AS f32x4*)(smem + CS_OFF + cnb1 * 64 + g * 16) = cs1;
      }
      if (SELF) {
        if (g == (l15 >> 2)) {
          const int r = l15 & 3;
#pragma unroll
          for (int mi = 0; mi < 2; ++mi) {
            const float sq = r == 0 ? rq[mi][0] : r == 1 ? rq[mi][1] : r == 2 ? rq[mi][2] : rq[mi][3];
            const float m = rs[mi][0] * (1.0f / K);
            const float v = __builtin_fmaxf(sq * (1.0f / K) - m * m, 0.f);
            *(LDS_AS f32x2*)(smem + ST_OFF + (lw * 32 + mi * 16 + l15) * 8) = f32x2{m, __builtin_amdgcn_rsqf(v + 1e-5f)};
          }
        }
      }
    }
    bar_lgkm();
    return;
  }

  const int kg = wave >> 2, w4 = wave & 3;
  const int wm = w4 / WVN, wn = w4 % WVN;
  auto nb_of = [&](int ni) -> int {
    if (MODE == 0) return 2 * ni + wn;
    return wn * TN + ni;
  };
  const int tl0 = wm * TM * 16 + kg * HM * 16;
  const int tok0 = m0 + tl0;

  if (LN && !SELF) {
    if (wave < 2) {
      float sm = 0.f, sq = 0.f;
#pragma unroll
      for (int i = 0; i < 8; ++i) {
        const f32x4 t = *(const f32x4*)(stats + (size_t)(m0 + tid) * 32 + i * 4);
        sm += t[0] + t[2]; sq += t[1] + t[3];
      }
      const float m = sm * (1.0f / K);
      const float v = __builtin_fmaxf(sq * (1.0f / K) - m * m, 0.f);
      *(LDS_AS f32x2*)(smem + ST_OFF + tid * 8) = f32x2{m, __builtin_amdgcn_rsqf(v + 1e-5f)};
    }
  }
  if (MODE == 2) {
    if (wave == 2 && lane < BN / 4) *(LDS_AS u32x2*)(smem + BI_OFF + lane * 8) = *(const u32x2*)(bias + nt * BN + lane * 4);
  }
  u32x2 resv[HM][TN];
  u32x2 biasv[TN];
  if (MODE == 1 || MODE == 3) {
#pragma unroll
    for (int mi = 0; mi < HM; ++mi)
#pragma unroll
      for (int ni = 0; ni < TN; ++ni)
        resv[mi][ni] = *(const u32x2*)(res + ((flags & 1) ? tile_off(tok0 + mi * 16, colbase(nb_of(ni)), DM / 16) + l15 * 16 + 4 * g
                                                             : (tok0 + mi * 16 + l15) * DM + colbase(nb_of(ni)) + 4 * g));
  }
  if (MODE == 3) {
#pragma unroll
    for (int ni = 0; ni < TN; ++ni) biasv[ni] = *(const u32x2*)(bias + colbase(nb_of(ni)) + 4 * g);
  }

  const int xo_ = XT ? wm * TM * 2048 + (kg * 2 + (g >> 1)) * 512 + l15 * 32 + (g & 1) * 16
                     : ((wm * TM * 16 + l15) * 128 + ((g ^ (l15 >> 1)) * 16)) ^ (kg * 64);
  const int xoA = xo_ + kg * HM * 2048, xoB = xo_ + (1 - kg) * HM * 2048;
  const int wo0 = XB + (kg * 4 + g) * NB * 256 + (q_ + 4 * (g & 1)) * 32 + p_ * 8 + nb_of(0) * 256;
  const int wo1 = XB + (kg * 4 + g) * NB * 256 + (q_ + 4 * (1 - (g & 1))) * 32 + p_ * 8 + nb_of(0) * 256;
  constexpr int NSTEP = (MODE == 0) ? 512 : 256;

  f32x4 acc[TM][TN];
#pragma unroll
  for (int mi = 0; mi < TM; ++mi)
#pragma unroll
    for (int ni = 0; ni < TN; ++ni) acc[mi][ni] = f32x4{0.f, 0.f, 0.f, 0.f};

  bf16x8 xf[2][TM], wf[2][TN];
  auto ldfrags = [&](auto KT) {
    constexpr int kt = decltype(KT)::value, p = kt & 1;
    LDS_AS char* base = smem + (kt % NST) * STB;
#pragma unroll
    for (int mi = 0; mi < TM; ++mi) xf[p][mi] = *(const LDS_AS bf16x8*)(base + (mi < HM ? xoA + mi * 2048 : xoB + (mi - HM) * 2048));
#pragma unroll
    for (int ni = 0; ni < TN; ++ni) wf[p][ni] = tr8(base + wo0 + ni * NSTEP, base + wo1 + ni * NSTEP);
  };
  GST(1);
  bar_lgkm();
  GST(2);
  ldfrags(IC<0>{});
#define LATE_N 100
  auto kstep = [&](auto KT) {
    constexpr int kt = decltype(KT)::value, p = kt & 1;
    constexpr bool late = (kt + 1 < NK) && (kt + 1 >= NK - LATE_N);
    if constexpr (kt + 1 < NK && !late) {
      bar_lgkm();
      ldfrags(IC<kt + 1>{});
    }
    if constexpr (kt == PF_AT) {
      const int line = j * 512 + tid;
      if (line < pf_lines) touch(pf + (size_t)line * 128, (unsigned)(uintptr_t)smem + PF_OFF);
    }
    SBAR();
#pragma unroll
    for (int mj = 0; mj < TM; ++mj)
#pragma unroll
      for (int ni = 0; ni < TN; ++ni) {
        const int mi = (mj + HM) % TM;
        if (MODE == 0 && ni == 2) acc[mi][ni] = MFMA16(xf[p][mi], wf[p][ni], acc[mi][ni]);
        else                      acc[mi][ni] = MFMA16(wf[p][ni], xf[p][mi], acc[mi][ni]);
      }
    SBAR();
    if constexpr (late) {
      bar_lgkm();
      ldfrags(IC<kt + 1>{});
      SBAR();
    }
  };
  static_for<0, NK>(kstep);

  GST(3);
  {
    LDS_AS f32x4* wr = (LDS_AS f32x4*)(smem + wave * (NX4 * 1024) + lane * 16);
#pragma unroll
    for (int mi = 0; mi < HM; ++mi)
#pragma unroll
      for (int ni = 0; ni < TN; ++ni) wr[(mi * TN + ni) * 64] = acc[HM + mi][ni];
  }
  GST(4);
  bar_lgkm();
  GST(5);
  f32x4 fa[HM][TN];
  {
    const LDS_AS f32x4* rd = (const LDS_AS f32x4*)(smem + (wave ^ 4) * (NX4 * 1024) + lane * 16);
#pragma unroll
    for (int mi = 0; mi < HM; ++mi)
#pragma unroll
      for (int ni = 0; ni < TN; ++ni) fa[mi][ni] = acc[mi][ni] + rd[(mi * TN + ni) * 64];
  }
  auto tok_stats = [&](int tl) -> f32x2 { return *(const LDS_AS f32x2*)(smem + ST_OFF + tl * 8); };
  auto col_sum4 = [&](int nb) -> f32x4 { return *(const LDS_AS f32x4*)(smem + CS_OFF + nb * 64 + g * 16); };

  if (MODE == 0) {
    const int head = nt * 2 + wn;
    const float qscale = 0.25f * 1.4426950408889634f;
    const f32x4 cq = col_sum4(nb_of(0)), ck = col_sum4(nb_of(1));
    const float cv = *(const LDS_AS float*)(smem + CS_OFF + nb_of(2) * 64 + l15 * 4);
#pragma unroll
    for (int mi = 0; mi < HM; ++mi) {
      const int tok = tok0 + mi * 16 + l15;
      const f32x2 st = tok_stats(tl0 + mi * 16 + l15);
      {
        const float sc = st[1] * qscale;
        float v[4];
#pragma unroll
        for (int r = 0; r < 4; ++r) v[r] = sc * __builtin_fmaf(-st[0], cq[r], fa[mi][0][r]);
        ST8(out0 + ((size_t)head * SEQ + tok) * 16 + 4 * g, pack4(v[0], v[1], v[2], v[3]));
      }
      {
        float v[4];
#pragma unroll
        for (int r = 0; r < 4; ++r) v[r] = st[1] * __builtin_fmaf(-st[0], ck[r], fa[mi][1][r]);
        ST8(out1 + (size_t)head * 32768 + (tok >> 5) * 512 + (g >> 1) * 256 + (tok & 31) * 8 + (g & 1) * 4,
            pack4(v[0], v[1], v[2], v[3]));
      }
      {
        float v[4];
#pragma unroll
        for (int r = 0; r < 4; ++r) {
          const f32x2 sr = tok_stats(tl0 + mi * 16 + 4 * g + r);
          v[r] = sr[1] * __builtin_fmaf(-sr[0], cv, fa[mi][2][r]);
        }
        const int ks16 = (tok0 + mi * 16) >> 4;
        ST8(out2 + (size_t)head * 32768 + ks16 * 256 + (g & 1) * 128 + l15 * 8 + (g >> 1) * 4,
            pack4(v[0], v[1], v[2], v[3]));
      }
    }
  } else if (MODE == 2) {
#pragma unroll
    for (int mi = 0; mi < HM; ++mi) {
      const int tok = tok0 + mi * 16 + l15;
      const f32x2 st = tok_stats(tl0 + mi * 16 + l15);
      const float nrm = -st[0] * st[1];
#pragma unroll
      for (int ni = 0; ni < TN; ++ni) {
        const f32x4 cc = col_sum4(nb_of(ni));
        const u32x2 bv = *(const LDS_AS u32x2*)(smem + BI_OFF + ni * 32 + g * 8);
        const float bb[4] = {bf_lo(bv[0]), bf_hi(bv[0]), bf_lo(bv[1]), bf_hi(bv[1])};
        float v[4];
#pragma unroll
        for (int r = 0; r < 4; ++r)
          v[r] = gelu_tanh(__builtin_fmaf(st[1], fa[mi][ni][r], __builtin_fmaf(nrm, cc[r], bb[r])));
        ST8(out0 + tile_off(tok0 + mi * 16, colbase(nb_of(ni)), FF / 16) + l15 * 16 + 4 * g, pack4(v[0], v[1], v[2], v[3]));
      }
    }
  } else {
#pragma unroll
    for (int mi = 0; mi < HM; ++mi) {
      const int tok = tok0 + mi * 16 + l15;
      float sm = 0.f, sq = 0.f;
#pragma unroll
      for (int ni = 0; ni < TN; ++ni) {
        float rr[4] = {bf_lo(resv[mi][ni][0]), bf_hi(resv[mi][ni][0]), bf_lo(resv[mi][ni][1]), bf_hi(resv[mi][ni][1])};
        if (MODE == 3) {
          rr[0] += bf_lo(biasv[ni][0]); rr[1] += bf_hi(biasv[ni][0]); rr[2] += bf_lo(biasv[ni][1]); rr[3] += bf_hi(biasv[ni][1]);
        }
        const u32x2 pk = pack4(rr[0] + fa[mi][ni][0], rr[1] + fa[mi][ni][1], rr[2] + fa[mi][ni][2], rr[3] + fa[mi][ni][3]);
        ST8(out0 + ((flags & 2) ? tile_off(tok0 + mi * 16, colbase(nb_of(ni)), DM / 16) + l15 * 16 + 4 * g
                                : tok * DM + colbase(nb_of(ni)) + 4 * g), pk);
        const float w[4] = {bf_lo(pk[0]), bf_hi(pk[0]), bf_lo(pk[1]), bf_hi(pk[1])};
#pragma unroll
        for (int r = 0; r < 4; ++r) { sm += w[r]; sq = __builtin_fmaf(w[r], w[r], sq); }
      }
      sm = sum_g(sm); sq = sum_g(sq);
      if (g == 0) *(f32x2*)(stats + (size_t)tok * 32 + (nt * 2 + wn) * 2) = f32x2{sm, sq};
    }
  }
  GST(6);
  asm volatile("s_waitcnt vmcnt(0)" ::: "memory");
  GST(7);
  GST_FLUSH;
}

constexpr int A_KOFF = 0, A_VOFF = 65536, A_ONES = 131072, A_QOFF = 139264, A_COMB = A_QOFF, A_LDS = A_COMB + 8 * 64 * 40;

#define MFMA32(a, b, c) __builtin_amdgcn_mfma_f32_32x32x16_bf16(a, b, c, 0, 0, 0)
__device__ __forceinline__ bf16x8 exp_pack8(const f32x16& s, int o) {
  bf16x8 p;
#pragma unroll
  for (int r = 0; r < 8; r += 2) {
    f32x2 e = {__builtin_amdgcn_exp2f(s[o + r]), __builtin_amdgcn_exp2f(s[o + r + 1])};
    bf16x2 c = __builtin_convertvector(e, bf16x2);
    p[r] = c[0]; p[r + 1] = c[1];
  }
  return p;
}
#define LDF(p) (*(const LDS_AS bf16x8*)(p))
#define ASTEP(SC, SN, VA, VB, VNA, VNB, KO, VO)                              \
  do {                                                                       \
    SN = MFMA32(kf, qf, negm); SBAR();                                       \
    kf = LDF(kp + (KO)); VNA = LDF(vp + (VO)); VNB = LDF(vp + (VO) + 512); SBAR(); \
    { bf16x8 pa = exp_pack8(SC, 0); SBAR(); o = MFMA32(VA, pa, o); SBAR(); } \
    { bf16x8 pb = exp_pack8(SC, 8); SBAR(); o = MFMA32(VB, pb, o); SBAR(); } \
  } while (0)

__device__ __forceinline__ float max16(const f32x16 s) {
  float a = __builtin_fmaxf(__builtin_fmaxf(s[0], s[1]), s[2]);
  float c = __builtin_fmaxf(__builtin_fmaxf(s[3], s[4]), s[5]);
  a = __builtin_fmaxf(__builtin_fmaxf(a, s[6]), s[7]);
  c = __builtin_fmaxf(__builtin_fmaxf(c, s[8]), s[9]);
  a = __builtin_fmaxf(__builtin_fmaxf(a, s[10]), s[11]);
  c = __builtin_fmaxf(__builtin_fmaxf(c, s[12]), s[13]);
  a = __builtin_fmaxf(__builtin_fmaxf(a, s[14]), s[15]);
  return __builtin_fmaxf(a, c);
}
__device__ __forceinline__ float xhalf_max(float m) {
  auto rr = __builtin_amdgcn_permlane32_swap(__float_as_uint(m), __float_as_uint(m), false, false);
  return __builtin_fmaxf(__uint_as_float(rr[0]), __uint_as_float(rr[1]));
}

__global__ __launch_bounds__(1024, 4) void attn_kernel(const bf16_t* __restrict__ Q, const bf16_t* __restrict__ Kf,
                                                       const bf16_t* __restrict__ Vf, bf16_t* __restrict__ O,
                                                       const char* __restrict__ pf0, int pf0_lines,
                                                       const char* __restrict__ pf1, int pf1_lines) {
  extern __shared__ __attribute__((aligned(16))) char smem_g[];
  LDS_AS char* smem = (LDS_AS char*)smem_g;
  const int tid = threadIdx.x, lane = tid & 63;
  const int wave = __builtin_amdgcn_readfirstlane(tid >> 6);
  const int kh = wave >> 3, qw = wave & 7;
  const int b = blockIdx.x, xcd = b & 7, j = b >> 3;
  const int head = xcd * 4 + (j & 3), qt = j >> 2;
  const int r32 = lane & 31, hh = lane >> 5;
  const int q0 = qt * 256 + qw * 32;

  const unsigned lds0 = (unsigned)(uintptr_t)smem;
    const bf16_t* ks = Kf + (size_t)head * 32768 + kh * 16384 + qw * 512 + lane * 8;
    const bf16_t* vs = Vf + (size_t)head * 32768 + kh * 16384 + qw * 512 + lane * 8;
    const unsigned kd = lds0 + A_KOFF + kh * 32768 + qw * 1024, vd = lds0 + A_VOFF + kh * 32768 + qw * 1024;
    glds16(Q + ((size_t)head * SEQ + q0 + r32) * 16 + hh * 8, lds0 + A_QOFF + qw * 1024);
    glds16(ks, kd);
    glds16(vs, vd);

  if (tid < 448) {
    const unsigned v = (tid & 31) == 0 ? 0x3F803F80u : 0u;
    *(LDS_AS u32x4*)(smem + A_ONES + tid * 16) = u32x4{v, v, v, v};
  }

  LDS_AS char* kb = smem + A_KOFF + kh * 32768 + lane * 16;
  const bool real = r32 < 16;
  LDS_AS char* vb = real ? (smem + A_VOFF + kh * 32768 + hh * 256 + r32 * 16) : (smem + A_ONES + (r32 == 16 ? 0 : 16));
  const int vstride = real ? 2048 : 0;

  wait_bar<0>();
  const bf16x8 qf = LDF(smem + A_QOFF + qw * 1024 + lane * 16);
  f32x16 negm;
  float mref;
  {
    f32x16 z = {};
    f32x16 s = MFMA32(LDF(smem + A_KOFF + lane * 16), qf, z);
    mref = xhalf_max(max16(s));
#pragma unroll
    for (int r = 0; r < 16; ++r) negm[r] = -mref;
  }
  f32x16 o = {};
  {
    LDS_AS char* kp = kb;
    LDS_AS char* vp = vb;
    bf16x8 kf = LDF(kp), va0 = LDF(vp), vb0 = LDF(vp + 512), va1, vb1;
    f32x16 sA = MFMA32(kf, qf, negm), sB;
    kf = LDF(kp + 1024);
    SBAR();
#pragma unroll 1
    for (int t = 0; t < 8; t += 2) {
      if (t < 6) {
        if ((wave >> 2) == (t >> 1)) {
          const int c = (t >> 1) + 1, w2 = (wave & 3) * 2;
          const bf16_t* k2 = Kf + (size_t)head * 32768 + c * 4096 + w2 * 512 + lane * 8;
          const bf16_t* v2 = Vf + (size_t)head * 32768 + c * 4096 + w2 * 512 + lane * 8;
          const unsigned d2 = lds0 + c * 8192 + w2 * 1024;
#pragma unroll
          for (int hf = 0; hf < 2; ++hf)
#pragma unroll
            for (int i = 0; i < 2; ++i) {
              glds16(k2 + hf * 16384 + i * 512, d2 + A_KOFF + hf * 32768 + i * 1024);
              glds16(v2 + hf * 16384 + i * 512, d2 + A_VOFF + hf * 32768 + i * 1024);
            }
        }
      } else {
        wait_bar<0>();
      }
      ASTEP(sA, sB, va0, vb0, va1, vb1, 2048, 1024);
      ASTEP(sB, sA, va1, vb1, va0, vb0, 3072, 2048);
      kp += 2048; vp += vstride;
    }
    {
      const int line = j * 1024 + tid;
      const char* pp = line < pf0_lines ? pf0 + (size_t)line * 128 : pf1 + (size_t)(line - pf0_lines) * 128;
      if (line < pf0_lines + pf1_lines) touch(pp, lds0 + A_ONES + 7168);
    }
#pragma unroll 1
    for (int it = 0; it < 6; ++it) {
      {
        const int pr = (it + (wave >> 2)) & 3;
        if (pr == 0) __builtin_amdgcn_s_setprio(0);
        else if (pr == 1) __builtin_amdgcn_s_setprio(1);
        else if (pr == 2) __builtin_amdgcn_s_setprio(2);
        else __builtin_amdgcn_s_setprio(3);
      }
      ASTEP(sA, sB, va0, vb0, va1, vb1, 2048, 1024);
      ASTEP(sB, sA, va1, vb1, va0, vb0, 3072, 2048);
      ASTEP(sA, sB, va0, vb0, va1, vb1, 4096, 3072);
      ASTEP(sB, sA, va1, vb1, va0, vb0, 5120, 4096);
      kp += 4096; vp += 2 * vstride;
    }
    __builtin_amdgcn_s_setprio(0);
  }
  if (__builtin_expect(__any(!(o[8] < 1.2676506e30f)), 0)) {
    float m = -INFINITY;
    f32x16 z = {};
#pragma unroll 1
    for (int t = 0; t < 32; ++t) {
      f32x16 sc = MFMA32(LDF(kb + t * 1024), qf, z);
      m = __builtin_fmaxf(m, max16(sc));
    }
    mref = xhalf_max(m);
#pragma unroll
    for (int r = 0; r < 16; ++r) negm[r] = -mref;
    o = z;
#pragma unroll 1
    for (int t = 0; t < 32; ++t) {
      const f32x16 sc = MFMA32(LDF(kb + t * 1024), qf, negm);
      LDS_AS char* vq = vb + (vstride >> 1) * t;
      o = MFMA32(LDF(vq), exp_pack8(sc, 0), o);
      o = MFMA32(LDF(vq + 512), exp_pack8(sc, 8), o);
    }
  }

  {
    auto rr = __builtin_amdgcn_permlane32_swap(__float_as_uint(o[8]), __float_as_uint(o[8]), false, false);
    o[8] = __uint_as_float(rr[0]);
  }
  LDS_AS float* cb = (LDS_AS float*)(smem + A_COMB + qw * (64 * 40) + lane * 4);
  if (kh) {
#pragma unroll
    for (int r = 0; r < 9; ++r) cb[r * 64] = o[r];
    cb[9 * 64] = mref;
  }
  bar_lgkm();
  if (!kh) {
    const float m2 = cb[9 * 64];
    const float mm = __builtin_fmaxf(mref, m2);
    const float f1 = __builtin_amdgcn_exp2f(mref - mm), f2 = __builtin_amdgcn_exp2f(m2 - mm);
    const float l = o[8] * f1 + cb[8 * 64] * f2;
    const float inv = __builtin_amdgcn_rcpf(l);
    const float a1 = f1 * inv, a2 = f2 * inv;
    float v[8];
#pragma unroll
    for (int r = 0; r < 8; ++r) v[r] = o[r] * a1 + cb[r * 64] * a2;
    bf16_t* op = O + tile_off(q0 + r32, head * 16, DM / 16) + (r32 & 15) * 16 + 4 * hh;
    *(u32x2*)(op) = pack4(v[0], v[1], v[2], v[3]);
    *(u32x2*)(op + 8) = pack4(v[4], v[5], v[6], v[7]);
  }
  asm volatile("s_waitcnt vmcnt(0)" ::: "memory");
}

constexpr int QKV_NST = 5, WO_NST = 6, F1_NST = 5, F2_NST = 6;
#define NL_SMALL 4
#define QKV0_K gemm_kernel<128, 96, 2, 2, 512, 1536, QKV_NST, 0, 1, 4, 0>
#define QKV1_K gemm_kernel<128, 96, 2, 2, 512, 1536, QKV_NST, 0, 0, 4, 1>
#define WO_K gemm_kernel<64, 64, 2, 2, 512, 512, WO_NST, 1, 0, NL_SMALL, 1>
#define F1_K gemm_kernel<128, 112, 4, 1, 512, 1792, F1_NST, 2, 0, 4, 1>
#define F2_K gemm_kernel<64, 64, 2, 2, 1792, 512, F2_NST, 3, 0, NL_SMALL, 1>
constexpr int QKV_LDS = QKV_NST * (128 + 96) * 128 + 2048, WO_LDS = WO_NST * 128 * 128 + 2048,
              F1_LDS = F1_NST * (128 + 112) * 128 + 2048, F2_LDS = F2_NST * 128 * 128 + 2048;

extern "C" void kernel_launch(void* const* d_in, const int* in_sizes, int n_in, void* d_out, int out_size, void* d_ws,
                              size_t ws_size, hipStream_t stream) {
  const bf16_t* x = (const bf16_t*)d_in[0];
  const bf16_t* wqkv = (const bf16_t*)d_in[1];
  const bf16_t* wo = (const bf16_t*)d_in[2];
  const bf16_t* w1 = (const bf16_t*)d_in[4];
  const bf16_t* b1 = (const bf16_t*)d_in[5];
  const bf16_t* w2 = (const bf16_t*)d_in[6];
  const bf16_t* b2 = (const bf16_t*)d_in[7];
  char* ws = (char*)d_ws;
  bf16_t* Qb = (bf16_t*)(ws + (0 << 20));
  bf16_t* Kb = (bf16_t*)(ws + (2 << 20));
  bf16_t* Vb = (bf16_t*)(ws + (4 << 20));
  bf16_t* Ob = (bf16_t*)(ws + (6 << 20));
  bf16_t* X1 = (bf16_t*)(ws + (8 << 20));
  bf16_t* Hb = (bf16_t*)(ws + (10 << 20));
  bf16_t* X2 = (bf16_t*)(ws + (18 << 20));
  float* St1 = (float*)(ws + (20 << 20));
  float* St2 = (float*)(ws + (21 << 20));
  bf16_t* out = (bf16_t*)d_out;

  hipFuncSetAttribute((const void*)QKV0_K, hipFuncAttributeMaxDynamicSharedMemorySize, QKV_LDS);
  hipFuncSetAttribute((const void*)QKV1_K, hipFuncAttributeMaxDynamicSharedMemorySize, QKV_LDS);
  hipFuncSetAttribute((const void*)WO_K, hipFuncAttributeMaxDynamicSharedMemorySize, WO_LDS);
  hipFuncSetAttribute((const void*)F1_K, hipFuncAttributeMaxDynamicSharedMemorySize, F1_LDS);
  hipFuncSetAttribute((const void*)F2_K, hipFuncAttributeMaxDynamicSharedMemorySize, F2_LDS);
  hipFuncSetAttribute((const void*)attn_kernel, hipFuncAttributeMaxDynamicSharedMemorySize, A_LDS);

  for (int layer = 0; layer < 2; ++layer) {
    const bf16_t* xin = layer == 0 ? x : X2;
    bf16_t* xout = layer == 0 ? X2 : out;
    const char* none = (const char*)x;
    if (layer == 0) QKV0_K<<<256, 768, QKV_LDS, stream>>>(xin, wqkv, nullptr, nullptr, Qb, Kb, Vb, nullptr, none, 0, 0);
    else            QKV1_K<<<256, 768, QKV_LDS, stream>>>(xin, wqkv, nullptr, nullptr, Qb, Kb, Vb, St2, none, 0, 0);
    attn_kernel<<<256, 1024, A_LDS, stream>>>(Qb, Kb, Vb, Ob, (const char*)wo, DM * DM * 2 / 128, (const char*)w1, DM * FF * 2 / 128);
    WO_K<<<256, 512 + 64 * NL_SMALL, WO_LDS, stream>>>(Ob, wo, nullptr, xin, X1, nullptr, nullptr, St1, none, 0, (layer ? 1 : 0) | 2);
    F1_K<<<256, 768, F1_LDS, stream>>>(X1, w1, b1, nullptr, Hb, nullptr, nullptr, St1, (const char*)w2, FF * DM * 2 / 128, 0);
    F2_K<<<256, 512 + 64 * NL_SMALL, F2_LDS, stream>>>(Hb, w2, b2, X1, xout, nullptr, nullptr, St2, (const char*)wqkv,
                                       layer == 0 ? DM * 3 * DM * 2 / 128 : 0, 1 | (layer ? 0 : 2));
  }
}
```

```cpp
#include <hip/hip_runtime.h>
#include <hip/hip_bf16.h>
#include <stdint.h>

typedef __bf16 bf16_t;
typedef __bf16 bf16x2 __attribute__((ext_vector_type(2)));
typedef __bf16 bf16x4 __attribute__((ext_vector_type(4)));
typedef __bf16 bf16x8 __attribute__((ext_vector_type(8)));
typedef float f32x2 __attribute__((ext_vector_type(2)));
typedef float f32x4 __attribute__((ext_vector_type(4)));
typedef float f32x16 __attribute__((ext_vector_type(16)));
typedef unsigned u32x2 __attribute__((ext_vector_type(2)));
typedef unsigned u32x4 __attribute__((ext_vector_type(4)));

#define LDS_AS __attribute__((address_space(3)))
#define GLB_AS __attribute__((address_space(1)))

constexpr int SEQ = 2048, DM = 512, NH = 32, DH = 16, FF = 1792;

__device__ __forceinline__ void glds16(const void* g, unsigned lds_base) {
  unsigned keep;
  asm volatile("s_mov_b32 %0, m0\n\ts_mov_b32 m0, %2\n\ts_nop 0\n\tglobal_load_lds_dwordx4 %1, off\n\ts_mov_b32 m0, %0"
               : "=&s"(keep) : "v"(g), "s"(lds_base) : "memory");
}
template <int IMM>
__device__ __forceinline__ void glds16s(unsigned voff, const void* sbase, unsigned lds_base) {
  asm volatile("s_add_i32 m0, %2, %3\n\ts_nop 0\n\tglobal_load_lds_dwordx4 %0, %1"
               :: "v"(voff), "s"(sbase), "s"(lds_base), "i"(IMM) : "memory", "scc");
}
__device__ __forceinline__ void touch(const void* p, unsigned lds_dummy) {
  unsigned keep;
  asm volatile("s_mov_b32 %0, m0\n\ts_mov_b32 m0, %2\n\ts_nop 0\n\tglobal_load_lds_dword %1, off\n\ts_mov_b32 m0, %0"
               : "=&s"(keep) : "v"(p), "s"(lds_dummy) : "memory");
}
#define ST8(ptr, val) (*(u32x2*)(ptr) = (val))
#define SBAR() __builtin_amdgcn_sched_barrier(0)
#define PIN(x) asm volatile("" : "+v"(x))
__device__ __forceinline__ int tile_off(int tok, int col, int nc16) { return ((tok >> 4) * nc16 + (col >> 4)) * 256; }
__device__ __forceinline__ float rbf(float v) { return (float)(__bf16)v; }
__device__ __forceinline__ u32x2 pack4(float a, float b, float c, float d) {
  f32x2 lo = {a, b}, hi = {c, d};
  bf16x2 l = __builtin_convertvector(lo, bf16x2), h = __builtin_convertvector(hi, bf16x2);
  u32x2 r = {__builtin_bit_cast(unsigned, l), __builtin_bit_cast(unsigned, h)};
  return r;
}
__device__ __forceinline__ float bf_lo(unsigned u) { return __builtin_bit_cast(float, u << 16); }
__device__ __forceinline__ float bf_hi(unsigned u) { return __builtin_bit_cast(float, u & 0xffff0000u); }

#define GST(i)
#define GST_DECL
#define GST_FLUSH
template <int N>
__device__ __forceinline__ void wait_bar() {
  asm volatile("s_waitcnt vmcnt(%0) lgkmcnt(0)\n\ts_barrier" ::"i"(N) : "memory");
}

template <int I> struct IC { static constexpr int value = I; };
template <int I, int N, class F>
__device__ __forceinline__ void static_for(F& f) {
  if constexpr (I < N) { f(IC<I>{}); static_for<I + 1, N>(f); }
}

__device__ __forceinline__ float gelu_tanh(float x) {
  const float c1 = -2.0f * 0.7978845608028654f * 1.4426950408889634f;
  const float c2 = c1 * 0.044715f;
  float t = x * __builtin_fmaf(x * x, c2, c1);
  float e = __builtin_amdgcn_exp2f(t);
  return x * __builtin_amdgcn_rcpf(1.0f + e);
}

template <int N>
__device__ __forceinline__ void wait_vm() { asm volatile("s_waitcnt vmcnt(%0)" ::"i"(N) : "memory"); }
__device__ __forceinline__ void bar_lgkm() { asm volatile("s_waitcnt lgkmcnt(0)\n\ts_barrier" ::: "memory"); }
#define MFMA16(a, b, c) __builtin_amdgcn_mfma_f32_16x16x32_bf16(a, b, c, 0, 0, 0)
__device__ __forceinline__ float sum_g(float v) {
  auto a = __builtin_amdgcn_permlane16_swap(__float_as_uint(v), __float_as_uint(v), false, false);
  v = __uint_as_float(a[0]) + __uint_as_float(a[1]);
  auto b = __builtin_amdgcn_permlane32_swap(__float_as_uint(v), __float_as_uint(v), false, false);
  return __uint_as_float(b[0]) + __uint_as_float(b[1]);
}

template <int BM, int BN, int WVM, int WVN, int K, int LDW, int NST, int MODE, int SELF, int NL, int XT>
__global__ __launch_bounds__(512 + 64 * NL, 2 + NL / 4) void gemm_kernel(const bf16_t* __restrict__ X, const bf16_t* __restrict__ W,
                                                      const bf16_t* __restrict__ bias, const bf16_t* __restrict__ res,
                                                      bf16_t* __restrict__ out0, bf16_t* __restrict__ out1,
                                                      bf16_t* __restrict__ out2, float* __restrict__ stats,
                                                      const char* __restrict__ pf, int pf_lines, int flags) {
  extern __shared__ __attribute__((aligned(16))) char smem_g[];
  LDS_AS char* smem = (LDS_AS char*)smem_g;
  constexpr int TM = BM / WVM / 16, TN = BN / WVN / 16, NB = BN / 16, HM = TM / 2;
  constexpr int XB = BM * 128, WB = BN * 128, STB = XB + WB;
  constexpr int NXI = BM / 8, NI = (BM + BN) / 8, LPL = (NI + NL - 1) / NL, NK = K / 64;
  constexpr int NFULL = NI - NL * (LPL - 1);
#define DEP_BIG 2
#define DEP_SMALL 3
  constexpr int DEP = (STB >= 28 * 1024) ? DEP_BIG : DEP_SMALL;
  static_assert(NST >= DEP + 1, "ring too small");
  constexpr bool LN = (MODE == 0 || MODE == 2);
  constexpr bool GRP = (NB % 4 == 0);
  constexpr int NSEG = NB / 4;
  constexpr int NCS = (NB + 3) / 4;
  constexpr int PF_AT = (NK - 4);
  constexpr int NX4 = HM * TN;
  constexpr int EX_OFF = NST * STB;
  constexpr int CS_OFF = EX_OFF;
  constexpr int ST_OFF = CS_OFF + 1024;
  constexpr int PF_OFF = ST_OFF + 1024;
  constexpr int BI_OFF = PF_OFF + 256;
  static_assert(8 * NX4 * 1024 <= ((NK - 1) % NST) * STB, "exchange area overlaps the last stage");

  const int tid = threadIdx.x, lane = tid & 63;
  const int wave = __builtin_amdgcn_readfirstlane(tid >> 6);
  const int b = blockIdx.x, xcd = b & 7, j = b >> 3;
  int mt, nt;
  if (BM == 128) { mt = xcd * 2 + (j & 1); nt = j >> 1; }
  else           { mt = xcd * 4 + (j & 3); nt = j >> 2; }
  GST_DECL; GST(0);
  const int m0 = mt * BM;
  const int l15 = lane & 15, g = lane >> 4;
  const int q_ = (lane & 15) >> 2, p_ = lane & 3;
  const __bf16 one = (__bf16)1.0f;
  const bf16x8 ones8 = {one, one, one, one, one, one, one, one};

  auto colbase = [&](int nb) -> int {
    if (MODE == 0) return (nb >> 2) * 512 + (nt * 4 + (nb & 3)) * 16;
    return nt * BN + nb * 16;
  };
  auto waddr = [&](int kb, int nb) -> int {
    if (GRP) return XB + (kb * NSEG + (nb >> 2)) * 1024 + q_ * 128 + (((nb & 3) ^ (((q_ >> 1) & 1) | ((kb & 1) << 1))) * 32) + p_ * 8;
    return XB + (kb * NB + nb) * 256 + (q_ + 4 * (kb & 1)) * 32 + p_ * 8;
  };
  auto wt1 = [&](int a) -> int { return GRP ? a + 512 : a ^ 128; };
  auto tr8 = [&](LDS_AS char* p0, LDS_AS char* p1) -> bf16x8 {
    bf16x4 a = __builtin_amdgcn_ds_read_tr16_b64_v4bf16((LDS_AS bf16x4*)p0);
    bf16x4 c = __builtin_amdgcn_ds_read_tr16_b64_v4bf16((LDS_AS bf16x4*)p1);
    return __builtin_shufflevector(a, c, 0, 1, 2, 3, 4, 5, 6, 7);
  };

  if (wave >= 8) {
    const int lw = wave - 8;
    constexpr int XS = NXI / NL;
    unsigned voff[LPL];
    {
      const int row = 8 * lw + (lane >> 3);
      const int ch = (lane & 7) ^ ((row >> 1) & 7);
      if (XT) voff[0] = (unsigned)((lw >> 1) * (K / 16) * 512 + (lw & 1) * 1024 + lane * 16);
      else    voff[0] = (unsigned)(row * K + ch * 8) * 2u;
#pragma unroll
      for (int i = 1; i < XS; ++i) voff[i] = voff[0] + i * (8 * NL) * K * 2;
#pragma unroll
      for (int i = XS; i < LPL; ++i) {
        const int wi = lw + NL * (i - XS);
        if (GRP) {
          const int kb = wi / NSEG, seg = wi % NSEG, r = lane >> 3;
          const int c = ((lane & 7) >> 1) ^ (((r >> 1) & 1) | ((kb & 1) << 1));
          voff[i] = (unsigned)((kb * 8 + r) * LDW + colbase(seg * 4 + c) + (lane & 1) * 8) * 2u;
        } else {
          const int sidx = 4 * wi + (lane >> 4);
          const int kb = sidx / NB, nb = sidx % NB;
          const int krow = kb * 8 + (((lane >> 1) & 7) ^ ((kb & 1) << 2));
          voff[i] = (unsigned)(krow * LDW + colbase(nb) + (lane & 1) * 8) * 2u;
        }
      }
    }
    const char* xs_ = (const char*)(X + (size_t)m0 * K);
    const char* ws_ = (const char*)W;
    const unsigned lds0 = (unsigned)(uintptr_t)smem + lw * 1024;
    const bool full = lw < NFULL;
    auto issue = [&](auto ST) {
      constexpr int st = decltype(ST)::value;
      auto one_ = [&](auto I) {
        constexpr int i = decltype(I)::value;
        if (i < LPL - 1 || NFULL == NL || full)
          glds16s<(st % NST) * STB + i * (1024 * NL)>(voff[i], i < XS ? xs_ + st * (XT ? 2048 : 128) : ws_ + (size_t)st * (64 * LDW * 2), lds0);
      };
      static_for<0, LPL>(one_);
    };
    static_for<0, (DEP < NK ? DEP : NK)>(issue);

    constexpr int RS = BM / 64;
    int ca[NCS][2];
#pragma unroll
    for (int c = 0; c < NCS; ++c) {
      const int nb = (lw + 4 * c < NB) ? lw + 4 * c : NB - 1;
      ca[c][0] = waddr(g, nb); ca[c][1] = waddr(4 + g, nb);
    }
    const int xo = (lw * RS * 16 + l15) * 128 + ((g ^ (l15 >> 1)) * 16);
    f32x4 cs[NCS], rs[RS], rq[RS];
#pragma unroll
    for (int c = 0; c < NCS; ++c) cs[c] = f32x4{0.f, 0.f, 0.f, 0.f};
#pragma unroll
    for (int mi = 0; mi < RS; ++mi) rs[mi] = rq[mi] = f32x4{0.f, 0.f, 0.f, 0.f};
    auto lstep = [&](auto KT) {
      constexpr int kt = decltype(KT)::value;
      constexpr int last = (kt + DEP - 1 < NK - 1) ? kt + DEP - 1 : NK - 1;
      if (NFULL == NL || full) wait_vm<(last - kt) * LPL>(); else wait_vm<(last - kt) * (LPL - 1)>();
      bar_lgkm();
      if constexpr (kt + DEP < NK) issue(IC<kt + DEP>{});
      if (LN) {
        LDS_AS char* base = smem + (kt % NST) * STB;
#pragma unroll
        for (int ks = 0; ks < 2; ++ks) {
#pragma unroll
          for (int c = 0; c < NCS; ++c) cs[c] = MFMA16(tr8(base + ca[c][ks], base + wt1(ca[c][ks])), ones8, cs[c]);
          if (SELF) {
#pragma unroll
            for (int mi = 0; mi < RS; ++mi) {
              const bf16x8 xf = *(const LDS_AS bf16x8*)(base + ((xo ^ (ks * 64)) + mi * 2048));
              rs[mi] = MFMA16(ones8, xf, rs[mi]);
              rq[mi] = MFMA16(xf, xf, rq[mi]);
            }
          }
        }
      }
    };
    static_for<0, NK>(lstep);
    if (LN) {
      if (l15 == 0) {
#pragma unroll
        for (int c = 0; c < NCS; ++c)
          *(LDS_AS f32x4*)(smem + CS_OFF + ((lw + 4 * c < NB) ? lw + 4 * c : NB - 1) * 64 + g * 16) = cs[c];
      }
      if (SELF) {
        if (g == (l15 >> 2)) {
          const int r = l15 & 3;
#pragma unroll
          for (int mi = 0; mi < RS; ++mi) {
            const float sq = r == 0 ? rq[mi][0] : r == 1 ? rq[mi][1] : r == 2 ? rq[mi][2] : rq[mi][3];
            const float m = rs[mi][0] * (1.0f / K);
            const float v = __builtin_fmaxf(sq * (1.0f / K) - m * m, 0.f);
            *(LDS_AS f32x2*)(smem + ST_OFF + (lw * RS * 16 + mi * 16 + l15) * 8) = f32x2{m, __builtin_amdgcn_rsqf(v + 1e-5f)};
          }
        }
      }
    }
    bar_lgkm();
    return;
  }

  const int kg = wave >> 2, w4 = wave & 3;
  const int wm = w4 / WVN, wn = w4 % WVN;
  auto nb_of = [&](int ni) -> int {
    if (MODE == 0) return 4 * ni + wn;
    return wn * TN + ni;
  };
  const int tl0 = wm * TM * 16 + kg * HM * 16;
  const int tok0 = m0 + tl0;

  if (LN && !SELF) {
    if (wave < BM / 64) {
      float sm = 0.f, sq = 0.f;
#pragma unroll
      for (int i = 0; i < 8; ++i) {
        const f32x4 t = *(const f32x4*)(stats + (size_t)(m0 + tid) * 32 + i * 4);
        sm += t[0] + t[2]; sq += t[1] + t[3];
      }
      const float m = sm * (1.0f / K);
      const float v = __builtin_fmaxf(sq * (1.0f / K) - m * m, 0.f);
      *(LDS_AS f32x2*)(smem + ST_OFF + tid * 8) = f32x2{m, __builtin_amdgcn_rsqf(v + 1e-5f)};
    }
  }
  if (MODE == 2) {
    if (wave == 2 && lane < BN / 4) *(LDS_AS u32x2*)(smem + BI_OFF + lane * 8) = *(const u32x2*)(bias + nt * BN + lane * 4);
  }
  u32x2 resv[HM][TN];
  u32x2 biasv[TN];
  if (MODE == 1 || MODE == 3) {
#pragma unroll
    for (int mi = 0; mi < HM; ++mi)
#pragma unroll
      for (int ni = 0; ni < TN; ++ni)
        resv[mi][ni] = *(const u32x2*)(res + ((flags & 1) ? tile_off(tok0 + mi * 16, colbase(nb_of(ni)), DM / 16) + l15 * 16 + 4 * g
                                                             : (tok0 + mi * 16 + l15) * DM + colbase(nb_of(ni)) + 4 * g));
  }
  if (MODE == 3) {
#pragma unroll
    for (int ni = 0; ni < TN; ++ni) biasv[ni] = *(const u32x2*)(bias + colbase(nb_of(ni)) + 4 * g);
  }

  const int xo_ = XT ? wm * TM * 2048 + (kg * 2 + (g >> 1)) * 512 + l15 * 32 + (g & 1) * 16
                     : ((wm * TM * 16 + l15) * 128 + ((g ^ (l15 >> 1)) * 16)) ^ (kg * 64);
  const int xoA = xo_ + kg * HM * 2048, xoB = xo_ + (1 - kg) * HM * 2048;
  int wr_[TN];
#pragma unroll
  for (int ni = 0; ni < TN; ++ni) wr_[ni] = GRP ? waddr(kg * 4 + g, nb_of(ni)) : waddr(kg * 4 + g, nb_of(0)) + ni * 256;
  const int wr1_ = wt1(wr_[0]);

  f32x4 acc[TM][TN];
#pragma unroll
  for (int mi = 0; mi < TM; ++mi)
#pragma unroll
    for (int ni = 0; ni < TN; ++ni) acc[mi][ni] = f32x4{0.f, 0.f, 0.f, 0.f};

  bf16x8 xf[2][TM], wf[2][TN];
  auto ldfrags = [&](auto KT) {
    constexpr int kt = decltype(KT)::value, p = kt & 1;
    LDS_AS char* base = smem + (kt % NST) * STB;
#pragma unroll
    for (int mi = 0; mi < TM; ++mi) xf[p][mi] = *(const LDS_AS bf16x8*)(base + (mi < HM ? xoA + mi * 2048 : xoB + (mi - HM) * 2048));
#pragma unroll
    for (int ni = 0; ni < TN; ++ni) wf[p][ni] = tr8(base + wr_[ni], base + (GRP ? wr_[ni] + 512 : wr1_ + ni * 256));
  };
  GST(1);
  bar_lgkm();
  GST(2);
  ldfrags(IC<0>{});
#define LATE_N 100
  auto kstep = [&](auto KT) {
    constexpr int kt = decltype(KT)::value, p = kt & 1;
    constexpr bool late = (kt + 1 < NK) && (kt + 1 >= NK - LATE_N);
    if constexpr (kt + 1 < NK && !late) {
      bar_lgkm();
      ldfrags(IC<kt + 1>{});
    }
    if constexpr (kt == PF_AT) {
      const int line = j * 512 + tid;
      if (line < pf_lines) touch(pf + (size_t)line * 128, (unsigned)(uintptr_t)smem + PF_OFF);
    }
    SBAR();
#pragma unroll
    for (int mj = 0; mj < TM; ++mj)
#pragma unroll
      for (int ni = 0; ni < TN; ++ni) {
        const int mi = (mj + HM) % TM;
        if (MODE == 0 && ni == 2) acc[mi][ni] = MFMA16(xf[p][mi], wf[p][ni], acc[mi][ni]);
        else                      acc[mi][ni] = MFMA16(wf[p][ni], xf[p][mi], acc[mi][ni]);
      }
    SBAR();
    if constexpr (late) {
      bar_lgkm();
      ldfrags(IC<kt + 1>{});
      SBAR();
    }
  };
  static_for<0, NK>(kstep);

  GST(3);
  {
    LDS_AS f32x4* wr = (LDS_AS f32x4*)(smem + wave * (NX4 * 1024) + lane * 16);
#pragma unroll
    for (int mi = 0; mi < HM; ++mi)
#pragma unroll
      for (int ni = 0; ni < TN; ++ni) wr[(mi * TN + ni) * 64] = acc[HM + mi][ni];
  }
  GST(4);
  bar_lgkm();
  GST(5);
  f32x4 fa[HM][TN];
  {
    const LDS_AS f32x4* rd = (const LDS_AS f32x4*)(smem + (wave ^ 4) * (NX4 * 1024) + lane * 16);
#pragma unroll
    for (int mi = 0; mi < HM; ++mi)
#pragma unroll
      for (int ni = 0; ni < TN; ++ni) fa[mi][ni] = acc[mi][ni] + rd[(mi * TN + ni) * 64];
  }
  auto tok_stats = [&](int tl) -> f32x2 { return *(const LDS_AS f32x2*)(smem + ST_OFF + tl * 8); };
  auto col_sum4 = [&](int nb) -> f32x4 { return *(const LDS_AS f32x4*)(smem + CS_OFF + nb * 64 + g * 16); };

  if (MODE == 0) {
    const int head = nt * 4 + wn;
    const float qscale = 0.25f * 1.4426950408889634f;
    const f32x4 cq = col_sum4(nb_of(0)), ck = col_sum4(nb_of(1));
    const float cv = *(const LDS_AS float*)(smem + CS_OFF + nb_of(2) * 64 + l15 * 4);
#pragma unroll
    for (int mi = 0; mi < HM; ++mi) {
      const int tok = tok0 + mi * 16 + l15;
      const f32x2 st = tok_stats(tl0 + mi * 16 + l15);
      {
        const float sc = st[1] * qscale;
        float v[4];
#pragma unroll
        for (int r = 0; r < 4; ++r) v[r] = sc * __builtin_fmaf(-st[0], cq[r], fa[mi][0][r]);
        ST8(out0 + ((size_t)head * SEQ + tok) * 16 + 4 * g, pack4(v[0], v[1], v[2], v[3]));
      }
      {
        float v[4];
#pragma unroll
        for (int r = 0; r < 4; ++r) v[r] = st[1] * __builtin_fmaf(-st[0], ck[r], fa[mi][1][r]);
        ST8(out1 + (size_t)head * 32768 + (tok >> 5) * 512 + (g >> 1) * 256 + (tok & 31) * 8 + (g & 1) * 4,
            pack4(v[0], v[1], v[2], v[3]));
      }
      {
        float v[4];
#pragma unroll
        for (int r = 0; r < 4; ++r) {
          const f32x2 sr = tok_stats(tl0 + mi * 16 + 4 * g + r);
          v[r] = sr[1] * __builtin_fmaf(-sr[0], cv, fa[mi][2][r]);
        }
        const int ks16 = (tok0 + mi * 16) >> 4;
        ST8(out2 + (size_t)head * 32768 + ks16 * 256 + (g & 1) * 128 + l15 * 8 + (g >> 1) * 4,
            pack4(v[0], v[1], v[2], v[3]));
      }
    }
  } else if (MODE == 2) {
#pragma unroll
    for (int mi = 0; mi < HM; ++mi) {
      const int tok = tok0 + mi * 16 + l15;
      const f32x2 st = tok_stats(tl0 + mi * 16 + l15);
      const float nrm = -st[0] * st[1];
#pragma unroll
      for (int ni = 0; ni < TN; ++ni) {
        const f32x4 cc = col_sum4(nb_of(ni));
        const u32x2 bv = *(const LDS_AS u32x2*)(smem + BI_OFF + ni * 32 + g * 8);
        const float bb[4] = {bf_lo(bv[0]), bf_hi(bv[0]), bf_lo(bv[1]), bf_hi(bv[1])};
        float v[4];
#pragma unroll
        for (int r = 0; r < 4; ++r)
          v[r] = gelu_tanh(__builtin_fmaf(st[1], fa[mi][ni][r], __builtin_fmaf(nrm, cc[r], bb[r])));
        ST8(out0 + tile_off(tok0 + mi * 16, colbase(nb_of(ni)), FF / 16) + l15 * 16 + 4 * g, pack4(v[0], v[1], v[2], v[3]));
      }
    }
  } else {
#pragma unroll
    for (int mi = 0; mi < HM; ++mi) {
      const int tok = tok0 + mi * 16 + l15;
      float sm = 0.f, sq = 0.f;
#pragma unroll
      for (int ni = 0; ni < TN; ++ni) {
        float rr[4] = {bf_lo(resv[mi][ni][0]), bf_hi(resv[mi][ni][0]), bf_lo(resv[mi][ni][1]), bf_hi(resv[mi][ni][1])};
        if (MODE == 3) {
          rr[0] += bf_lo(biasv[ni][0]); rr[1] += bf_hi(biasv[ni][0]); rr[2] += bf_lo(biasv[ni][1]); rr[3] += bf_hi(biasv[ni][1]);
        }
        const u32x2 pk = pack4(rr[0] + fa[mi][ni][0], rr[1] + fa[mi][ni][1], rr[2] + fa[mi][ni][2], rr[3] + fa[mi][ni][3]);
        ST8(out0 + ((flags & 2) ? tile_off(tok0 + mi * 16, colbase(nb_of(ni)), DM / 16) + l15 * 16 + 4 * g
                                : tok * DM + colbase(nb_of(ni)) + 4 * g), pk);
        const float w[4] = {bf_lo(pk[0]), bf_hi(pk[0]), bf_lo(pk[1]), bf_hi(pk[1])};
#pragma unroll
        for (int r = 0; r < 4; ++r) { sm += w[r]; sq = __builtin_fmaf(w[r], w[r], sq); }
      }
      sm = sum_g(sm); sq = sum_g(sq);
      if (g == 0) *(f32x2*)(stats + (size_t)tok * 32 + (nt * 2 + wn) * 2) = f32x2{sm, sq};
    }
  }
  GST(6);
  asm volatile("s_waitcnt vmcnt(0)" ::: "memory");
  GST(7);
  GST_FLUSH;
}

constexpr int A_KOFF = 0, A_VOFF = 65536, A_ONES = 131072, A_QOFF = 139264, A_COMB = A_QOFF, A_LDS = A_COMB + 8 * 64 * 40;

#define MFMA32(a, b, c) __builtin_amdgcn_mfma_f32_32x32x16_bf16(a, b, c, 0, 0, 0)
__device__ __forceinline__ bf16x8 exp_pack8(const f32x16& s, int o) {
  bf16x8 p;
#pragma unroll
  for (int r = 0; r < 8; r += 2) {
    f32x2 e = {__builtin_amdgcn_exp2f(s[o + r]), __builtin_amdgcn_exp2f(s[o + r + 1])};
    bf16x2 c = __builtin_convertvector(e, bf16x2);
    p[r] = c[0]; p[r + 1] = c[1];
  }
  return p;
}
#define LDF(p) (*(const LDS_AS bf16x8*)(p))
#define ASTEP(SC, SN, VA, VB, VNA, VNB, KO, VO)                              \
  do {                                                                       \
    SN = MFMA32(kf, qf, negm); SBAR();                                       \
    kf = LDF(kp + (KO)); VNA = LDF(vp + (VO)); VNB = LDF(vp + (VO) + 512); SBAR(); \
    { bf16x8 pa = exp_pack8(SC, 0); SBAR(); o = MFMA32(VA, pa, o); SBAR(); } \
    { bf16x8 pb = exp_pack8(SC, 8); SBAR(); o = MFMA32(VB, pb, o); SBAR(); } \
  } while (0)

__device__ __forceinline__ float max16(const f32x16 s) {
  float a = __builtin_fmaxf(__builtin_fmaxf(s[0], s[1]), s[2]);
  float c = __builtin_fmaxf(__builtin_fmaxf(s[3], s[4]), s[5]);
  a = __builtin_fmaxf(__builtin_fmaxf(a, s[6]), s[7]);
  c = __builtin_fmaxf(__builtin_fmaxf(c, s[8]), s[9]);
  a = __builtin_fmaxf(__builtin_fmaxf(a, s[10]), s[11]);
  c = __builtin_fmaxf(__builtin_fmaxf(c, s[12]), s[13]);
  a = __builtin_fmaxf(__builtin_fmaxf(a, s[14]), s[15]);
  return __builtin_fmaxf(a, c);
}
__device__ __forceinline__ float xhalf_max(float m) {
  auto rr = __builtin_amdgcn_permlane32_swap(__float_as_uint(m), __float_as_uint(m), false, false);
  return __builtin_fmaxf(__uint_as_float(rr[0]), __uint_as_float(rr[1]));
}

__global__ __launch_bounds__(1024, 4) void attn_kernel(const bf16_t* __restrict__ Q, const bf16_t* __restrict__ Kf,
                                                       const bf16_t* __restrict__ Vf, bf16_t* __restrict__ O,
                                                       const char* __restrict__ pf0, int pf0_lines,
                                                       const char* __restrict__ pf1, int pf1_lines) {
  extern __shared__ __attribute__((aligned(16))) char smem_g[];
  LDS_AS char* smem = (LDS_AS char*)smem_g;
  const int tid = threadIdx.x, lane = tid & 63;
  const int wave = __builtin_amdgcn_readfirstlane(tid >> 6);
  const int kh = wave >> 3, qw = wave & 7;
  const int b = blockIdx.x, xcd = b & 7, j = b >> 3;
  const int head = xcd * 4 + (j & 3), qt = j >> 2;
  const int r32 = lane & 31, hh = lane >> 5;
  const int q0 = qt * 256 + qw * 32;

  const unsigned lds0 = (unsigned)(uintptr_t)smem;
    const bf16_t* ks = Kf + (size_t)head * 32768 + kh * 16384 + qw * 512 + lane * 8;
    const bf16_t* vs = Vf + (size_t)head * 32768 + kh * 16384 + qw * 512 + lane * 8;
    const unsigned kd = lds0 + A_KOFF + kh * 32768 + qw * 1024, vd = lds0 + A_VOFF + kh * 32768 + qw * 1024;
    glds16(Q + ((size_t)head * SEQ + q0 + r32) * 16 + hh * 8, lds0 + A_QOFF + qw * 1024);
    glds16(ks, kd);
    glds16(vs, vd);

  if (tid < 448) {
    const unsigned v = (tid & 31) == 0 ? 0x3F803F80u : 0u;
    *(LDS_AS u32x4*)(smem + A_ONES + tid * 16) = u32x4{v, v, v, v};
  }

  LDS_AS char* kb = smem + A_KOFF + kh * 32768 + lane * 16;
  const bool real = r32 < 16;
  LDS_AS char* vb = real ? (smem + A_VOFF + kh * 32768 + hh * 256 + r32 * 16) : (smem + A_ONES + (r32 == 16 ? 0 : 16));
  const int vstride = real ? 2048 : 0;

  wait_bar<0>();
  const bf16x8 qf = LDF(smem + A_QOFF + qw * 1024 + lane * 16);
  f32x16 negm;
  float mref;
  {
    f32x16 z = {};
    f32x16 s = MFMA32(LDF(smem + A_KOFF + lane * 16), qf, z);
    mref = xhalf_max(max16(s));
#pragma unroll
    for (int r = 0; r < 16; ++r) negm[r] = -mref;
  }
  f32x16 o = {};
  {
    LDS_AS char* kp = kb;
    LDS_AS char* vp = vb;
    bf16x8 kf = LDF(kp), va0 = LDF(vp), vb0 = LDF(vp + 512), va1, vb1;
    f32x16 sA = MFMA32(kf, qf, negm), sB;
    kf = LDF(kp + 1024);
    SBAR();
#pragma unroll 1
    for (int t = 0; t < 8; t += 2) {
      if (t < 6) {
        if ((wave >> 2) == (t >> 1)) {
          const int c = (t >> 1) + 1, w2 = (wave & 3) * 2;
          const bf16_t* k2 = Kf + (size_t)head * 32768 + c * 4096 + w2 * 512 + lane * 8;
          const bf16_t* v2 = Vf + (size_t)head * 32768 + c * 4096 + w2 * 512 + lane * 8;
          const unsigned d2 = lds0 + c * 8192 + w2 * 1024;
#pragma unroll
          for (int hf = 0; hf < 2; ++hf)
#pragma unroll
            for (int i = 0; i < 2; ++i) {
              glds16(k2 + hf * 16384 + i * 512, d2 + A_KOFF + hf * 32768 + i * 1024);
              glds16(v2 + hf * 16384 + i * 512, d2 + A_VOFF + hf * 32768 + i * 1024);
            }
        }
      } else {
        wait_bar<0>();
      }
      ASTEP(sA, sB, va0, vb0, va1, vb1, 2048, 1024);
      ASTEP(sB, sA, va1, vb1, va0, vb0, 3072, 2048);
      kp += 2048; vp += vstride;
    }
    {
      const int line = j * 1024 + tid;
      const char* pp = line < pf0_lines ? pf0 + (size_t)line * 128 : pf1 + (size_t)(line - pf0_lines) * 128;
      if (line < pf0_lines + pf1_lines) touch(pp, lds0 + A_ONES + 7168);
    }
#pragma unroll 1
    for (int it = 0; it < 6; ++it) {
      {
        const int pr = (it + (wave >> 2)) & 3;
        if (pr == 0) __builtin_amdgcn_s_setprio(0);
        else if (pr == 1) __builtin_amdgcn_s_setprio(1);
        else if (pr == 2) __builtin_amdgcn_s_setprio(2);
        else __builtin_amdgcn_s_setprio(3);
      }
      ASTEP(sA, sB, va0, vb0, va1, vb1, 2048, 1024);
      ASTEP(sB, sA, va1, vb1, va0, vb0, 3072, 2048);
      ASTEP(sA, sB, va0, vb0, va1, vb1, 4096, 3072);
      ASTEP(sB, sA, va1, vb1, va0, vb0, 5120, 4096);
      kp += 4096; vp += 2 * vstride;
    }
    __builtin_amdgcn_s_setprio(0);
  }
  if (__builtin_expect(__any(!(o[8] < 1.2676506e30f)), 0)) {
    float m = -INFINITY;
    f32x16 z = {};
#pragma unroll 1
    for (int t = 0; t < 32; ++t) {
      f32x16 sc = MFMA32(LDF(kb + t * 1024), qf, z);
      m = __builtin_fmaxf(m, max16(sc));
    }
    mref = xhalf_max(m);
#pragma unroll
    for (int r = 0; r < 16; ++r) negm[r] = -mref;
    o = z;
#pragma unroll 1
    for (int t = 0; t < 32; ++t) {
      const f32x16 sc = MFMA32(LDF(kb + t * 1024), qf, negm);
      LDS_AS char* vq = vb + (vstride >> 1) * t;
      o = MFMA32(LDF(vq), exp_pack8(sc, 0), o);
      o = MFMA32(LDF(vq + 512), exp_pack8(sc, 8), o);
    }
  }

  {
    auto rr = __builtin_amdgcn_permlane32_swap(__float_as_uint(o[8]), __float_as_uint(o[8]), false, false);
    o[8] = __uint_as_float(rr[0]);
  }
  LDS_AS float* cb = (LDS_AS float*)(smem + A_COMB + qw * (64 * 40) + lane * 4);
  if (kh) {
#pragma unroll
    for (int r = 0; r < 9; ++r) cb[r * 64] = o[r];
    cb[9 * 64] = mref;
  }
  bar_lgkm();
  if (!kh) {
    const float m2 = cb[9 * 64];
    const float mm = __builtin_fmaxf(mref, m2);
    const float f1 = __builtin_amdgcn_exp2f(mref - mm), f2 = __builtin_amdgcn_exp2f(m2 - mm);
    const float l = o[8] * f1 + cb[8 * 64] * f2;
    const float inv = __builtin_amdgcn_rcpf(l);
    const float a1 = f1 * inv, a2 = f2 * inv;
    float v[8];
#pragma unroll
    for (int r = 0; r < 8; ++r) v[r] = o[r] * a1 + cb[r * 64] * a2;
    bf16_t* op = O + tile_off(q0 + r32, head * 16, DM / 16) + (r32 & 15) * 16 + 4 * hh;
    *(u32x2*)(op) = pack4(v[0], v[1], v[2], v[3]);
    *(u32x2*)(op + 8) = pack4(v[4], v[5], v[6], v[7]);
  }
  asm volatile("s_waitcnt vmcnt(0)" ::: "memory");
}

constexpr int QKV_NST = 4, WO_NST = 6, F1_NST = 5, F2_NST = 6;
#define NL_SMALL 4
#define QKV0_K gemm_kernel<64, 192, 1, 4, 512, 1536, QKV_NST, 0, 1, 4, 0>
#define QKV1_K gemm_kernel<64, 192, 1, 4, 512, 1536, QKV_NST, 0, 0, 4, 1>
#define WO_K gemm_kernel<64, 64, 2, 2, 512, 512, WO_NST, 1, 0, NL_SMALL, 1>
#define F1_K gemm_kernel<128, 112, 4, 1, 512, 1792, F1_NST, 2, 0, 4, 1>
#define F2_K gemm_kernel<64, 64, 2, 2, 1792, 512, F2_NST, 3, 0, NL_SMALL, 1>
constexpr int QKV_LDS = QKV_NST * (64 + 192) * 128 + 4096, WO_LDS = WO_NST * 128 * 128 + 4096,
              F1_LDS = F1_NST * (128 + 112) * 128 + 4096, F2_LDS = F2_NST * 128 * 128 + 4096;

extern "C" void kernel_launch(void* const* d_in, const int* in_sizes, int n_in, void* d_out, int out_size, void* d_ws,
                              size_t ws_size, hipStream_t stream) {
  const bf16_t* x = (const bf16_t*)d_in[0];
  const bf16_t* wqkv = (const bf16_t*)d_in[1];
  const bf16_t* wo = (const bf16_t*)d_in[2];
  const bf16_t* w1 = (const bf16_t*)d_in[4];
  const bf16_t* b1 = (const bf16_t*)d_in[5];
  const bf16_t* w2 = (const bf16_t*)d_in[6];
  const bf16_t* b2 = (const bf16_t*)d_in[7];
  char* ws = (char*)d_ws;
  bf16_t* Qb = (bf16_t*)(ws + (0 << 20));
  bf16_t* Kb = (bf16_t*)(ws + (2 << 20));
  bf16_t* Vb = (bf16_t*)(ws + (4 << 20));
  bf16_t* Ob = (bf16_t*)(ws + (6 << 20));
  bf16_t* X1 = (bf16_t*)(ws + (8 << 20));
  bf16_t* Hb = (bf16_t*)(ws + (10 << 20));
  bf16_t* X2 = (bf16_t*)(ws + (18 << 20));
  float* St1 = (float*)(ws + (20 << 20));
  float* St2 = (float*)(ws + (21 << 20));
  bf16_t* out = (bf16_t*)d_out;

  hipFuncSetAttribute((const void*)QKV0_K, hipFuncAttributeMaxDynamicSharedMemorySize, QKV_LDS);
  hipFuncSetAttribute((const void*)QKV1_K, hipFuncAttributeMaxDynamicSharedMemorySize, QKV_LDS);
  hipFuncSetAttribute((const void*)WO_K, hipFuncAttributeMaxDynamicSharedMemorySize, WO_LDS);
  hipFuncSetAttribute((const void*)F1_K, hipFuncAttributeMaxDynamicSharedMemorySize, F1_LDS);
  hipFuncSetAttribute((const void*)F2_K, hipFuncAttributeMaxDynamicSharedMemorySize, F2_LDS);
  hipFuncSetAttribute((const void*)attn_kernel, hipFuncAttributeMaxDynamicSharedMemorySize, A_LDS);

  for (int layer = 0; layer < 2; ++layer) {
    const bf16_t* xin = layer == 0 ? x : X2;
    bf16_t* xout = layer == 0 ? X2 : out;
    const char* none = (const char*)x;
    if (layer == 0) QKV0_K<<<256, 768, QKV_LDS, stream>>>(xin, wqkv, nullptr, nullptr, Qb, Kb, Vb, nullptr, none, 0, 0);
    else            QKV1_K<<<256, 768, QKV_LDS, stream>>>(xin, wqkv, nullptr, nullptr, Qb, Kb, Vb, St2, none, 0, 0);
    attn_kernel<<<256, 1024, A_LDS, stream>>>(Qb, Kb, Vb, Ob, (const char*)wo, DM * DM * 2 / 128, (const char*)w1, DM * FF * 2 / 128);
    WO_K<<<256, 512 + 64 * NL_SMALL, WO_LDS, stream>>>(Ob, wo, nullptr, xin, X1, nullptr, nullptr, St1, none, 0, (layer ? 1 : 0) | 2);
    F1_K<<<256, 768, F1_LDS, stream>>>(X1, w1, b1, nullptr, Hb, nullptr, nullptr, St1, (const char*)w2, FF * DM * 2 / 128, 0);
    F2_K<<<256, 512 + 64 * NL_SMALL, F2_LDS, stream>>>(Hb, w2, b2, X1, xout, nullptr, nullptr, St2, (const char*)wqkv,
                                       layer == 0 ? DM * 3 * DM * 2 / 128 : 0, 1 | (layer ? 0 : 2));
  }
}
```

```cpp
#include <hip/hip_runtime.h>
#include <hip/hip_bf16.h>
#include <stdint.h>

typedef __bf16 bf16_t;
typedef __bf16 bf16x2 __attribute__((ext_vector_type(2)));
typedef __bf16 bf16x4 __attribute__((ext_vector_type(4)));
typedef __bf16 bf16x8 __attribute__((ext_vector_type(8)));
typedef float f32x2 __attribute__((ext_vector_type(2)));
typedef float f32x4 __attribute__((ext_vector_type(4)));
typedef float f32x16 __attribute__((ext_vector_type(16)));
typedef unsigned u32x2 __attribute__((ext_vector_type(2)));
typedef unsigned u32x4 __attribute__((ext_vector_type(4)));

#define LDS_AS __attribute__((address_space(3)))
#define GLB_AS __attribute__((address_space(1)))

constexpr int SEQ = 2048, DM = 512, NH = 32, DH = 16, FF = 1792;

__device__ __forceinline__ void glds16(const void* g, unsigned lds_base) {
  unsigned keep;
  asm volatile("s_mov_b32 %0, m0\n\ts_mov_b32 m0, %2\n\ts_nop 0\n\tglobal_load_lds_dwordx4 %1, off\n\ts_mov_b32 m0, %0"
               : "=&s"(keep) : "v"(g), "s"(lds_base) : "memory");
}
template <int IMM>
__device__ __forceinline__ void glds16s(unsigned voff, const void* sbase, unsigned lds_base) {
  asm volatile("s_add_i32 m0, %2, %3\n\ts_nop 0\n\tglobal_load_lds_dwordx4 %0, %1"
               :: "v"(voff), "s"(sbase), "s"(lds_base), "i"(IMM) : "memory", "scc");
}
__device__ __forceinline__ void touch(const void* p, unsigned lds_dummy) {
  unsigned keep;
  asm volatile("s_mov_b32 %0, m0\n\ts_mov_b32 m0, %2\n\ts_nop 0\n\tglobal_load_lds_dword %1, off\n\ts_mov_b32 m0, %0"
               : "=&s"(keep) : "v"(p), "s"(lds_dummy) : "memory");
}
#define ST8(ptr, val) (*(u32x2*)(ptr) = (val))
#define SBAR() __builtin_amdgcn_sched_barrier(0)
#define PIN(x) asm volatile("" : "+v"(x))
__device__ __forceinline__ int tile_off(int tok, int col, int nc16) { return ((tok >> 4) * nc16 + (col >> 4)) * 256; }
__device__ __forceinline__ float rbf(float v) { return (float)(__bf16)v; }
__device__ __forceinline__ u32x2 pack4(float a, float b, float c, float d) {
  f32x2 lo = {a, b}, hi = {c, d};
  bf16x2 l = __builtin_convertvector(lo, bf16x2), h = __builtin_convertvector(hi, bf16x2);
  u32x2 r = {__builtin_bit_cast(unsigned, l), __builtin_bit_cast(unsigned, h)};
  return r;
}
__device__ __forceinline__ float bf_lo(unsigned u) { return __builtin_bit_cast(float, u << 16); }
__device__ __forceinline__ float bf_hi(unsigned u) { return __builtin_bit_cast(float, u & 0xffff0000u); }

#define GST(i)
#define GST_DECL
#define GST_FLUSH
template <int N>
__device__ __forceinline__ void wait_bar() {
  asm volatile("s_waitcnt vmcnt(%0) lgkmcnt(0)\n\ts_barrier" ::"i"(N) : "memory");
}

template <int I> struct IC { static constexpr int value = I; };
template <int I, int N, class F>
__device__ __forceinline__ void static_for(F& f) {
  if constexpr (I < N) { f(IC<I>{}); static_for<I + 1, N>(f); }
}

__device__ __forceinline__ float gelu_tanh(float x) {
  const float c1 = -2.0f * 0.7978845608028654f * 1.4426950408889634f;
  const float c2 = c1 * 0.044715f;
  float t = x * __builtin_fmaf(x * x, c2, c1);
  float e = __builtin_amdgcn_exp2f(t);
  return x * __builtin_amdgcn_rcpf(1.0f + e);
}

template <int N>
__device__ __forceinline__ void wait_vm() { asm volatile("s_waitcnt vmcnt(%0)" ::"i"(N) : "memory"); }
__device__ __forceinline__ void bar_lgkm() { asm volatile("s_waitcnt lgkmcnt(0)\n\ts_barrier" ::: "memory"); }
#define MFMA16(a, b, c) __builtin_amdgcn_mfma_f32_16x16x32_bf16(a, b, c, 0, 0, 0)
__device__ __forceinline__ float sum_g(float v) {
  auto a = __builtin_amdgcn_permlane16_swap(__float_as_uint(v), __float_as_uint(v), false, false);
  v = __uint_as_float(a[0]) + __uint_as_float(a[1]);
  auto b = __builtin_amdgcn_permlane32_swap(__float_as_uint(v), __float_as_uint(v), false, false);
  return __uint_as_float(b[0]) + __uint_as_float(b[1]);
}

template <int BM, int BN, int WVM, int WVN, int K, int LDW, int NST, int MODE, int SELF, int NL, int XT>
__global__ __launch_bounds__(512 + 64 * NL, 2 + NL / 4) void gemm_kernel(const bf16_t* __restrict__ X, const bf16_t* __restrict__ W,
                                                      const bf16_t* __restrict__ bias, const bf16_t* __restrict__ res,
                                                      bf16_t* __restrict__ out0, bf16_t* __restrict__ out1,
                                                      bf16_t* __restrict__ out2, float* __restrict__ stats,
                                                      const char* __restrict__ pf, int pf_lines, int flags) {
  extern __shared__ __attribute__((aligned(16))) char smem_g[];
  LDS_AS char* smem = (LDS_AS char*)smem_g;
  constexpr int TM = BM / WVM / 16, TN = BN / WVN / 16, NB = BN / 16, HM = TM / 2;
  constexpr int XB = BM * 128, WB = BN * 128, STB = XB + WB;
  constexpr int NXI = BM / 8, NI = (BM + BN) / 8, LPL = (NI + NL - 1) / NL, NK = K / 64;
  constexpr int NFULL = NI - NL * (LPL - 1);
#define DEP_BIG 2
#define DEP_SMALL 3
  constexpr int DEP = (STB >= 28 * 1024) ? DEP_BIG : DEP_SMALL;
  static_assert(NST >= DEP + 1, "ring too small");
  constexpr bool LN = (MODE == 0 || MODE == 2);
  constexpr bool GRP = (NB % 4 == 0);
  constexpr int NSEG = NB / 4;
  constexpr int NCS = (NB + 3) / 4;
  constexpr bool PFK = (MODE >= 2);
  constexpr int NX4 = HM * TN;
  constexpr int EX_OFF = NST * STB;
  constexpr int CS_OFF = EX_OFF;
  constexpr int ST_OFF = CS_OFF + 1024;
  constexpr int PF_OFF = ST_OFF + 1024;
  constexpr int BI_OFF = PF_OFF + 256;
  static_assert(8 * NX4 * 1024 <= ((NK - 1) % NST) * STB, "exchange area overlaps the last stage");

  const int tid = threadIdx.x, lane = tid & 63;
  const int wave = __builtin_amdgcn_readfirstlane(tid >> 6);
  const int b = blockIdx.x, xcd = b & 7, j = b >> 3;
  int mt, nt;
  if (BM == 128) { mt = xcd * 2 + (j & 1); nt = j >> 1; }
  else           { mt = xcd * 4 + (j & 3); nt = j >> 2; }
  GST_DECL; GST(0);
  const int m0 = mt * BM;
  const int l15 = lane & 15, g = lane >> 4;
  const int q_ = (lane & 15) >> 2, p_ = lane & 3;
  const __bf16 one = (__bf16)1.0f;
  const bf16x8 ones8 = {one, one, one, one, one, one, one, one};

  auto colbase = [&](int nb) -> int {
    if (MODE == 0) return (nb >> 2) * 512 + (nt * 4 + (nb & 3)) * 16;
    return nt * BN + nb * 16;
  };
  auto waddr = [&](int kb, int nb) -> int {
    if (GRP) return XB + (kb * NSEG + (nb >> 2)) * 1024 + q_ * 128 + (((nb & 3) ^ (((q_ >> 1) & 1) | ((kb & 1) << 1))) * 32) + p_ * 8;
    return XB + (kb * NB + nb) * 256 + (q_ + 4 * (kb & 1)) * 32 + p_ * 8;
  };
  auto wt1 = [&](int a) -> int { return GRP ? a + 512 : a ^ 128; };
  auto tr8 = [&](LDS_AS char* p0, LDS_AS char* p1) -> bf16x8 {
    bf16x4 a = __builtin_amdgcn_ds_read_tr16_b64_v4bf16((LDS_AS bf16x4*)p0);
    bf16x4 c = __builtin_amdgcn_ds_read_tr16_b64_v4bf16((LDS_AS bf16x4*)p1);
    return __builtin_shufflevector(a, c, 0, 1, 2, 3, 4, 5, 6, 7);
  };

  if (wave >= 8) {
    const int lw = wave - 8;
    __builtin_amdgcn_s_setprio(3);
    constexpr int XS = NXI / NL;
    unsigned voff[LPL];
    {
      const int row = 8 * lw + (lane >> 3);
      const int ch = (lane & 7) ^ ((row >> 1) & 7);
      if (XT) voff[0] = (unsigned)((lw >> 1) * (K / 16) * 512 + (lw & 1) * 1024 + lane * 16);
      else    voff[0] = (unsigned)(row * K + ch * 8) * 2u;
#pragma unroll
      for (int i = 1; i < XS; ++i) voff[i] = voff[0] + i * (8 * NL) * K * 2;
#pragma unroll
      for (int i = XS; i < LPL; ++i) {
        const int wi = lw + NL * (i - XS);
        if (GRP) {
          const int kb = wi / NSEG, seg = wi % NSEG, r = lane >> 3;
          const int c = ((lane & 7) >> 1) ^ (((r >> 1) & 1) | ((kb & 1) << 1));
          voff[i] = (unsigned)((kb * 8 + r) * LDW + colbase(seg * 4 + c) + (lane & 1) * 8) * 2u;
        } else {
          const int sidx = 4 * wi + (lane >> 4);
          const int kb = sidx / NB, nb = sidx % NB;
          const int krow = kb * 8 + (((lane >> 1) & 7) ^ ((kb & 1) << 2));
          voff[i] = (unsigned)(krow * LDW + colbase(nb) + (lane & 1) * 8) * 2u;
        }
      }
    }
    const char* xs_ = (const char*)(X + (size_t)m0 * K);
    const char* ws_ = (const char*)W;
    const unsigned lds0 = (unsigned)(uintptr_t)smem + lw * 1024;
    const bool full = lw < NFULL;
    auto issue = [&](auto ST) {
      constexpr int st = decltype(ST)::value;
      auto one_ = [&](auto I) {
        constexpr int i = decltype(I)::value;
        if (i < LPL - 1 || NFULL == NL || full)
          glds16s<(st % NST) * STB + i * (1024 * NL)>(voff[i], i < XS ? xs_ + st * (XT ? 2048 : 128) : ws_ + (size_t)st * (64 * LDW * 2), lds0);
      };
      static_for<0, LPL>(one_);
    };
    static_for<0, (DEP < NK ? DEP : NK)>(issue);

    constexpr int RS = BM / 64;
    int ca[NCS][2];
#pragma unroll
    for (int c = 0; c < NCS; ++c) {
      const int nb = (lw + 4 * c < NB) ? lw + 4 * c : NB - 1;
      ca[c][0] = waddr(g, nb); ca[c][1] = waddr(4 + g, nb);
    }
    const int xo = (lw * RS * 16 + l15) * 128 + ((g ^ (l15 >> 1)) * 16);
    f32x4 cs[NCS], rs[RS], rq[RS];
#pragma unroll
    for (int c = 0; c < NCS; ++c) cs[c] = f32x4{0.f, 0.f, 0.f, 0.f};
#pragma unroll
    for (int mi = 0; mi < RS; ++mi) rs[mi] = rq[mi] = f32x4{0.f, 0.f, 0.f, 0.f};
    auto lstep = [&](auto KT) {
      constexpr int kt = decltype(KT)::value;
      constexpr int last = (kt + DEP - 1 < NK - 1) ? kt + DEP - 1 : NK - 1;
      constexpr int tch = (PFK && kt + DEP > NK - 1) ? 2 : 0;
      if (NFULL == NL || full) wait_vm<(last - kt) * LPL + tch>(); else wait_vm<(last - kt) * (LPL - 1) + tch>();
      bar_lgkm();
      if constexpr (kt + DEP < NK) issue(IC<kt + DEP>{});
      if constexpr (PFK && kt + DEP == NK - 1) {
#pragma unroll
        for (int i = 0; i < 2; ++i) {
          int line = j * 512 + i * 256 + lw * 64 + lane;
          line = line < pf_lines ? line : 0;
          touch(pf + (size_t)line * 128, (unsigned)(uintptr_t)smem + PF_OFF);
        }
      }
      if (LN) {
        LDS_AS char* base = smem + (kt % NST) * STB;
#pragma unroll
        for (int ks = 0; ks < 2; ++ks) {
#pragma unroll
          for (int c = 0; c < NCS; ++c) cs[c] = MFMA16(tr8(base + ca[c][ks], base + wt1(ca[c][ks])), ones8, cs[c]);
          if (SELF) {
#pragma unroll
            for (int mi = 0; mi < RS; ++mi) {
              const bf16x8 xf = *(const LDS_AS bf16x8*)(base + ((xo ^ (ks * 64)) + mi * 2048));
              rs[mi] = MFMA16(ones8, xf, rs[mi]);
              rq[mi] = MFMA16(xf, xf, rq[mi]);
            }
          }
        }
      }
    };
    static_for<0, NK>(lstep);
    if (LN) {
      if (l15 == 0) {
#pragma unroll
        for (int c = 0; c < NCS; ++c)
          *(LDS_AS f32x4*)(smem + CS_OFF + ((lw + 4 * c < NB) ? lw + 4 * c : NB - 1) * 64 + g * 16) = cs[c];
      }
      if (SELF) {
        if (g == (l15 >> 2)) {
          const int r = l15 & 3;
#pragma unroll
          for (int mi = 0; mi < RS; ++mi) {
            const float sq = r == 0 ? rq[mi][0] : r == 1 ? rq[mi][1] : r == 2 ? rq[mi][2] : rq[mi][3];
            const float m = rs[mi][0] * (1.0f / K);
            const float v = __builtin_fmaxf(sq * (1.0f / K) - m * m, 0.f);
            *(LDS_AS f32x2*)(smem + ST_OFF + (lw * RS * 16 + mi * 16 + l15) * 8) = f32x2{m, __builtin_amdgcn_rsqf(v + 1e-5f)};
          }
        }
      }
    }
    bar_lgkm();
    if (PFK) asm volatile("s_waitcnt vmcnt(0)" ::: "memory");
    return;
  }

  const int kg = wave >> 2, w4 = wave & 3;
  const int wm = w4 / WVN, wn = w4 % WVN;
  auto nb_of = [&](int ni) -> int {
    if (MODE == 0) return 4 * ni + wn;
    return wn * TN + ni;
  };
  const int tl0 = wm * TM * 16 + kg * HM * 16;
  const int tok0 = m0 + tl0;

  if (LN && !SELF) {
    if (wave < BM / 64) {
      float sm = 0.f, sq = 0.f;
#pragma unroll
      for (int i = 0; i < 8; ++i) {
        const f32x4 t = *(const f32x4*)(stats + (size_t)(m0 + tid) * 32 + i * 4);
        sm += t[0] + t[2]; sq += t[1] + t[3];
      }
      const float m = sm * (1.0f / K);
      const float v = __builtin_fmaxf(sq * (1.0f / K) - m * m, 0.f);
      *(LDS_AS f32x2*)(smem + ST_OFF + tid * 8) = f32x2{m, __builtin_amdgcn_rsqf(v + 1e-5f)};
    }
  }
  if (MODE == 2) {
    if (wave == 2 && lane < BN / 4) *(LDS_AS u32x2*)(smem + BI_OFF + lane * 8) = *(const u32x2*)(bias + nt * BN + lane * 4);
  }
  u32x2 resv[HM][TN];
  u32x2 biasv[TN];
  if (MODE == 1 || MODE == 3) {
#pragma unroll
    for (int mi = 0; mi < HM; ++mi)
#pragma unroll
      for (int ni = 0; ni < TN; ++ni)
        resv[mi][ni] = *(const u32x2*)(res + ((flags & 1) ? tile_off(tok0 + mi * 16, colbase(nb_of(ni)), DM / 16) + l15 * 16 + 4 * g
                                                             : (tok0 + mi * 16 + l15) * DM + colbase(nb_of(ni)) + 4 * g));
  }
  if (MODE == 3) {
#pragma unroll
    for (int ni = 0; ni < TN; ++ni) biasv[ni] = *(const u32x2*)(bias + colbase(nb_of(ni)) + 4 * g);
  }

  const int xo_ = XT ? wm * TM * 2048 + (kg * 2 + (g >> 1)) * 512 + l15 * 32 + (g & 1) * 16
                     : ((wm * TM * 16 + l15) * 128 + ((g ^ (l15 >> 1)) * 16)) ^ (kg * 64);
  const int xoA = xo_ + kg * HM * 2048, xoB = xo_ + (1 - kg) * HM * 2048;
  int wr_[TN];
#pragma unroll
  for (int ni = 0; ni < TN; ++ni) wr_[ni] = GRP ? waddr(kg * 4 + g, nb_of(ni)) : waddr(kg * 4 + g, nb_of(0)) + ni * 256;
  const int wr1_ = wt1(wr_[0]);

  f32x4 acc[TM][TN];
#pragma unroll
  for (int mi = 0; mi < TM; ++mi)
#pragma unroll
    for (int ni = 0; ni < TN; ++ni) acc[mi][ni] = f32x4{0.f, 0.f, 0.f, 0.f};

  bf16x8 xf[2][TM], wf[2][TN];
  auto ldfrags = [&](auto KT) {
    constexpr int kt = decltype(KT)::value, p = kt & 1;
    LDS_AS char* base = smem + (kt % NST) * STB;
#pragma unroll
    for (int mi = 0; mi < TM; ++mi) xf[p][mi] = *(const LDS_AS bf16x8*)(base + (mi < HM ? xoA + mi * 2048 : xoB + (mi - HM) * 2048));
#pragma unroll
    for (int ni = 0; ni < TN; ++ni) wf[p][ni] = tr8(base + wr_[ni], base + (GRP ? wr_[ni] + 512 : wr1_ + ni * 256));
  };
  GST(1);
  bar_lgkm();
  GST(2);
  ldfrags(IC<0>{});
#define LATE_N 100
  auto kstep = [&](auto KT) {
    constexpr int kt = decltype(KT)::value, p = kt & 1;
    constexpr bool late = (kt + 1 < NK) && (kt + 1 >= NK - LATE_N);
    if constexpr (kt + 1 < NK && !late) {
      bar_lgkm();
      ldfrags(IC<kt + 1>{});
    }
    SBAR();
#pragma unroll
    for (int mj = 0; mj < TM; ++mj)
#pragma unroll
      for (int ni = 0; ni < TN; ++ni) {
        const int mi = (mj + HM) % TM;
        if (MODE == 0 && ni == 2) acc[mi][ni] = MFMA16(xf[p][mi], wf[p][ni], acc[mi][ni]);
        else                      acc[mi][ni] = MFMA16(wf[p][ni], xf[p][mi], acc[mi][ni]);
      }
    SBAR();
    if constexpr (late) {
      bar_lgkm();
      ldfrags(IC<kt + 1>{});
      SBAR();
    }
  };
  static_for<0, NK>(kstep);

  GST(3);
  {
    LDS_AS f32x4* wr = (LDS_AS f32x4*)(smem + wave * (NX4 * 1024) + lane * 16);
#pragma unroll
    for (int mi = 0; mi < HM; ++mi)
#pragma unroll
      for (int ni = 0; ni < TN; ++ni) wr[(mi * TN + ni) * 64] = acc[HM + mi][ni];
  }
  GST(4);
  bar_lgkm();
  GST(5);
  f32x4 fa[HM][TN];
  {
    const LDS_AS f32x4* rd = (const LDS_AS f32x4*)(smem + (wave ^ 4) * (NX4 * 1024) + lane * 16);
#pragma unroll
    for (int mi = 0; mi < HM; ++mi)
#pragma unroll
      for (int ni = 0; ni < TN; ++ni) fa[mi][ni] = acc[mi][ni] + rd[(mi * TN + ni) * 64];
  }
  auto tok_stats = [&](int tl) -> f32x2 { return *(const LDS_AS f32x2*)(smem + ST_OFF + tl * 8); };
  auto col_sum4 = [&](int nb) -> f32x4 { return *(const LDS_AS f32x4*)(smem + CS_OFF + nb * 64 + g * 16); };

  if (MODE == 0) {
    const int head = nt * 4 + wn;
    const float qscale = 0.25f * 1.4426950408889634f;
    const f32x4 cq = col_sum4(nb_of(0)), ck = col_sum4(nb_of(1));
    const float cv = *(const LDS_AS float*)(smem + CS_OFF + nb_of(2) * 64 + l15 * 4);
#pragma unroll
    for (int mi = 0; mi < HM; ++mi) {
      const int tok = tok0 + mi * 16 + l15;
      const f32x2 st = tok_stats(tl0 + mi * 16 + l15);
      {
        const float sc = st[1] * qscale;
        float v[4];
#pragma unroll
        for (int r = 0; r < 4; ++r) v[r] = sc * __builtin_fmaf(-st[0], cq[r], fa[mi][0][r]);
        ST8(out0 + ((size_t)head * SEQ + tok) * 16 + 4 * g, pack4(v[0], v[1], v[2], v[3]));
      }
      {
        float v[4];
#pragma unroll
        for (int r = 0; r < 4; ++r) v[r] = st[1] * __builtin_fmaf(-st[0], ck[r], fa[mi][1][r]);
        ST8(out1 + (size_t)head * 32768 + (tok >> 5) * 512 + (g >> 1) * 256 + (tok & 31) * 8 + (g & 1) * 4,
            pack4(v[0], v[1], v[2], v[3]));
      }
      {
        float v[4];
#pragma unroll
        for (int r = 0; r < 4; ++r) {
          const f32x2 sr = tok_stats(tl0 + mi * 16 + 4 * g + r);
          v[r] = sr[1] * __builtin_fmaf(-sr[0], cv, fa[mi][2][r]);
        }
        const int ks16 = (tok0 + mi * 16) >> 4;
        ST8(out2 + (size_t)head * 32768 + ks16 * 256 + (g & 1) * 128 + l15 * 8 + (g >> 1) * 4,
            pack4(v[0], v[1], v[2], v[3]));
      }
    }
  } else if (MODE == 2) {
#pragma unroll
    for (int mi = 0; mi < HM; ++mi) {
      const int tok = tok0 + mi * 16 + l15;
      const f32x2 st = tok_stats(tl0 + mi * 16 + l15);
      const float nrm = -st[0] * st[1];
#pragma unroll
      for (int ni = 0; ni < TN; ++ni) {
        const f32x4 cc = col_sum4(nb_of(ni));
        const u32x2 bv = *(const LDS_AS u32x2*)(smem + BI_OFF + ni * 32 + g * 8);
        const float bb[4] = {bf_lo(bv[0]), bf_hi(bv[0]), bf_lo(bv[1]), bf_hi(bv[1])};
        float v[4];
#pragma unroll
        for (int r = 0; r < 4; ++r)
          v[r] = gelu_tanh(__builtin_fmaf(st[1], fa[mi][ni][r], __builtin_fmaf(nrm, cc[r], bb[r])));
        ST8(out0 + tile_off(tok0 + mi * 16, colbase(nb_of(ni)), FF / 16) + l15 * 16 + 4 * g, pack4(v[0], v[1], v[2], v[3]));
      }
    }
  } else {
#pragma unroll
    for (int mi = 0; mi < HM; ++mi) {
      const int tok = tok0 + mi * 16 + l15;
      float sm = 0.f, sq = 0.f;
#pragma unroll
      for (int ni = 0; ni < TN; ++ni) {
        float rr[4] = {bf_lo(resv[mi][ni][0]), bf_hi(resv[mi][ni][0]), bf_lo(resv[mi][ni][1]), bf_hi(resv[mi][ni][1])};
        if (MODE == 3) {
          rr[0] += bf_lo(biasv[ni][0]); rr[1] += bf_hi(biasv[ni][0]); rr[2] += bf_lo(biasv[ni][1]); rr[3] += bf_hi(biasv[ni][1]);
        }
        const u32x2 pk = pack4(rr[0] + fa[mi][ni][0], rr[1] + fa[mi][ni][1], rr[2] + fa[mi][ni][2], rr[3] + fa[mi][ni][3]);
        ST8(out0 + ((flags & 2) ? tile_off(tok0 + mi * 16, colbase(nb_of(ni)), DM / 16) + l15 * 16 + 4 * g
                                : tok * DM + colbase(nb_of(ni)) + 4 * g), pk);
        const float w[4] = {bf_lo(pk[0]), bf_hi(pk[0]), bf_lo(pk[1]), bf_hi(pk[1])};
#pragma unroll
        for (int r = 0; r < 4; ++r) { sm += w[r]; sq = __builtin_fmaf(w[r], w[r], sq); }
      }
      sm = sum_g(sm); sq = sum_g(sq);
      if (g == 0) *(f32x2*)(stats + (size_t)tok * 32 + (nt * 2 + wn) * 2) = f32x2{sm, sq};
    }
  }
  GST(6);
  GST(7);
  GST_FLUSH;
}

constexpr int A_KOFF = 0, A_VOFF = 65536, A_ONES = 131072, A_QOFF = 139264, A_COMB = A_QOFF, A_LDS = A_COMB + 8 * 64 * 40;

#define MFMA32(a, b, c) __builtin_amdgcn_mfma_f32_32x32x16_bf16(a, b, c, 0, 0, 0)
__device__ __forceinline__ bf16x8 exp_pack8(const f32x16& s, int o) {
  bf16x8 p;
#pragma unroll
  for (int r = 0; r < 8; r += 2) {
    f32x2 e = {__builtin_amdgcn_exp2f(s[o + r]), __builtin_amdgcn_exp2f(s[o + r + 1])};
    bf16x2 c = __builtin_convertvector(e, bf16x2);
    p[r] = c[0]; p[r + 1] = c[1];
  }
  return p;
}
#define LDF(p) (*(const LDS_AS bf16x8*)(p))
#define ASTEP(SC, SN, VA, VB, VNA, VNB, KO, VO)                              \
  do {                                                                       \
    SN = MFMA32(kf, qf, negm); SBAR();                                       \
    kf = LDF(kp + (KO)); VNA = LDF(vp + (VO)); VNB = LDF(vp + (VO) + 512); SBAR(); \
    { bf16x8 pa = exp_pack8(SC, 0); SBAR(); o = MFMA32(VA, pa, o); SBAR(); } \
    { bf16x8 pb = exp_pack8(SC, 8); SBAR(); o = MFMA32(VB, pb, o); SBAR(); } \
  } while (0)

__device__ __forceinline__ float max16(const f32x16 s) {
  float a = __builtin_fmaxf(__builtin_fmaxf(s[0], s[1]), s[2]);
  float c = __builtin_fmaxf(__builtin_fmaxf(s[3], s[4]), s[5]);
  a = __builtin_fmaxf(__builtin_fmaxf(a, s[6]), s[7]);
  c = __builtin_fmaxf(__builtin_fmaxf(c, s[8]), s[9]);
  a = __builtin_fmaxf(__builtin_fmaxf(a, s[10]), s[11]);
  c = __builtin_fmaxf(__builtin_fmaxf(c, s[12]), s[13]);
  a = __builtin_fmaxf(__builtin_fmaxf(a, s[14]), s[15]);
  return __builtin_fmaxf(a, c);
}
__device__ __forceinline__ float xhalf_max(float m) {
  auto rr = __builtin_amdgcn_permlane32_swap(__float_as_uint(m), __float_as_uint(m), false, false);
  return __builtin_fmaxf(__uint_as_float(rr[0]), __uint_as_float(rr[1]));
}

__global__ __launch_bounds__(1024, 4) void attn_kernel(const bf16_t* __restrict__ Q, const bf16_t* __restrict__ Kf,
                                                       const bf16_t* __restrict__ Vf, bf16_t* __restrict__ O,
                                                       const char* __restrict__ pf0, int pf0_lines,
                                                       const char* __restrict__ pf1, int pf1_lines) {
  extern __shared__ __attribute__((aligned(16))) char smem_g[];
  LDS_AS char* smem = (LDS_AS char*)smem_g;
  const int tid = threadIdx.x, lane = tid & 63;
  const int wave = __builtin_amdgcn_readfirstlane(tid >> 6);
  const int kh = wave >> 3, qw = wave & 7;
  const int b = blockIdx.x, xcd = b & 7, j = b >> 3;
  const int head = xcd * 4 + (j & 3), qt = j >> 2;
  const int r32 = lane & 31, hh = lane >> 5;
  const int q0 = qt * 256 + qw * 32;

  const unsigned lds0 = (unsigned)(uintptr_t)smem;
    const bf16_t* ks = Kf + (size_t)head * 32768 + kh * 16384 + qw * 512 + lane * 8;
    const bf16_t* vs = Vf + (size_t)head * 32768 + kh * 16384 + qw * 512 + lane * 8;
    const unsigned kd = lds0 + A_KOFF + kh * 32768 + qw * 1024, vd = lds0 + A_VOFF + kh * 32768 + qw * 1024;
    glds16(Q + ((size_t)head * SEQ + q0 + r32) * 16 + hh * 8, lds0 + A_QOFF + qw * 1024);
    glds16(ks, kd);
    glds16(vs, vd);

  if (tid < 448) {
    const unsigned v = (tid & 31) == 0 ? 0x3F803F80u : 0u;
    *(LDS_AS u32x4*)(smem + A_ONES + tid * 16) = u32x4{v, v, v, v};
  }

  LDS_AS char* kb = smem + A_KOFF + kh * 32768 + lane * 16;
  const bool real = r32 < 16;
  LDS_AS char* vb = real ? (smem + A_VOFF + kh * 32768 + hh * 256 + r32 * 16) : (smem + A_ONES + (r32 == 16 ? 0 : 16));
  const int vstride = real ? 2048 : 0;

  wait_bar<0>();
  const bf16x8 qf = LDF(smem + A_QOFF + qw * 1024 + lane * 16);
  f32x16 negm;
  float mref;
  {
    f32x16 z = {};
    f32x16 s = MFMA32(LDF(smem + A_KOFF + lane * 16), qf, z);
    mref = xhalf_max(max16(s));
#pragma unroll
    for (int r = 0; r < 16; ++r) negm[r] = -mref;
  }
  f32x16 o = {};
  {
    LDS_AS char* kp = kb;
    LDS_AS char* vp = vb;
    bf16x8 kf = LDF(kp), va0 = LDF(vp), vb0 = LDF(vp + 512), va1, vb1;
    f32x16 sA = MFMA32(kf, qf, negm), sB;
    kf = LDF(kp + 1024);
    SBAR();
#pragma unroll 1
    for (int t = 0; t < 8; t += 2) {
      if (t < 6) {
        if ((wave >> 2) == (t >> 1)) {
          const int c = (t >> 1) + 1, w2 = (wave & 3) * 2;
          const bf16_t* k2 = Kf + (size_t)head * 32768 + c * 4096 + w2 * 512 + lane * 8;
          const bf16_t* v2 = Vf + (size_t)head * 32768 + c * 4096 + w2 * 512 + lane * 8;
          const unsigned d2 = lds0 + c * 8192 + w2 * 1024;
#pragma unroll
          for (int hf = 0; hf < 2; ++hf)
#pragma unroll
            for (int i = 0; i < 2; ++i) {
              glds16(k2 + hf * 16384 + i * 512, d2 + A_KOFF + hf * 32768 + i * 1024);
              glds16(v2 + hf * 16384 + i * 512, d2 + A_VOFF + hf * 32768 + i * 1024);
            }
        }
      } else {
        wait_bar<0>();
      }
      ASTEP(sA, sB, va0, vb0, va1, vb1, 2048, 1024);
      ASTEP(sB, sA, va1, vb1, va0, vb0, 3072, 2048);
      kp += 2048; vp += vstride;
    }
    {
      const int line = j * 1024 + tid;
      const char* pp = line < pf0_lines ? pf0 + (size_t)line * 128 : pf1 + (size_t)(line - pf0_lines) * 128;
      if (line < pf0_lines + pf1_lines) touch(pp, lds0 + A_ONES + 7168);
    }
#pragma unroll 1
    for (int it = 0; it < 6; ++it) {
      {
        const int pr = (it + (wave >> 2)) & 3;
        if (pr == 0) __builtin_amdgcn_s_setprio(0);
        else if (pr == 1) __builtin_amdgcn_s_setprio(1);
        else if (pr == 2) __builtin_amdgcn_s_setprio(2);
        else __builtin_amdgcn_s_setprio(3);
      }
      ASTEP(sA, sB, va0, vb0, va1, vb1, 2048, 1024);
      ASTEP(sB, sA, va1, vb1, va0, vb0, 3072, 2048);
      ASTEP(sA, sB, va0, vb0, va1, vb1, 4096, 3072);
      ASTEP(sB, sA, va1, vb1, va0, vb0, 5120, 4096);
      kp += 4096; vp += 2 * vstride;
    }
    __builtin_amdgcn_s_setprio(0);
  }
  if (__builtin_expect(__any(!(o[8] < 1.2676506e30f)), 0)) {
    float m = -INFINITY;
    f32x16 z = {};
#pragma unroll 1
    for (int t = 0; t < 32; ++t) {
      f32x16 sc = MFMA32(LDF(kb + t * 1024), qf, z);
      m = __builtin_fmaxf(m, max16(sc));
    }
    mref = xhalf_max(m);
#pragma unroll
    for (int r = 0; r < 16; ++r) negm[r] = -mref;
    o = z;
#pragma unroll 1
    for (int t = 0; t < 32; ++t) {
      const f32x16 sc = MFMA32(LDF(kb + t * 1024), qf, negm);
      LDS_AS char* vq = vb + (vstride >> 1) * t;
      o = MFMA32(LDF(vq), exp_pack8(sc, 0), o);
      o = MFMA32(LDF(vq + 512), exp_pack8(sc, 8), o);
    }
  }

  {
    auto rr = __builtin_amdgcn_permlane32_swap(__float_as_uint(o[8]), __float_as_uint(o[8]), false, false);
    o[8] = __uint_as_float(rr[0]);
  }
  LDS_AS float* cb = (LDS_AS float*)(smem + A_COMB + qw * (64 * 40) + lane * 4);
  if (kh) {
#pragma unroll
    for (int r = 0; r < 9; ++r) cb[r * 64] = o[r];
    cb[9 * 64] = mref;
  }
  bar_lgkm();
  if (!kh) {
    const float m2 = cb[9 * 64];
    const float mm = __builtin_fmaxf(mref, m2);
    const float f1 = __builtin_amdgcn_exp2f(mref - mm), f2 = __builtin_amdgcn_exp2f(m2 - mm);
    const float l = o[8] * f1 + cb[8 * 64] * f2;
    const float inv = __builtin_amdgcn_rcpf(l);
    const float a1 = f1 * inv, a2 = f2 * inv;
    float v[8];
#pragma unroll
    for (int r = 0; r < 8; ++r) v[r] = o[r] * a1 + cb[r * 64] * a2;
    bf16_t* op = O + tile_off(q0 + r32, head * 16, DM / 16) + (r32 & 15) * 16 + 4 * hh;
    *(u32x2*)(op) = pack4(v[0], v[1], v[2], v[3]);
    *(u32x2*)(op + 8) = pack4(v[4], v[5], v[6], v[7]);
  }
  asm volatile("s_waitcnt vmcnt(0)" ::: "memory");
}

constexpr int QKV_NST = 4, WO_NST = 6, F1_NST = 5, F2_NST = 6;
#define NL_SMALL 4
#define QKV0_K gemm_kernel<64, 192, 1, 4, 512, 1536, QKV_NST, 0, 1, 4, 0>
#define QKV1_K gemm_kernel<64, 192, 1, 4, 512, 1536, QKV_NST, 0, 0, 4, 1>
#define WO_K gemm_kernel<64, 64, 2, 2, 512, 512, WO_NST, 1, 0, NL_SMALL, 1>
#define F1_K gemm_kernel<128, 112, 4, 1, 512, 1792, F1_NST, 2, 0, 4, 1>
#define F2_K gemm_kernel<64, 64, 2, 2, 1792, 512, F2_NST, 3, 0, NL_SMALL, 1>
constexpr int QKV_LDS = QKV_NST * (64 + 192) * 128 + 4096, WO_LDS = WO_NST * 128 * 128 + 4096,
              F1_LDS = F1_NST * (128 + 112) * 128 + 4096, F2_LDS = F2_NST * 128 * 128 + 4096;

extern "C" void kernel_launch(void* const* d_in, const int* in_sizes, int n_in, void* d_out, int out_size, void* d_ws,
                              size_t ws_size, hipStream_t stream) {
  const bf16_t* x = (const bf16_t*)d_in[0];
  const bf16_t* wqkv = (const bf16_t*)d_in[1];
  const bf16_t* wo = (const bf16_t*)d_in[2];
  const bf16_t* w1 = (const bf16_t*)d_in[4];
  const bf16_t* b1 = (const bf16_t*)d_in[5];
  const bf16_t* w2 = (const bf16_t*)d_in[6];
  const bf16_t* b2 = (const bf16_t*)d_in[7];
  char* ws = (char*)d_ws;
  bf16_t* Qb = (bf16_t*)(ws + (0 << 20));
  bf16_t* Kb = (bf16_t*)(ws + (2 << 20));
  bf16_t* Vb = (bf16_t*)(ws + (4 << 20));
  bf16_t* Ob = (bf16_t*)(ws + (6 << 20));
  bf16_t* X1 = (bf16_t*)(ws + (8 << 20));
  bf16_t* Hb = (bf16_t*)(ws + (10 << 20));
  bf16_t* X2 = (bf16_t*)(ws + (18 << 20));
  float* St1 = (float*)(ws + (20 << 20));
  float* St2 = (float*)(ws + (21 << 20));
  bf16_t* out = (bf16_t*)d_out;

  hipFuncSetAttribute((const void*)QKV0_K, hipFuncAttributeMaxDynamicSharedMemorySize, QKV_LDS);
  hipFuncSetAttribute((const void*)QKV1_K, hipFuncAttributeMaxDynamicSharedMemorySize, QKV_LDS);
  hipFuncSetAttribute((const void*)WO_K, hipFuncAttributeMaxDynamicSharedMemorySize, WO_LDS);
  hipFuncSetAttribute((const void*)F1_K, hipFuncAttributeMaxDynamicSharedMemorySize, F1_LDS);
  hipFuncSetAttribute((const void*)F2_K, hipFuncAttributeMaxDynamicSharedMemorySize, F2_LDS);
  hipFuncSetAttribute((const void*)attn_kernel, hipFuncAttributeMaxDynamicSharedMemorySize, A_LDS);

  for (int layer = 0; layer < 2; ++layer) {
    const bf16_t* xin = layer == 0 ? x : X2;
    bf16_t* xout = layer == 0 ? X2 : out;
    const char* none = (const char*)x;
    if (layer == 0) QKV0_K<<<256, 768, QKV_LDS, stream>>>(xin, wqkv, nullptr, nullptr, Qb, Kb, Vb, nullptr, none, 0, 0);
    else            QKV1_K<<<256, 768, QKV_LDS, stream>>>(xin, wqkv, nullptr, nullptr, Qb, Kb, Vb, St2, none, 0, 0);
    attn_kernel<<<256, 1024, A_LDS, stream>>>(Qb, Kb, Vb, Ob, (const char*)wo, DM * DM * 2 / 128, (const char*)w1, DM * FF * 2 / 128);
    WO_K<<<256, 512 + 64 * NL_SMALL, WO_LDS, stream>>>(Ob, wo, nullptr, xin, X1, nullptr, nullptr, St1, none, 0, (layer ? 1 : 0) | 2);
    F1_K<<<256, 768, F1_LDS, stream>>>(X1, w1, b1, nullptr, Hb, nullptr, nullptr, St1, (const char*)w2, FF * DM * 2 / 128, 0);
    F2_K<<<256, 512 + 64 * NL_SMALL, F2_LDS, stream>>>(Hb, w2, b2, X1, xout, nullptr, nullptr, St2, (const char*)wqkv,
                                       layer == 0 ? DM * 3 * DM * 2 / 128 : 0, 1 | (layer ? 0 : 2));
  }
}
```

```cpp
#include <hip/hip_runtime.h>
#include <hip/hip_bf16.h>
#include <stdint.h>

typedef __bf16 bf16_t;
typedef __bf16 bf16x2 __attribute__((ext_vector_type(2)));
typedef __bf16 bf16x4 __attribute__((ext_vector_type(4)));
typedef __bf16 bf16x8 __attribute__((ext_vector_type(8)));
typedef float f32x2 __attribute__((ext_vector_type(2)));
typedef float f32x4 __attribute__((ext_vector_type(4)));
typedef float f32x16 __attribute__((ext_vector_type(16)));
typedef unsigned u32x2 __attribute__((ext_vector_type(2)));
typedef unsigned u32x4 __attribute__((ext_vector_type(4)));

#define LDS_AS __attribute__((address_space(3)))
#define GLB_AS __attribute__((address_space(1)))

constexpr int SEQ = 2048, DM = 512, NH = 32, DH = 16, FF = 1792;

__device__ __forceinline__ void glds16(const void* g, unsigned lds_base) {
  unsigned keep;
  asm volatile("s_mov_b32 %0, m0\n\ts_mov_b32 m0, %2\n\ts_nop 0\n\tglobal_load_lds_dwordx4 %1, off\n\ts_mov_b32 m0, %0"
               : "=&s"(keep) : "v"(g), "s"(lds_base) : "memory");
}
template <int IMM>
__device__ __forceinline__ void glds16s(unsigned voff, const void* sbase, unsigned lds_base) {
  asm volatile("s_add_i32 m0, %2, %3\n\ts_nop 0\n\tglobal_load_lds_dwordx4 %0, %1"
               :: "v"(voff), "s"(sbase), "s"(lds_base), "i"(IMM) : "memory", "scc");
}
__device__ __forceinline__ void touch(const void* p, unsigned lds_dummy) {
  unsigned keep;
  asm volatile("s_mov_b32 %0, m0\n\ts_mov_b32 m0, %2\n\ts_nop 0\n\tglobal_load_lds_dword %1, off\n\ts_mov_b32 m0, %0"
               : "=&s"(keep) : "v"(p), "s"(lds_dummy) : "memory");
}
#define ST8(ptr, val) (*(u32x2*)(ptr) = (val))
#define SBAR() __builtin_amdgcn_sched_barrier(0)
#define PIN(x) asm volatile("" : "+v"(x))
__device__ __forceinline__ int tile_off(int tok, int col, int nc16) { return ((tok >> 4) * nc16 + (col >> 4)) * 256; }
__device__ __forceinline__ float rbf(float v) { return (float)(__bf16)v; }
__device__ __forceinline__ u32x2 pack4(float a, float b, float c, float d) {
  f32x2 lo = {a, b}, hi = {c, d};
  bf16x2 l = __builtin_convertvector(lo, bf16x2), h = __builtin_convertvector(hi, bf16x2);
  u32x2 r = {__builtin_bit_cast(unsigned, l), __builtin_bit_cast(unsigned, h)};
  return r;
}
__device__ __forceinline__ float bf_lo(unsigned u) { return __builtin_bit_cast(float, u << 16); }
__device__ __forceinline__ float bf_hi(unsigned u) { return __builtin_bit_cast(float, u & 0xffff0000u); }

#define GST(i)
#define GST_DECL
#define GST_FLUSH
template <int N>
__device__ __forceinline__ void wait_bar() {
  asm volatile("s_waitcnt vmcnt(%0) lgkmcnt(0)\n\ts_barrier" ::"i"(N) : "memory");
}

template <int I> struct IC { static constexpr int value = I; };
template <int I, int N, class F>
__device__ __forceinline__ void static_for(F& f) {
  if constexpr (I < N) { f(IC<I>{}); static_for<I + 1, N>(f); }
}

__device__ __forceinline__ float gelu_tanh(float x) {
  const float c1 = -2.0f * 0.7978845608028654f * 1.4426950408889634f;
  const float c2 = c1 * 0.044715f;
  float t = x * __builtin_fmaf(x * x, c2, c1);
  float e = __builtin_amdgcn_exp2f(t);
  return x * __builtin_amdgcn_rcpf(1.0f + e);
}

template <int N>
__device__ __forceinline__ void wait_vm() { asm volatile("s_waitcnt vmcnt(%0)" ::"i"(N) : "memory"); }
__device__ __forceinline__ void bar_lgkm() { asm volatile("s_waitcnt lgkmcnt(0)\n\ts_barrier" ::: "memory"); }
#define MFMA16(a, b, c) __builtin_amdgcn_mfma_f32_16x16x32_bf16(a, b, c, 0, 0, 0)
__device__ __forceinline__ float sum_g(float v) {
  auto a = __builtin_amdgcn_permlane16_swap(__float_as_uint(v), __float_as_uint(v), false, false);
  v = __uint_as_float(a[0]) + __uint_as_float(a[1]);
  auto b = __builtin_amdgcn_permlane32_swap(__float_as_uint(v), __float_as_uint(v), false, false);
  return __uint_as_float(b[0]) + __uint_as_float(b[1]);
}

template <int BM, int BN, int WVM, int WVN, int K, int LDW, int NST, int MODE, int SELF, int NL, int XT>
__global__ __launch_bounds__(512 + 64 * NL, 2 + NL / 4) void gemm_kernel(const bf16_t* __restrict__ X, const bf16_t* __restrict__ W,
                                                      const bf16_t* __restrict__ bias, const bf16_t* __restrict__ res,
                                                      bf16_t* __restrict__ out0, bf16_t* __restrict__ out1,
                                                      bf16_t* __restrict__ out2, float* __restrict__ stats,
                                                      const char* __restrict__ pf, int pf_lines, int flags) {
  extern __shared__ __attribute__((aligned(16))) char smem_g[];
  LDS_AS char* smem = (LDS_AS char*)smem_g;
  constexpr int TM = BM / WVM / 16, TN = BN / WVN / 16, NB = BN / 16, HM = TM / 2;
  constexpr int XB = BM * 128, WB = BN * 128, STB = XB + WB;
  constexpr int NXI = BM / 8, NI = (BM + BN) / 8, LPL = (NI + NL - 1) / NL, NK = K / 64;
  constexpr int NFULL = NI - NL * (LPL - 1);
#define DEP_BIG 2
#define DEP_SMALL 3
  constexpr int DEP = (STB >= 28 * 1024) ? DEP_BIG : DEP_SMALL;
  static_assert(NST >= DEP + 1, "ring too small");
  constexpr bool LN = (MODE == 0 || MODE == 2);
  constexpr bool GRP = (NB % 4 == 0);
  constexpr int NSEG = NB / 4;
  constexpr int NCS = (NB + 3) / 4;
  constexpr bool PFK = (MODE >= 2);
  constexpr int NSH = (MODE == 2) ? 2 : 0;
  constexpr int NX4 = HM * TN + NSH;
  constexpr int EX_OFF = NST * STB;
  constexpr int CS_OFF = EX_OFF;
  constexpr int ST_OFF = CS_OFF + 1024;
  constexpr int PF_OFF = ST_OFF + 1024;
  constexpr int BI_OFF = PF_OFF + 256;
  static_assert(8 * NX4 * 1024 <= ((NK - 1) % NST) * STB, "exchange area overlaps the last stage");

  const int tid = threadIdx.x, lane = tid & 63;
  const int wave = __builtin_amdgcn_readfirstlane(tid >> 6);
  const int b = blockIdx.x, xcd = b & 7, j = b >> 3;
  int mt, nt;
  if (BM == 128) { mt = xcd * 2 + (j & 1); nt = j >> 1; }
  else           { mt = xcd * 4 + (j & 3); nt = j >> 2; }
  GST_DECL; GST(0);
  const int m0 = mt * BM;
  const int l15 = lane & 15, g = lane >> 4;
  const int q_ = (lane & 15) >> 2, p_ = lane & 3;
  const __bf16 one = (__bf16)1.0f;
  const bf16x8 ones8 = {one, one, one, one, one, one, one, one};

  auto colbase = [&](int nb) -> int {
    if (MODE == 0) return (nb >> 2) * 512 + (nt * 4 + (nb & 3)) * 16;
    return nt * BN + nb * 16;
  };
  auto waddr = [&](int kb, int nb) -> int {
    if (GRP) return XB + (kb * NSEG + (nb >> 2)) * 1024 + q_ * 128 + (((nb & 3) ^ (((q_ >> 1) & 1) | ((kb & 1) << 1))) * 32) + p_ * 8;
    return XB + (kb * NB + nb) * 256 + (q_ + 4 * (kb & 1)) * 32 + p_ * 8;
  };
  auto wt1 = [&](int a) -> int { return GRP ? a + 512 : a ^ 128; };
  auto tr8 = [&](LDS_AS char* p0, LDS_AS char* p1) -> bf16x8 {
    bf16x4 a = __builtin_amdgcn_ds_read_tr16_b64_v4bf16((LDS_AS bf16x4*)p0);
    bf16x4 c = __builtin_amdgcn_ds_read_tr16_b64_v4bf16((LDS_AS bf16x4*)p1);
    return __builtin_shufflevector(a, c, 0, 1, 2, 3, 4, 5, 6, 7);
  };

  auto ffn1_unit = [&](int tl, int nb, const f32x4 fa) {
    const f32x2 st = *(const LDS_AS f32x2*)(smem + ST_OFF + (tl + l15) * 8);
    const float nrm = -st[0] * st[1];
    const f32x4 cc = *(const LDS_AS f32x4*)(smem + CS_OFF + nb * 64 + g * 16);
    const u32x2 bv = *(const LDS_AS u32x2*)(smem + BI_OFF + nb * 32 + g * 8);
    const float bb[4] = {bf_lo(bv[0]), bf_hi(bv[0]), bf_lo(bv[1]), bf_hi(bv[1])};
    float v[4];
#pragma unroll
    for (int r = 0; r < 4; ++r) v[r] = gelu_tanh(__builtin_fmaf(st[1], fa[r], __builtin_fmaf(nrm, cc[r], bb[r])));
    ST8(out0 + tile_off(m0 + tl, nt * BN + nb * 16, FF / 16) + l15 * 16 + 4 * g, pack4(v[0], v[1], v[2], v[3]));
  };

  if (wave >= 8) {
    const int lw = wave - 8;
    __builtin_amdgcn_s_setprio(3);
    constexpr int XS = NXI / NL;
    unsigned voff[LPL];
    {
      const int row = 8 * lw + (lane >> 3);
      const int ch = (lane & 7) ^ ((row >> 1) & 7);
      if (XT) voff[0] = (unsigned)((lw >> 1) * (K / 16) * 512 + (lw & 1) * 1024 + lane * 16);
      else    voff[0] = (unsigned)(row * K + ch * 8) * 2u;
#pragma unroll
      for (int i = 1; i < XS; ++i) voff[i] = voff[0] + i * (8 * NL) * K * 2;
#pragma unroll
      for (int i = XS; i < LPL; ++i) {
        const int wi = lw + NL * (i - XS);
        if (GRP) {
          const int kb = wi / NSEG, seg = wi % NSEG, r = lane >> 3;
          const int c = ((lane & 7) >> 1) ^ (((r >> 1) & 1) | ((kb & 1) << 1));
          voff[i] = (unsigned)((kb * 8 + r) * LDW + colbase(seg * 4 + c) + (lane & 1) * 8) * 2u;
        } else {
          const int sidx = 4 * wi + (lane >> 4);
          const int kb = sidx / NB, nb = sidx % NB;
          const int krow = kb * 8 + (((lane >> 1) & 7) ^ ((kb & 1) << 2));
          voff[i] = (unsigned)(krow * LDW + colbase(nb) + (lane & 1) * 8) * 2u;
        }
      }
    }
    const char* xs_ = (const char*)(X + (size_t)m0 * K);
    const char* ws_ = (const char*)W;
    const unsigned lds0 = (unsigned)(uintptr_t)smem + lw * 1024;
    const bool full = lw < NFULL;
    auto issue = [&](auto ST) {
      constexpr int st = decltype(ST)::value;
      auto one_ = [&](auto I) {
        constexpr int i = decltype(I)::value;
        if (i < LPL - 1 || NFULL == NL || full)
          glds16s<(st % NST) * STB + i * (1024 * NL)>(voff[i], i < XS ? xs_ + st * (XT ? 2048 : 128) : ws_ + (size_t)st * (64 * LDW * 2), lds0);
      };
      static_for<0, LPL>(one_);
    };
    static_for<0, (DEP < NK ? DEP : NK)>(issue);

    constexpr int RS = BM / 64;
    int ca[NCS][2];
#pragma unroll
    for (int c = 0; c < NCS; ++c) {
      const int nb = (lw + 4 * c < NB) ? lw + 4 * c : NB - 1;
      ca[c][0] = waddr(g, nb); ca[c][1] = waddr(4 + g, nb);
    }
    const int xo = (lw * RS * 16 + l15) * 128 + ((g ^ (l15 >> 1)) * 16);
    f32x4 cs[NCS], rs[RS], rq[RS];
#pragma unroll
    for (int c = 0; c < NCS; ++c) cs[c] = f32x4{0.f, 0.f, 0.f, 0.f};
#pragma unroll
    for (int mi = 0; mi < RS; ++mi) rs[mi] = rq[mi] = f32x4{0.f, 0.f, 0.f, 0.f};
    auto lstep = [&](auto KT) {
      constexpr int kt = decltype(KT)::value;
      constexpr int last = (kt + DEP - 1 < NK - 1) ? kt + DEP - 1 : NK - 1;
      constexpr int tch = (PFK && kt + DEP > NK - 1) ? 2 : 0;
      if (NFULL == NL || full) wait_vm<(last - kt) * LPL + tch>(); else wait_vm<(last - kt) * (LPL - 1) + tch>();
      bar_lgkm();
      if constexpr (kt + DEP < NK) issue(IC<kt + DEP>{});
      if constexpr (PFK && kt + DEP == NK - 1) {
#pragma unroll
        for (int i = 0; i < 2; ++i) {
          int line = j * 512 + i * 256 + lw * 64 + lane;
          line = line < pf_lines ? line : 0;
          touch(pf + (size_t)line * 128, (unsigned)(uintptr_t)smem + PF_OFF);
        }
      }
      if (LN) {
        LDS_AS char* base = smem + (kt % NST) * STB;
#pragma unroll
        for (int ks = 0; ks < 2; ++ks) {
#pragma unroll
          for (int c = 0; c < NCS; ++c) cs[c] = MFMA16(tr8(base + ca[c][ks], base + wt1(ca[c][ks])), ones8, cs[c]);
          if (SELF) {
#pragma unroll
            for (int mi = 0; mi < RS; ++mi) {
              const bf16x8 xf = *(const LDS_AS bf16x8*)(base + ((xo ^ (ks * 64)) + mi * 2048));
              rs[mi] = MFMA16(ones8, xf, rs[mi]);
              rq[mi] = MFMA16(xf, xf, rq[mi]);
            }
          }
        }
      }
    };
    static_for<0, NK>(lstep);
    if (LN) {
      if (l15 == 0) {
#pragma unroll
        for (int c = 0; c < NCS; ++c)
          *(LDS_AS f32x4*)(smem + CS_OFF + ((lw + 4 * c < NB) ? lw + 4 * c : NB - 1) * 64 + g * 16) = cs[c];
      }
      if (SELF) {
        if (g == (l15 >> 2)) {
          const int r = l15 & 3;
#pragma unroll
          for (int mi = 0; mi < RS; ++mi) {
            const float sq = r == 0 ? rq[mi][0] : r == 1 ? rq[mi][1] : r == 2 ? rq[mi][2] : rq[mi][3];
            const float m = rs[mi][0] * (1.0f / K);
            const float v = __builtin_fmaxf(sq * (1.0f / K) - m * m, 0.f);
            *(LDS_AS f32x2*)(smem + ST_OFF + (lw * RS * 16 + mi * 16 + l15) * 8) = f32x2{m, __builtin_amdgcn_rsqf(v + 1e-5f)};
          }
        }
      }
    }
    bar_lgkm();
    if (MODE == 2) {
      __builtin_amdgcn_s_setprio(0);
#pragma unroll
      for (int u = 0; u < 2; ++u) {
        const int w = 2 * lw + u;
        const int tl = (w & 3) * TM * 16 + (w >> 2) * HM * 16;
        const LDS_AS f32x4* own = (const LDS_AS f32x4*)(smem + w * (NX4 * 1024) + lane * 16);
        const LDS_AS f32x4* oth = (const LDS_AS f32x4*)(smem + (w ^ 4) * (NX4 * 1024) + lane * 16);
#pragma unroll
        for (int e = 0; e < NSH; ++e) ffn1_unit(tl, TN - NSH + e, own[(TN + e) * 64] + oth[(TN - NSH + e) * 64]);
      }
    }
    if (PFK) asm volatile("s_waitcnt vmcnt(0)" ::: "memory");
    return;
  }

  const int kg = wave >> 2, w4 = wave & 3;
  const int wm = w4 / WVN, wn = w4 % WVN;
  auto nb_of = [&](int ni) -> int {
    if (MODE == 0) return 4 * ni + wn;
    return wn * TN + ni;
  };
  const int tl0 = wm * TM * 16 + kg * HM * 16;
  const int tok0 = m0 + tl0;

  if (LN && !SELF) {
    if (wave < BM / 64) {
      float sm = 0.f, sq = 0.f;
#pragma unroll
      for (int i = 0; i < 8; ++i) {
        const f32x4 t = *(const f32x4*)(stats + (size_t)(m0 + tid) * 32 + i * 4);
        sm += t[0] + t[2]; sq += t[1] + t[3];
      }
      const float m = sm * (1.0f / K);
      const float v = __builtin_fmaxf(sq * (1.0f / K) - m * m, 0.f);
      *(LDS_AS f32x2*)(smem + ST_OFF + tid * 8) = f32x2{m, __builtin_amdgcn_rsqf(v + 1e-5f)};
    }
  }
  if (MODE == 2) {
    if (wave == 2 && lane < BN / 4) *(LDS_AS u32x2*)(smem + BI_OFF + lane * 8) = *(const u32x2*)(bias + nt * BN + lane * 4);
  }
  u32x2 resv[HM][TN];
  u32x2 biasv[TN];
  if (MODE == 1 || MODE == 3) {
#pragma unroll
    for (int mi = 0; mi < HM; ++mi)
#pragma unroll
      for (int ni = 0; ni < TN; ++ni)
        resv[mi][ni] = *(const u32x2*)(res + ((flags & 1) ? tile_off(tok0 + mi * 16, colbase(nb_of(ni)), DM / 16) + l15 * 16 + 4 * g
                                                             : (tok0 + mi * 16 + l15) * DM + colbase(nb_of(ni)) + 4 * g));
  }
  if (MODE == 3) {
#pragma unroll
    for (int ni = 0; ni < TN; ++ni) biasv[ni] = *(const u32x2*)(bias + colbase(nb_of(ni)) + 4 * g);
  }

  const int xo_ = XT ? wm * TM * 2048 + (kg * 2 + (g >> 1)) * 512 + l15 * 32 + (g & 1) * 16
                     : ((wm * TM * 16 + l15) * 128 + ((g ^ (l15 >> 1)) * 16)) ^ (kg * 64);
  const int xoA = xo_ + kg * HM * 2048, xoB = xo_ + (1 - kg) * HM * 2048;
  int wr_[TN];
#pragma unroll
  for (int ni = 0; ni < TN; ++ni) wr_[ni] = GRP ? waddr(kg * 4 + g, nb_of(ni)) : waddr(kg * 4 + g, nb_of(0)) + ni * 256;
  const int wr1_ = wt1(wr_[0]);

  f32x4 acc[TM][TN];
#pragma unroll
  for (int mi = 0; mi < TM; ++mi)
#pragma unroll
    for (int ni = 0; ni < TN; ++ni) acc[mi][ni] = f32x4{0.f, 0.f, 0.f, 0.f};

  bf16x8 xf[2][TM], wf[2][TN];
  auto ldfrags = [&](auto KT) {
    constexpr int kt = decltype(KT)::value, p = kt & 1;
    LDS_AS char* base = smem + (kt % NST) * STB;
#pragma unroll
    for (int mi = 0; mi < TM; ++mi) xf[p][mi] = *(const LDS_AS bf16x8*)(base + (mi < HM ? xoA + mi * 2048 : xoB + (mi - HM) * 2048));
#pragma unroll
    for (int ni = 0; ni < TN; ++ni) wf[p][ni] = tr8(base + wr_[ni], base + (GRP ? wr_[ni] + 512 : wr1_ + ni * 256));
  };
  GST(1);
  bar_lgkm();
  GST(2);
  ldfrags(IC<0>{});
#define LATE_N 100
  auto kstep = [&](auto KT) {
    constexpr int kt = decltype(KT)::value, p = kt & 1;
    constexpr bool late = (kt + 1 < NK) && (kt + 1 >= NK - LATE_N);
    if constexpr (kt + 1 < NK && !late) {
      bar_lgkm();
      ldfrags(IC<kt + 1>{});
    }
    SBAR();
#pragma unroll
    for (int mj = 0; mj < TM; ++mj)
#pragma unroll
      for (int ni = 0; ni < TN; ++ni) {
        const int mi = (mj + HM) % TM;
        if (MODE == 0 && ni == 2) acc[mi][ni] = MFMA16(xf[p][mi], wf[p][ni], acc[mi][ni]);
        else                      acc[mi][ni] = MFMA16(wf[p][ni], xf[p][mi], acc[mi][ni]);
      }
    SBAR();
    if constexpr (late) {
      bar_lgkm();
      ldfrags(IC<kt + 1>{});
      SBAR();
    }
  };
  static_for<0, NK>(kstep);

  GST(3);
  {
    LDS_AS f32x4* wr = (LDS_AS f32x4*)(smem + wave * (NX4 * 1024) + lane * 16);
#pragma unroll
    for (int mi = 0; mi < HM; ++mi)
#pragma unroll
      for (int ni = 0; ni < TN; ++ni) wr[(mi * TN + ni) * 64] = acc[HM + mi][ni];
#pragma unroll
    for (int e = 0; e < NSH; ++e) wr[(HM * TN + e) * 64] = acc[0][TN - NSH + e];
  }
  GST(4);
  bar_lgkm();
  GST(5);
  f32x4 fa[HM][TN];
  {
    const LDS_AS f32x4* rd = (const LDS_AS f32x4*)(smem + (wave ^ 4) * (NX4 * 1024) + lane * 16);
#pragma unroll
    for (int mi = 0; mi < HM; ++mi)
#pragma unroll
      for (int ni = 0; ni < TN - NSH; ++ni) fa[mi][ni] = acc[mi][ni] + rd[(mi * TN + ni) * 64];
  }
  auto tok_stats = [&](int tl) -> f32x2 { return *(const LDS_AS f32x2*)(smem + ST_OFF + tl * 8); };
  auto col_sum4 = [&](int nb) -> f32x4 { return *(const LDS_AS f32x4*)(smem + CS_OFF + nb * 64 + g * 16); };

  if (MODE == 0) {
    const int head = nt * 4 + wn;
    const float qscale = 0.25f * 1.4426950408889634f;
    const f32x4 cq = col_sum4(nb_of(0)), ck = col_sum4(nb_of(1));
    const float cv = *(const LDS_AS float*)(smem + CS_OFF + nb_of(2) * 64 + l15 * 4);
#pragma unroll
    for (int mi = 0; mi < HM; ++mi) {
      const int tok = tok0 + mi * 16 + l15;
      const f32x2 st = tok_stats(tl0 + mi * 16 + l15);
      {
        const float sc = st[1] * qscale;
        float v[4];
#pragma unroll
        for (int r = 0; r < 4; ++r) v[r] = sc * __builtin_fmaf(-st[0], cq[r], fa[mi][0][r]);
        ST8(out0 + ((size_t)head * SEQ + tok) * 16 + 4 * g, pack4(v[0], v[1], v[2], v[3]));
      }
      {
        float v[4];
#pragma unroll
        for (int r = 0; r < 4; ++r) v[r] = st[1] * __builtin_fmaf(-st[0], ck[r], fa[mi][1][r]);
        ST8(out1 + (size_t)head * 32768 + (tok >> 5) * 512 + (g >> 1) * 256 + (tok & 31) * 8 + (g & 1) * 4,
            pack4(v[0], v[1], v[2], v[3]));
      }
      {
        float v[4];
#pragma unroll
        for (int r = 0; r < 4; ++r) {
          const f32x2 sr = tok_stats(tl0 + mi * 16 + 4 * g + r);
          v[r] = sr[1] * __builtin_fmaf(-sr[0], cv, fa[mi][2][r]);
        }
        const int ks16 = (tok0 + mi * 16) >> 4;
        ST8(out2 + (size_t)head * 32768 + ks16 * 256 + (g & 1) * 128 + l15 * 8 + (g >> 1) * 4,
            pack4(v[0], v[1], v[2], v[3]));
      }
    }
  } else if (MODE == 2) {
#pragma unroll
    for (int ni = 0; ni < TN - NSH; ++ni) ffn1_unit(tl0, ni, fa[0][ni]);
  } else {
#pragma unroll
    for (int mi = 0; mi < HM; ++mi) {
      const int tok = tok0 + mi * 16 + l15;
      float sm = 0.f, sq = 0.f;
#pragma unroll
      for (int ni = 0; ni < TN; ++ni) {
        float rr[4] = {bf_lo(resv[mi][ni][0]), bf_hi(resv[mi][ni][0]), bf_lo(resv[mi][ni][1]), bf_hi(resv[mi][ni][1])};
        if (MODE == 3) {
          rr[0] += bf_lo(biasv[ni][0]); rr[1] += bf_hi(biasv[ni][0]); rr[2] += bf_lo(biasv[ni][1]); rr[3] += bf_hi(biasv[ni][1]);
        }
        const u32x2 pk = pack4(rr[0] + fa[mi][ni][0], rr[1] + fa[mi][ni][1], rr[2] + fa[mi][ni][2], rr[3] + fa[mi][ni][3]);
        ST8(out0 + ((flags & 2) ? tile_off(tok0 + mi * 16, colbase(nb_of(ni)), DM / 16) + l15 * 16 + 4 * g
                                : tok * DM + colbase(nb_of(ni)) + 4 * g), pk);
        const float w[4] = {bf_lo(pk[0]), bf_hi(pk[0]), bf_lo(pk[1]), bf_hi(pk[1])};
#pragma unroll
        for (int r = 0; r < 4; ++r) { sm += w[r]; sq = __builtin_fmaf(w[r], w[r], sq); }
      }
      sm = sum_g(sm); sq = sum_g(sq);
      if (g == 0) *(f32x2*)(stats + (size_t)tok * 32 + (nt * 2 + wn) * 2) = f32x2{sm, sq};
    }
  }
  GST(6);
  GST(7);
  GST_FLUSH;
}

constexpr int A_KOFF = 0, A_VOFF = 65536, A_ONES = 131072, A_QOFF = 139264, A_COMB = A_QOFF, A_LDS = A_COMB + 8 * 64 * 40;

#define MFMA32(a, b, c) __builtin_amdgcn_mfma_f32_32x32x16_bf16(a, b, c, 0, 0, 0)
__device__ __forceinline__ bf16x8 exp_pack8(const f32x16& s, int o) {
  bf16x8 p;
#pragma unroll
  for (int r = 0; r < 8; r += 2) {
    f32x2 e = {__builtin_amdgcn_exp2f(s[o + r]), __builtin_amdgcn_exp2f(s[o + r + 1])};
    bf16x2 c = __builtin_convertvector(e, bf16x2);
    p[r] = c[0]; p[r + 1] = c[1];
  }
  return p;
}
#define LDF(p) (*(const LDS_AS bf16x8*)(p))
#define ASTEP(SC, SN, VA, VB, VNA, VNB, KO, VO)                              \
  do {                                                                       \
    SN = MFMA32(kf, qf, negm); SBAR();                                       \
    kf = LDF(kp + (KO)); VNA = LDF(vp + (VO)); VNB = LDF(vp + (VO) + 512); SBAR(); \
    { bf16x8 pa = exp_pack8(SC, 0); SBAR(); o = MFMA32(VA, pa, o); SBAR(); } \
    { bf16x8 pb = exp_pack8(SC, 8); SBAR(); o = MFMA32(VB, pb, o); SBAR(); } \
  } while (0)

__device__ __forceinline__ float max16(const f32x16 s) {
  float a = __builtin_fmaxf(__builtin_fmaxf(s[0], s[1]), s[2]);
  float c = __builtin_fmaxf(__builtin_fmaxf(s[3], s[4]), s[5]);
  a = __builtin_fmaxf(__builtin_fmaxf(a, s[6]), s[7]);
  c = __builtin_fmaxf(__builtin_fmaxf(c, s[8]), s[9]);
  a = __builtin_fmaxf(__builtin_fmaxf(a, s[10]), s[11]);
  c = __builtin_fmaxf(__builtin_fmaxf(c, s[12]), s[13]);
  a = __builtin_fmaxf(__builtin_fmaxf(a, s[14]), s[15]);
  return __builtin_fmaxf(a, c);
}
__device__ __forceinline__ float xhalf_max(float m) {
  auto rr = __builtin_amdgcn_permlane32_swap(__float_as_uint(m), __float_as_uint(m), false, false);
  return __builtin_fmaxf(__uint_as_float(rr[0]), __uint_as_float(rr[1]));
}

__global__ __launch_bounds__(1024, 4) void attn_kernel(const bf16_t* __restrict__ Q, const bf16_t* __restrict__ Kf,
                                                       const bf16_t* __restrict__ Vf, bf16_t* __restrict__ O,
                                                       const char* __restrict__ pf0, int pf0_lines,
                                                       const char* __restrict__ pf1, int pf1_lines) {
  extern __shared__ __attribute__((aligned(16))) char smem_g[];
  LDS_AS char* smem = (LDS_AS char*)smem_g;
  const int tid = threadIdx.x, lane = tid & 63;
  const int wave = __builtin_amdgcn_readfirstlane(tid >> 6);
  const int kh = wave >> 3, qw = wave & 7;
  const int b = blockIdx.x, xcd = b & 7, j = b >> 3;
  const int head = xcd * 4 + (j & 3), qt = j >> 2;
  const int r32 = lane & 31, hh = lane >> 5;
  const int q0 = qt * 256 + qw * 32;

  const unsigned lds0 = (unsigned)(uintptr_t)smem;
    const bf16_t* ks = Kf + (size_t)head * 32768 + kh * 16384 + qw * 512 + lane * 8;
    const bf16_t* vs = Vf + (size_t)head * 32768 + kh * 16384 + qw * 512 + lane * 8;
    const unsigned kd = lds0 + A_KOFF + kh * 32768 + qw * 1024, vd = lds0 + A_VOFF + kh * 32768 + qw * 1024;
    glds16(Q + ((size_t)head * SEQ + q0 + r32) * 16 + hh * 8, lds0 + A_QOFF + qw * 1024);
    glds16(ks, kd);
    glds16(vs, vd);

  if (tid < 448) {
    const unsigned v = (tid & 31) == 0 ? 0x3F803F80u : 0u;
    *(LDS_AS u32x4*)(smem + A_ONES + tid * 16) = u32x4{v, v, v, v};
  }

  LDS_AS char* kb = smem + A_KOFF + kh * 32768 + lane * 16;
  const bool real = r32 < 16;
  LDS_AS char* vb = real ? (smem + A_VOFF + kh * 32768 + hh * 256 + r32 * 16) : (smem + A_ONES + (r32 == 16 ? 0 : 16));
  const int vstride = real ? 2048 : 0;

  wait_bar<0>();
  const bf16x8 qf = LDF(smem + A_QOFF + qw * 1024 + lane * 16);
  f32x16 negm;
  float mref;
  {
    f32x16 z = {};
    f32x16 s = MFMA32(LDF(smem + A_KOFF + lane * 16), qf, z);
    mref = xhalf_max(max16(s));
#pragma unroll
    for (int r = 0; r < 16; ++r) negm[r] = -mref;
  }
  f32x16 o = {};
  {
    LDS_AS char* kp = kb;
    LDS_AS char* vp = vb;
    bf16x8 kf = LDF(kp), va0 = LDF(vp), vb0 = LDF(vp + 512), va1, vb1;
    f32x16 sA = MFMA32(kf, qf, negm), sB;
    kf = LDF(kp + 1024);
    SBAR();
#pragma unroll 1
    for (int t = 0; t < 8; t += 2) {
      if (t < 6) {
        if ((wave >> 2) == (t >> 1)) {
          const int c = (t >> 1) + 1, w2 = (wave & 3) * 2;
          const bf16_t* k2 = Kf + (size_t)head * 32768 + c * 4096 + w2 * 512 + lane * 8;
          const bf16_t* v2 = Vf + (size_t)head * 32768 + c * 4096 + w2 * 512 + lane * 8;
          const unsigned d2 = lds0 + c * 8192 + w2 * 1024;
#pragma unroll
          for (int hf = 0; hf < 2; ++hf)
#pragma unroll
            for (int i = 0; i < 2; ++i) {
              glds16(k2 + hf * 16384 + i * 512, d2 + A_KOFF + hf * 32768 + i * 1024);
              glds16(v2 + hf * 16384 + i * 512, d2 + A_VOFF + hf * 32768 + i * 1024);
            }
        }
      } else {
        wait_bar<0>();
      }
      ASTEP(sA, sB, va0, vb0, va1, vb1, 2048, 1024);
      ASTEP(sB, sA, va1, vb1, va0, vb0, 3072, 2048);
      kp += 2048; vp += vstride;
    }
    {
      const int line = j * 1024 + tid;
      const char* pp = line < pf0_lines ? pf0 + (size_t)line * 128 : pf1 + (size_t)(line - pf0_lines) * 128;
      if (line < pf0_lines + pf1_lines) touch(pp, lds0 + A_ONES + 7168);
    }
#pragma unroll 1
    for (int it = 0; it < 6; ++it) {
      {
        const int pr = (it + (wave >> 2)) & 3;
        if (pr == 0) __builtin_amdgcn_s_setprio(0);
        else if (pr == 1) __builtin_amdgcn_s_setprio(1);
        else if (pr == 2) __builtin_amdgcn_s_setprio(2);
        else __builtin_amdgcn_s_setprio(3);
      }
      ASTEP(sA, sB, va0, vb0, va1, vb1, 2048, 1024);
      ASTEP(sB, sA, va1, vb1, va0, vb0, 3072, 2048);
      ASTEP(sA, sB, va0, vb0, va1, vb1, 4096, 3072);
      ASTEP(sB, sA, va1, vb1, va0, vb0, 5120, 4096);
      kp += 4096; vp += 2 * vstride;
    }
    __builtin_amdgcn_s_setprio(0);
  }
  if (__builtin_expect(__any(!(o[8] < 1.2676506e30f)), 0)) {
    float m = -INFINITY;
    f32x16 z = {};
#pragma unroll 1
    for (int t = 0; t < 32; ++t) {
      f32x16 sc = MFMA32(LDF(kb + t * 1024), qf, z);
      m = __builtin_fmaxf(m, max16(sc));
    }
    mref = xhalf_max(m);
#pragma unroll
    for (int r = 0; r < 16; ++r) negm[r] = -mref;
    o = z;
#pragma unroll 1
    for (int t = 0; t < 32; ++t) {
      const f32x16 sc = MFMA32(LDF(kb + t * 1024), qf, negm);
      LDS_AS char* vq = vb + (vstride >> 1) * t;
      o = MFMA32(LDF(vq), exp_pack8(sc, 0), o);
      o = MFMA32(LDF(vq + 512), exp_pack8(sc, 8), o);
    }
  }

  {
    auto rr = __builtin_amdgcn_permlane32_swap(__float_as_uint(o[8]), __float_as_uint(o[8]), false, false);
    o[8] = __uint_as_float(rr[0]);
  }
  LDS_AS float* cb = (LDS_AS float*)(smem + A_COMB + qw * (64 * 40) + lane * 4);
  if (kh) {
#pragma unroll
    for (int r = 0; r < 9; ++r) cb[r * 64] = o[r];
    cb[9 * 64] = mref;
  }
  bar_lgkm();
  if (!kh) {
    const float m2 = cb[9 * 64];
    const float mm = __builtin_fmaxf(mref, m2);
    const float f1 = __builtin_amdgcn_exp2f(mref - mm), f2 = __builtin_amdgcn_exp2f(m2 - mm);
    const float l = o[8] * f1 + cb[8 * 64] * f2;
    const float inv = __builtin_amdgcn_rcpf(l);
    const float a1 = f1 * inv, a2 = f2 * inv;
    float v[8];
#pragma unroll
    for (int r = 0; r < 8; ++r) v[r] = o[r] * a1 + cb[r * 64] * a2;
    bf16_t* op = O + tile_off(q0 + r32, head * 16, DM / 16) + (r32 & 15) * 16 + 4 * hh;
    *(u32x2*)(op) = pack4(v[0], v[1], v[2], v[3]);
    *(u32x2*)(op + 8) = pack4(v[4], v[5], v[6], v[7]);
  }
  asm volatile("s_waitcnt vmcnt(0)" ::: "memory");
}

constexpr int QKV_NST = 4, WO_NST = 6, F1_NST = 4, F2_NST = 6;
#define NL_SMALL 4
#define QKV0_K gemm_kernel<64, 192, 1, 4, 512, 1536, QKV_NST, 0, 1, 4, 0>
#define QKV1_K gemm_kernel<64, 192, 1, 4, 512, 1536, QKV_NST, 0, 0, 4, 1>
#define WO_K gemm_kernel<64, 64, 2, 2, 512, 512, WO_NST, 1, 0, NL_SMALL, 1>
#define F1_K gemm_kernel<128, 112, 4, 1, 512, 1792, F1_NST, 2, 0, 4, 1>
#define F2_K gemm_kernel<64, 64, 2, 2, 1792, 512, F2_NST, 3, 0, NL_SMALL, 1>
constexpr int QKV_LDS = QKV_NST * (64 + 192) * 128 + 4096, WO_LDS = WO_NST * 128 * 128 + 4096,
              F1_LDS = F1_NST * (128 + 112) * 128 + 4096, F2_LDS = F2_NST * 128 * 128 + 4096;

extern "C" void kernel_launch(void* const* d_in, const int* in_sizes, int n_in, void* d_out, int out_size, void* d_ws,
                              size_t ws_size, hipStream_t stream) {
  const bf16_t* x = (const bf16_t*)d_in[0];
  const bf16_t* wqkv = (const bf16_t*)d_in[1];
  const bf16_t* wo = (const bf16_t*)d_in[2];
  const bf16_t* w1 = (const bf16_t*)d_in[4];
  const bf16_t* b1 = (const bf16_t*)d_in[5];
  const bf16_t* w2 = (const bf16_t*)d_in[6];
  const bf16_t* b2 = (const bf16_t*)d_in[7];
  char* ws = (char*)d_ws;
  bf16_t* Qb = (bf16_t*)(ws + (0 << 20));
  bf16_t* Kb = (bf16_t*)(ws + (2 << 20));
  bf16_t* Vb = (bf16_t*)(ws + (4 << 20));
  bf16_t* Ob = (bf16_t*)(ws + (6 << 20));
  bf16_t* X1 = (bf16_t*)(ws + (8 << 20));
  bf16_t* Hb = (bf16_t*)(ws + (10 << 20));
  bf16_t* X2 = (bf16_t*)(ws + (18 << 20));
  float* St1 = (float*)(ws + (20 << 20));
  float* St2 = (float*)(ws + (21 << 20));
  bf16_t* out = (bf16_t*)d_out;

  hipFuncSetAttribute((const void*)QKV0_K, hipFuncAttributeMaxDynamicSharedMemorySize, QKV_LDS);
  hipFuncSetAttribute((const void*)QKV1_K, hipFuncAttributeMaxDynamicSharedMemorySize, QKV_LDS);
  hipFuncSetAttribute((const void*)WO_K, hipFuncAttributeMaxDynamicSharedMemorySize, WO_LDS);
  hipFuncSetAttribute((const void*)F1_K, hipFuncAttributeMaxDynamicSharedMemorySize, F1_LDS);
  hipFuncSetAttribute((const void*)F2_K, hipFuncAttributeMaxDynamicSharedMemorySize, F2_LDS);
  hipFuncSetAttribute((const void*)attn_kernel, hipFuncAttributeMaxDynamicSharedMemorySize, A_LDS);

  for (int layer = 0; layer < 2; ++layer) {
    const bf16_t* xin = layer == 0 ? x : X2;
    bf16_t* xout = layer == 0 ? X2 : out;
    const char* none = (const char*)x;
    if (layer == 0) QKV0_K<<<256, 768, QKV_LDS, stream>>>(xin, wqkv, nullptr, nullptr, Qb, Kb, Vb, nullptr, none, 0, 0);
    else            QKV1_K<<<256, 768, QKV_LDS, stream>>>(xin, wqkv, nullptr, nullptr, Qb, Kb, Vb, St2, none, 0, 0);
    attn_kernel<<<256, 1024, A_LDS, stream>>>(Qb, Kb, Vb, Ob, (const char*)wo, DM * DM * 2 / 128, (const char*)w1, DM * FF * 2 / 128);
    WO_K<<<256, 512 + 64 * NL_SMALL, WO_LDS, stream>>>(Ob, wo, nullptr, xin, X1, nullptr, nullptr, St1, none, 0, (layer ? 1 : 0) | 2);
    F1_K<<<256, 768, F1_LDS, stream>>>(X1, w1, b1, nullptr, Hb, nullptr, nullptr, St1, (const char*)w2, FF * DM * 2 / 128, 0);
    F2_K<<<256, 512 + 64 * NL_SMALL, F2_LDS, stream>>>(Hb, w2, b2, X1, xout, nullptr, nullptr, St2, (const char*)wqkv,
                                       layer == 0 ? DM * 3 * DM * 2 / 128 : 0, 1 | (layer ? 0 : 2));
  }
}
```

```cpp
#include <hip/hip_runtime.h>
#include <hip/hip_bf16.h>
#include <stdint.h>

typedef __bf16 bf16_t;
typedef __bf16 bf16x2 __attribute__((ext_vector_type(2)));
typedef __bf16 bf16x4 __attribute__((ext_vector_type(4)));
typedef __bf16 bf16x8 __attribute__((ext_vector_type(8)));
typedef float f32x2 __attribute__((ext_vector_type(2)));
typedef float f32x4 __attribute__((ext_vector_type(4)));
typedef float f32x16 __attribute__((ext_vector_type(16)));
typedef unsigned u32x2 __attribute__((ext_vector_type(2)));
typedef unsigned u32x4 __attribute__((ext_vector_type(4)));

#define LDS_AS __attribute__((address_space(3)))

constexpr int SEQ = 2048, DM = 512, NH = 32, DH = 16, FF = 1792;

__device__ __forceinline__ void glds16(const void* g, unsigned lds_base) {
  unsigned keep;
  asm volatile("s_mov_b32 %0, m0\n\ts_mov_b32 m0, %2\n\ts_nop 0\n\tglobal_load_lds_dwordx4 %1, off\n\ts_mov_b32 m0, %0"
               : "=&s"(keep) : "v"(g), "s"(lds_base) : "memory");
}
template <int IMM>
__device__ __forceinline__ void glds16s(unsigned voff, const void* sbase, unsigned lds_base) {
  asm volatile("s_add_i32 m0, %2, %3\n\ts_nop 0\n\tglobal_load_lds_dwordx4 %0, %1"
               :: "v"(voff), "s"(sbase), "s"(lds_base), "i"(IMM) : "memory", "scc");
}
__device__ __forceinline__ void touch(const void* p, unsigned lds_dummy) {
  unsigned keep;
  asm volatile("s_mov_b32 %0, m0\n\ts_mov_b32 m0, %2\n\ts_nop 0\n\tglobal_load_lds_dword %1, off\n\ts_mov_b32 m0, %0"
               : "=&s"(keep) : "v"(p), "s"(lds_dummy) : "memory");
}
#define ST8(ptr, val) (*(u32x2*)(ptr) = (val))
#define SBAR() __builtin_amdgcn_sched_barrier(0)
#define PIN(x) asm volatile("" : "+v"(x))
__device__ __forceinline__ int tile_off(int tok, int col, int nc16) { return ((tok >> 4) * nc16 + (col >> 4)) * 256; }
__device__ __forceinline__ u32x2 pack4(float a, float b, float c, float d) {
  f32x2 lo = {a, b}, hi = {c, d};
  bf16x2 l = __builtin_convertvector(lo, bf16x2), h = __builtin_convertvector(hi, bf16x2);
  u32x2 r = {__builtin_bit_cast(unsigned, l), __builtin_bit_cast(unsigned, h)};
  return r;
}
__device__ __forceinline__ float bf_lo(unsigned u) { return __builtin_bit_cast(float, u << 16); }
__device__ __forceinline__ float bf_hi(unsigned u) { return __builtin_bit_cast(float, u & 0xffff0000u); }

template <int N>
__device__ __forceinline__ void wait_bar() {
  asm volatile("s_waitcnt vmcnt(%0) lgkmcnt(0)\n\ts_barrier" ::"i"(N) : "memory");
}

template <int I> struct IC { static constexpr int value = I; };
template <int I, int N, class F>
__device__ __forceinline__ void static_for(F& f) {
  if constexpr (I < N) { f(IC<I>{}); static_for<I + 1, N>(f); }
}

__device__ __forceinline__ float gelu_tanh(float x) {
  const float c1 = -2.0f * 0.7978845608028654f * 1.4426950408889634f;
  const float c2 = c1 * 0.044715f;
  float t = x * __builtin_fmaf(x * x, c2, c1);
  float e = __builtin_amdgcn_exp2f(t);
  return x * __builtin_amdgcn_rcpf(1.0f + e);
}

template <int N>
__device__ __forceinline__ void wait_vm() { asm volatile("s_waitcnt vmcnt(%0)" ::"i"(N) : "memory"); }
__device__ __forceinline__ void bar_lgkm() { asm volatile("s_waitcnt lgkmcnt(0)\n\ts_barrier" ::: "memory"); }
#define MFMA16(a, b, c) __builtin_amdgcn_mfma_f32_16x16x32_bf16(a, b, c, 0, 0, 0)
__device__ __forceinline__ float sum_g(float v) {
  auto a = __builtin_amdgcn_permlane16_swap(__float_as_uint(v), __float_as_uint(v), false, false);
  v = __uint_as_float(a[0]) + __uint_as_float(a[1]);
  auto b = __builtin_amdgcn_permlane32_swap(__float_as_uint(v), __float_as_uint(v), false, false);
  return __uint_as_float(b[0]) + __uint_as_float(b[1]);
}

template <int BM, int BN, int WVM, int WVN, int K, int LDW, int NST, int MODE, int SELF, int NL, int XT>
__global__ __launch_bounds__(512 + 64 * NL, 2 + NL / 4) void gemm_kernel(const bf16_t* __restrict__ X, const bf16_t* __restrict__ W,
                                                      const bf16_t* __restrict__ bias, const bf16_t* __restrict__ res,
                                                      bf16_t* __restrict__ out0, bf16_t* __restrict__ out1,
                                                      bf16_t* __restrict__ out2, float* __restrict__ stats,
                                                      const char* __restrict__ pf, int pf_lines, int flags) {
  extern __shared__ __attribute__((aligned(16))) char smem_g[];
  LDS_AS char* smem = (LDS_AS char*)smem_g;
  constexpr int TM = BM / WVM / 16, TN = BN / WVN / 16, NB = BN / 16, HM = TM / 2;
  constexpr int XB = BM * 128, WB = BN * 128, STB = XB + WB;
  constexpr int NXI = BM / 8, NI = (BM + BN) / 8, LPL = (NI + NL - 1) / NL, NK = K / 64;
  constexpr int NFULL = NI - NL * (LPL - 1);
  constexpr int DEP = (STB >= 28 * 1024) ? 2 : 3;
  static_assert(NST >= DEP + 1, "ring too small");
  constexpr bool LN = (MODE == 0 || MODE == 2);
  constexpr bool GRP = (NB % 4 == 0);
  constexpr int NSEG = NB / 4;
  constexpr int NCS = (NB + 3) / 4;
  constexpr bool PFK = (MODE >= 2);
  constexpr int NX4 = HM * TN;
  constexpr int EX_OFF = NST * STB;
  constexpr int CS_OFF = EX_OFF;
  constexpr int ST_OFF = CS_OFF + 1024;
  constexpr int PF_OFF = ST_OFF + 1024;
  constexpr int BI_OFF = PF_OFF + 256;
  static_assert(8 * NX4 * 1024 <= ((NK - 1) % NST) * STB, "exchange area overlaps the last stage");

  const int tid = threadIdx.x, lane = tid & 63;
  const int wave = __builtin_amdgcn_readfirstlane(tid >> 6);
  const int b = blockIdx.x, xcd = b & 7, j = b >> 3;
  int mt, nt;
  if (BM == 128) { mt = xcd * 2 + (j & 1); nt = j >> 1; }
  else           { mt = xcd * 4 + (j & 3); nt = j >> 2; }
  const int m0 = mt * BM;
  const int l15 = lane & 15, g = lane >> 4;
  const int q_ = (lane & 15) >> 2, p_ = lane & 3;
  const __bf16 one = (__bf16)1.0f;
  const bf16x8 ones8 = {one, one, one, one, one, one, one, one};

  auto colbase = [&](int nb) -> int {
    if (MODE == 0) return (nb >> 2) * 512 + (nt * 4 + (nb & 3)) * 16;
    return nt * BN + nb * 16;
  };
  auto waddr = [&](int kb, int nb) -> int {
    if (GRP) return XB + (kb * NSEG + (nb >> 2)) * 1024 + q_ * 128 + (((nb & 3) ^ (((q_ >> 1) & 1) | ((kb & 1) << 1))) * 32) + p_ * 8;
    return XB + (kb * NB + nb) * 256 + (q_ + 4 * (kb & 1)) * 32 + p_ * 8;
  };
  auto wt1 = [&](int a) -> int { return GRP ? a + 512 : a ^ 128; };
  auto tr8 = [&](LDS_AS char* p0, LDS_AS char* p1) -> bf16x8 {
    bf16x4 a = __builtin_amdgcn_ds_read_tr16_b64_v4bf16((LDS_AS bf16x4*)p0);
    bf16x4 c = __builtin_amdgcn_ds_read_tr16_b64_v4bf16((LDS_AS bf16x4*)p1);
    return __builtin_shufflevector(a, c, 0, 1, 2, 3, 4, 5, 6, 7);
  };

  if (wave >= 8) {
    const int lw = wave - 8;
    __builtin_amdgcn_s_setprio(3);
    constexpr int XS = NXI / NL;
    unsigned voff[LPL];
    {
      const int row = 8 * lw + (lane >> 3);
      const int ch = (lane & 7) ^ ((row >> 1) & 7);
      if (XT) voff[0] = (unsigned)((lw >> 1) * (K / 16) * 512 + (lw & 1) * 1024 + lane * 16);
      else    voff[0] = (unsigned)(row * K + ch * 8) * 2u;
#pragma unroll
      for (int i = 1; i < XS; ++i) voff[i] = voff[0] + i * (8 * NL) * K * 2;
#pragma unroll
      for (int i = XS; i < LPL; ++i) {
        const int wi = lw + NL * (i - XS);
        if (GRP) {
          const int kb = wi / NSEG, seg = wi % NSEG, r = lane >> 3;
          const int c = ((lane & 7) >> 1) ^ (((r >> 1) & 1) | ((kb & 1) << 1));
          voff[i] = (unsigned)((kb * 8 + r) * LDW + colbase(seg * 4 + c) + (lane & 1) * 8) * 2u;
        } else {
          const int sidx = 4 * wi + (lane >> 4);
          const int kb = sidx / NB, nb = sidx % NB;
          const int krow = kb * 8 + (((lane >> 1) & 7) ^ ((kb & 1) << 2));
          voff[i] = (unsigned)(krow * LDW + colbase(nb) + (lane & 1) * 8) * 2u;
        }
      }
    }
    const char* xs_ = (const char*)(X + (size_t)m0 * K);
    const char* ws_ = (const char*)W;
    const unsigned lds0 = (unsigned)(uintptr_t)smem + lw * 1024;
    const bool full = lw < NFULL;
    auto issue = [&](auto ST) {
      constexpr int st = decltype(ST)::value;
      auto one_ = [&](auto I) {
        constexpr int i = decltype(I)::value;
        if (i < LPL - 1 || NFULL == NL || full)
          glds16s<(st % NST) * STB + i * (1024 * NL)>(voff[i], i < XS ? xs_ + st * (XT ? 2048 : 128) : ws_ + (size_t)st * (64 * LDW * 2), lds0);
      };
      static_for<0, LPL>(one_);
    };
    static_for<0, (DEP < NK ? DEP : NK)>(issue);

    constexpr int RS = BM / 64;
    int ca[NCS][2];
#pragma unroll
    for (int c = 0; c < NCS; ++c) {
      const int nb = (lw + 4 * c < NB) ? lw + 4 * c : NB - 1;
      ca[c][0] = waddr(g, nb); ca[c][1] = waddr(4 + g, nb);
    }
    const int xo = (lw * RS * 16 + l15) * 128 + ((g ^ (l15 >> 1)) * 16);
    f32x4 cs[NCS], rs[RS], rq[RS];
#pragma unroll
    for (int c = 0; c < NCS; ++c) cs[c] = f32x4{0.f, 0.f, 0.f, 0.f};
#pragma unroll
    for (int mi = 0; mi < RS; ++mi) rs[mi] = rq[mi] = f32x4{0.f, 0.f, 0.f, 0.f};
    auto lstep = [&](auto KT) {
      constexpr int kt = decltype(KT)::value;
      constexpr int last = (kt + DEP - 1 < NK - 1) ? kt + DEP - 1 : NK - 1;
      constexpr int tch = (PFK && kt + DEP > NK - 1) ? 2 : 0;
      if (NFULL == NL || full) wait_vm<(last - kt) * LPL + tch>(); else wait_vm<(last - kt) * (LPL - 1) + tch>();
      bar_lgkm();
      if constexpr (kt + DEP < NK) issue(IC<kt + DEP>{});
      if constexpr (PFK && kt + DEP == NK - 1) {
#pragma unroll
        for (int i = 0; i < 2; ++i) {
          int line = j * 512 + i * 256 + lw * 64 + lane;
          line = line < pf_lines ? line : 0;
          touch(pf + (size_t)line * 128, (unsigned)(uintptr_t)smem + PF_OFF);
        }
      }
      if (LN) {
        LDS_AS char* base = smem + (kt % NST) * STB;
#pragma unroll
        for (int ks = 0; ks < 2; ++ks) {
#pragma unroll
          for (int c = 0; c < NCS; ++c) cs[c] = MFMA16(tr8(base + ca[c][ks], base + wt1(ca[c][ks])), ones8, cs[c]);
          if (SELF) {
#pragma unroll
            for (int mi = 0; mi < RS; ++mi) {
              const bf16x8 xf = *(const LDS_AS bf16x8*)(base + ((xo ^ (ks * 64)) + mi * 2048));
              rs[mi] = MFMA16(ones8, xf, rs[mi]);
              rq[mi] = MFMA16(xf, xf, rq[mi]);
            }
          }
        }
      }
    };
    static_for<0, NK>(lstep);
    if (LN) {
      if (l15 == 0) {
#pragma unroll
        for (int c = 0; c < NCS; ++c)
          *(LDS_AS f32x4*)(smem + CS_OFF + ((lw + 4 * c < NB) ? lw + 4 * c : NB - 1) * 64 + g * 16) = cs[c];
      }
      if (SELF) {
        if (g == (l15 >> 2)) {
          const int r = l15 & 3;
#pragma unroll
          for (int mi = 0; mi < RS; ++mi) {
            const float sq = r == 0 ? rq[mi][0] : r == 1 ? rq[mi][1] : r == 2 ? rq[mi][2] : rq[mi][3];
            const float m = rs[mi][0] * (1.0f / K);
            const float v = __builtin_fmaxf(sq * (1.0f / K) - m * m, 0.f);
            *(LDS_AS f32x2*)(smem + ST_OFF + (lw * RS * 16 + mi * 16 + l15) * 8) = f32x2{m, __builtin_amdgcn_rsqf(v + 1e-5f)};
          }
        }
      }
    }
    bar_lgkm();
    if (PFK) asm volatile("s_waitcnt vmcnt(0)" ::: "memory");
    return;
  }

  const int kg = wave >> 2, w4 = wave & 3;
  const int wm = w4 / WVN, wn = w4 % WVN;
  auto nb_of = [&](int ni) -> int {
    if (MODE == 0) return 4 * ni + wn;
    return wn * TN + ni;
  };
  const int tl0 = wm * TM * 16 + kg * HM * 16;
  const int tok0 = m0 + tl0;

  if (LN && !SELF) {
    if (wave < BM / 64) {
      float sm = 0.f, sq = 0.f;
#pragma unroll
      for (int i = 0; i < 8; ++i) {
        const f32x4 t = *(const f32x4*)(stats + (size_t)(m0 + tid) * 32 + i * 4);
        sm += t[0] + t[2]; sq += t[1] + t[3];
      }
      const float m = sm * (1.0f / K);
      const float v = __builtin_fmaxf(sq * (1.0f / K) - m * m, 0.f);
      *(LDS_AS f32x2*)(smem + ST_OFF + tid * 8) = f32x2{m, __builtin_amdgcn_rsqf(v + 1e-5f)};
    }
  }
  if (MODE == 2) {
    if (wave == 2 && lane < BN / 4) *(LDS_AS u32x2*)(smem + BI_OFF + lane * 8) = *(const u32x2*)(bias + nt * BN + lane * 4);
  }
  u32x2 resv[HM][TN];
  u32x2 biasv[TN];
  if (MODE == 1 || MODE == 3) {
#pragma unroll
    for (int mi = 0; mi < HM; ++mi)
#pragma unroll
      for (int ni = 0; ni < TN; ++ni)
        resv[mi][ni] = *(const u32x2*)(res + ((flags & 1) ? tile_off(tok0 + mi * 16, colbase(nb_of(ni)), DM / 16) + l15 * 16 + 4 * g
                                                             : (tok0 + mi * 16 + l15) * DM + colbase(nb_of(ni)) + 4 * g));
  }
  if (MODE == 3) {
#pragma unroll
    for (int ni = 0; ni < TN; ++ni) biasv[ni] = *(const u32x2*)(bias + colbase(nb_of(ni)) + 4 * g);
  }

  const int xo_ = XT ? wm * TM * 2048 + (kg * 2 + (g >> 1)) * 512 + l15 * 32 + (g & 1) * 16
                     : ((wm * TM * 16 + l15) * 128 + ((g ^ (l15 >> 1)) * 16)) ^ (kg * 64);
  const int xoA = xo_ + kg * HM * 2048, xoB = xo_ + (1 - kg) * HM * 2048;
  int wr_[TN];
#pragma unroll
  for (int ni = 0; ni < TN; ++ni) wr_[ni] = GRP ? waddr(kg * 4 + g, nb_of(ni)) : waddr(kg * 4 + g, nb_of(0)) + ni * 256;
  const int wr1_ = wt1(wr_[0]);

  f32x4 acc[TM][TN];
#pragma unroll
  for (int mi = 0; mi < TM; ++mi)
#pragma unroll
    for (int ni = 0; ni < TN; ++ni) acc[mi][ni] = f32x4{0.f, 0.f, 0.f, 0.f};

  auto kstep = [&](auto KT) {
    constexpr int kt = decltype(KT)::value;
    bar_lgkm();
    LDS_AS char* base = smem + (kt % NST) * STB;
    bf16x8 xf[TM], wf[TN];
#pragma unroll
    for (int mi = 0; mi < TM; ++mi) xf[mi] = *(const LDS_AS bf16x8*)(base + (mi < HM ? xoA + mi * 2048 : xoB + (mi - HM) * 2048));
#pragma unroll
    for (int ni = 0; ni < TN; ++ni) wf[ni] = tr8(base + wr_[ni], base + (GRP ? wr_[ni] + 512 : wr1_ + ni * 256));
    SBAR();
#pragma unroll
    for (int mj = 0; mj < TM; ++mj)
#pragma unroll
      for (int ni = 0; ni < TN; ++ni) {
        const int mi = (mj + HM) % TM;
        if (MODE == 0 && ni == 2) acc[mi][ni] = MFMA16(xf[mi], wf[ni], acc[mi][ni]);
        else                      acc[mi][ni] = MFMA16(wf[ni], xf[mi], acc[mi][ni]);
      }
    SBAR();
  };
  static_for<0, NK>(kstep);

  {
    LDS_AS f32x4* wr = (LDS_AS f32x4*)(smem + wave * (NX4 * 1024) + lane * 16);
#pragma unroll
    for (int mi = 0; mi < HM; ++mi)
#pragma unroll
      for (int ni = 0; ni < TN; ++ni) wr[(mi * TN + ni) * 64] = acc[HM + mi][ni];
  }
  bar_lgkm();
  f32x4 fa[HM][TN];
  {
    const LDS_AS f32x4* rd = (const LDS_AS f32x4*)(smem + (wave ^ 4) * (NX4 * 1024) + lane * 16);
#pragma unroll
    for (int mi = 0; mi < HM; ++mi)
#pragma unroll
      for (int ni = 0; ni < TN; ++ni) fa[mi][ni] = acc[mi][ni] + rd[(mi * TN + ni) * 64];
  }
  auto tok_stats = [&](int tl) -> f32x2 { return *(const LDS_AS f32x2*)(smem + ST_OFF + tl * 8); };
  auto col_sum4 = [&](int nb) -> f32x4 { return *(const LDS_AS f32x4*)(smem + CS_OFF + nb * 64 + g * 16); };

  if (MODE == 0) {
    const int head = nt * 4 + wn;
    const float qscale = 0.25f * 1.4426950408889634f;
    const f32x4 cq = col_sum4(nb_of(0)), ck = col_sum4(nb_of(1));
    const float cv = *(const LDS_AS float*)(smem + CS_OFF + nb_of(2) * 64 + l15 * 4);
#pragma unroll
    for (int mi = 0; mi < HM; ++mi) {
      const int tok = tok0 + mi * 16 + l15;
      const f32x2 st = tok_stats(tl0 + mi * 16 + l15);
      {
        const float sc = st[1] * qscale;
        float v[4];
#pragma unroll
        for (int r = 0; r < 4; ++r) v[r] = sc * __builtin_fmaf(-st[0], cq[r], fa[mi][0][r]);
        ST8(out0 + ((size_t)head * SEQ + tok) * 16 + 4 * g, pack4(v[0], v[1], v[2], v[3]));
      }
      {
        float v[4];
#pragma unroll
        for (int r = 0; r < 4; ++r) v[r] = st[1] * __builtin_fmaf(-st[0], ck[r], fa[mi][1][r]);
        ST8(out1 + (size_t)head * 32768 + (tok >> 5) * 512 + (g >> 1) * 256 + (tok & 31) * 8 + (g & 1) * 4,
            pack4(v[0], v[1], v[2], v[3]));
      }
      {
        float v[4];
#pragma unroll
        for (int r = 0; r < 4; ++r) {
          const f32x2 sr = tok_stats(tl0 + mi * 16 + 4 * g + r);
          v[r] = sr[1] * __builtin_fmaf(-sr[0], cv, fa[mi][2][r]);
        }
        const int ks16 = (tok0 + mi * 16) >> 4;
        ST8(out2 + (size_t)head * 32768 + ks16 * 256 + (g & 1) * 128 + l15 * 8 + (g >> 1) * 4,
            pack4(v[0], v[1], v[2], v[3]));
      }
    }
  } else if (MODE == 2) {
#pragma unroll
    for (int mi = 0; mi < HM; ++mi) {
      const int tok = tok0 + mi * 16 + l15;
      const f32x2 st = tok_stats(tl0 + mi * 16 + l15);
      const float nrm = -st[0] * st[1];
#pragma unroll
      for (int ni = 0; ni < TN; ++ni) {
        const f32x4 cc = col_sum4(nb_of(ni));
        const u32x2 bv = *(const LDS_AS u32x2*)(smem + BI_OFF + ni * 32 + g * 8);
        const float bb[4] = {bf_lo(bv[0]), bf_hi(bv[0]), bf_lo(bv[1]), bf_hi(bv[1])};
        float v[4];
#pragma unroll
        for (int r = 0; r < 4; ++r)
          v[r] = gelu_tanh(__builtin_fmaf(st[1], fa[mi][ni][r], __builtin_fmaf(nrm, cc[r], bb[r])));
        ST8(out0 + tile_off(tok0 + mi * 16, colbase(nb_of(ni)), FF / 16) + l15 * 16 + 4 * g, pack4(v[0], v[1], v[2], v[3]));
      }
    }
  } else {
#pragma unroll
    for (int mi = 0; mi < HM; ++mi) {
      const int tok = tok0 + mi * 16 + l15;
      float sm = 0.f, sq = 0.f;
#pragma unroll
      for (int ni = 0; ni < TN; ++ni) {
        float rr[4] = {bf_lo(resv[mi][ni][0]), bf_hi(resv[mi][ni][0]), bf_lo(resv[mi][ni][1]), bf_hi(resv[mi][ni][1])};
        if (MODE == 3) {
          rr[0] += bf_lo(biasv[ni][0]); rr[1] += bf_hi(biasv[ni][0]); rr[2] += bf_lo(biasv[ni][1]); rr[3] += bf_hi(biasv[ni][1]);
        }
        const u32x2 pk = pack4(rr[0] + fa[mi][ni][0], rr[1] + fa[mi][ni][1], rr[2] + fa[mi][ni][2], rr[3] + fa[mi][ni][3]);
        ST8(out0 + ((flags & 2) ? tile_off(tok0 + mi * 16, colbase(nb_of(ni)), DM / 16) + l15 * 16 + 4 * g
                                : tok * DM + colbase(nb_of(ni)) + 4 * g), pk);
        const float w[4] = {bf_lo(pk[0]), bf_hi(pk[0]), bf_lo(pk[1]), bf_hi(pk[1])};
#pragma unroll
        for (int r = 0; r < 4; ++r) { sm += w[r]; sq = __builtin_fmaf(w[r], w[r], sq); }
      }
      sm = sum_g(sm); sq = sum_g(sq);
      if (g == 0) *(f32x2*)(stats + (size_t)tok * 32 + (nt * 2 + wn) * 2) = f32x2{sm, sq};
    }
  }
}

constexpr int A_KOFF = 0, A_VOFF = 65536, A_ONES = 131072, A_QOFF = 139264, A_COMB = A_QOFF, A_LDS = A_COMB + 8 * 64 * 40;

#define MFMA32(a, b, c) __builtin_amdgcn_mfma_f32_32x32x16_bf16(a, b, c, 0, 0, 0)
__device__ __forceinline__ bf16x8 exp_pack8(const f32x16& s, int o) {
  bf16x8 p;
#pragma unroll
  for (int r = 0; r < 8; r += 2) {
    f32x2 e = {__builtin_amdgcn_exp2f(s[o + r]), __builtin_amdgcn_exp2f(s[o + r + 1])};
    bf16x2 c = __builtin_convertvector(e, bf16x2);
    p[r] = c[0]; p[r + 1] = c[1];
  }
  return p;
}
#define LDF(p) (*(const LDS_AS bf16x8*)(p))
#define ASTEP(SC, SN, VA, VB, VNA, VNB, KO, VO)                              \
  do {                                                                       \
    SN = MFMA32(kf, qf, negm); SBAR();                                       \
    kf = LDF(kp + (KO)); VNA = LDF(vp + (VO)); VNB = LDF(vp + (VO) + 512); SBAR(); \
    { bf16x8 pa = exp_pack8(SC, 0); SBAR(); o = MFMA32(VA, pa, o); SBAR(); } \
    { bf16x8 pb = exp_pack8(SC, 8); SBAR(); o = MFMA32(VB, pb, o); SBAR(); } \
  } while (0)

__device__ __forceinline__ float max16(const f32x16 s) {
  float a = __builtin_fmaxf(__builtin_fmaxf(s[0], s[1]), s[2]);
  float c = __builtin_fmaxf(__builtin_fmaxf(s[3], s[4]), s[5]);
  a = __builtin_fmaxf(__builtin_fmaxf(a, s[6]), s[7]);
  c = __builtin_fmaxf(__builtin_fmaxf(c, s[8]), s[9]);
  a = __builtin_fmaxf(__builtin_fmaxf(a, s[10]), s[11]);
  c = __builtin_fmaxf(__builtin_fmaxf(c, s[12]), s[13]);
  a = __builtin_fmaxf(__builtin_fmaxf(a, s[14]), s[15]);
  return __builtin_fmaxf(a, c);
}
__device__ __forceinline__ float xhalf_max(float m) {
  auto rr = __builtin_amdgcn_permlane32_swap(__float_as_uint(m), __float_as_uint(m), false, false);
  return __builtin_fmaxf(__uint_as_float(rr[0]), __uint_as_float(rr[1]));
}

__global__ __launch_bounds__(1024, 4) void attn_kernel(const bf16_t* __restrict__ Q, const bf16_t* __restrict__ Kf,
                                                       const bf16_t* __restrict__ Vf, bf16_t* __restrict__ O,
                                                       const char* __restrict__ pf0, int pf0_lines,
                                                       const char* __restrict__ pf1, int pf1_lines) {
  extern __shared__ __attribute__((aligned(16))) char smem_g[];
  LDS_AS char* smem = (LDS_AS char*)smem_g;
  const int tid = threadIdx.x, lane = tid & 63;
  const int wave = __builtin_amdgcn_readfirstlane(tid >> 6);
  const int kh = wave >> 3, qw = wave & 7;
  const int b = blockIdx.x, xcd = b & 7, j = b >> 3;
  const int head = xcd * 4 + (j & 3), qt = j >> 2;
  const int r32 = lane & 31, hh = lane >> 5;
  const int q0 = qt * 256 + qw * 32;

  const unsigned lds0 = (unsigned)(uintptr_t)smem;
    const bf16_t* ks = Kf + (size_t)head * 32768 + kh * 16384 + qw * 512 + lane * 8;
    const bf16_t* vs = Vf + (size_t)head * 32768 + kh * 16384 + qw * 512 + lane * 8;
    const unsigned kd = lds0 + A_KOFF + kh * 32768 + qw * 1024, vd = lds0 + A_VOFF + kh * 32768 + qw * 1024;
    glds16(Q + ((size_t)head * SEQ + q0 + r32) * 16 + hh * 8, lds0 + A_QOFF + qw * 1024);
    glds16(ks, kd);
    glds16(vs, vd);

  if (tid < 448) {
    const unsigned v = (tid & 31) == 0 ? 0x3F803F80u : 0u;
    *(LDS_AS u32x4*)(smem + A_ONES + tid * 16) = u32x4{v, v, v, v};
  }

  LDS_AS char* kb = smem + A_KOFF + kh * 32768 + lane * 16;
  const bool real = r32 < 16;
  LDS_AS char* vb = real ? (smem + A_VOFF + kh * 32768 + hh * 256 + r32 * 16) : (smem + A_ONES + (r32 == 16 ? 0 : 16));
  const int vstride = real ? 2048 : 0;

  wait_bar<0>();
  const bf16x8 qf = LDF(smem + A_QOFF + qw * 1024 + lane * 16);
  f32x16 negm;
  float mref;
  {
    f32x16 z = {};
    f32x16 s = MFMA32(LDF(smem + A_KOFF + lane * 16), qf, z);
    mref = xhalf_max(max16(s));
#pragma unroll
    for (int r = 0; r < 16; ++r) negm[r] = -mref;
  }
  f32x16 o = {};
  {
    LDS_AS char* kp = kb;
    LDS_AS char* vp = vb;
    bf16x8 kf = LDF(kp), va0 = LDF(vp), vb0 = LDF(vp + 512), va1, vb1;
    f32x16 sA = MFMA32(kf, qf, negm), sB;
    kf = LDF(kp + 1024);
    SBAR();
#pragma unroll 1
    for (int t = 0; t < 8; t += 2) {
      if (t < 6) {
        if ((wave >> 2) == (t >> 1)) {
          const int c = (t >> 1) + 1, w2 = (wave & 3) * 2;
          const bf16_t* k2 = Kf + (size_t)head * 32768 + c * 4096 + w2 * 512 + lane * 8;
          const bf16_t* v2 = Vf + (size_t)head * 32768 + c * 4096 + w2 * 512 + lane * 8;
          const unsigned d2 = lds0 + c * 8192 + w2 * 1024;
#pragma unroll
          for (int hf = 0; hf < 2; ++hf)
#pragma unroll
            for (int i = 0; i < 2; ++i) {
              glds16(k2 + hf * 16384 + i * 512, d2 + A_KOFF + hf * 32768 + i * 1024);
              glds16(v2 + hf * 16384 + i * 512, d2 + A_VOFF + hf * 32768 + i * 1024);
            }
        }
      } else {
        wait_bar<0>();
      }
      ASTEP(sA, sB, va0, vb0, va1, vb1, 2048, 1024);
      ASTEP(sB, sA, va1, vb1, va0, vb0, 3072, 2048);
      kp += 2048; vp += vstride;
    }
    {
      const int line = j * 1024 + tid;
      const char* pp = line < pf0_lines ? pf0 + (size_t)line * 128 : pf1 + (size_t)(line - pf0_lines) * 128;
      if (line < pf0_lines + pf1_lines) touch(pp, lds0 + A_ONES + 7168);
    }
#pragma unroll 1
    for (int it = 0; it < 6; ++it) {
      {
        const int pr = (it + (wave >> 2)) & 3;
        if (pr == 0) __builtin_amdgcn_s_setprio(0);
        else if (pr == 1) __builtin_amdgcn_s_setprio(1);
        else if (pr == 2) __builtin_amdgcn_s_setprio(2);
        else __builtin_amdgcn_s_setprio(3);
      }
      ASTEP(sA, sB, va0, vb0, va1, vb1, 2048, 1024);
      ASTEP(sB, sA, va1, vb1, va0, vb0, 3072, 2048);
      ASTEP(sA, sB, va0, vb0, va1, vb1, 4096, 3072);
      ASTEP(sB, sA, va1, vb1, va0, vb0, 5120, 4096);
      kp += 4096; vp += 2 * vstride;
    }
    __builtin_amdgcn_s_setprio(0);
  }
  if (__builtin_expect(__any(!(o[8] < 1.1529215e18f)), 0)) {
    float m = -INFINITY;
    f32x16 z = {};
#pragma unroll 1
    for (int t = 0; t < 32; ++t) {
      f32x16 sc = MFMA32(LDF(kb + t * 1024), qf, z);
      m = __builtin_fmaxf(m, max16(sc));
    }
    mref = xhalf_max(m);
#pragma unroll
    for (int r = 0; r < 16; ++r) negm[r] = -mref;
    o = z;
#pragma unroll 1
    for (int t = 0; t < 32; ++t) {
      const f32x16 sc = MFMA32(LDF(kb + t * 1024), qf, negm);
      LDS_AS char* vq = vb + (vstride >> 1) * t;
      o = MFMA32(LDF(vq), exp_pack8(sc, 0), o);
      o = MFMA32(LDF(vq + 512), exp_pack8(sc, 8), o);
    }
  }

  {
    auto rr = __builtin_amdgcn_permlane32_swap(__float_as_uint(o[8]), __float_as_uint(o[8]), false, false);
    o[8] = __uint_as_float(rr[0]);
  }
  LDS_AS float* cb = (LDS_AS float*)(smem + A_COMB + qw * (64 * 40) + lane * 4);
  if (kh) {
#pragma unroll
    for (int r = 0; r < 9; ++r) cb[r * 64] = o[r];
    cb[9 * 64] = mref;
  }
  bar_lgkm();
  if (!kh) {
    const float m2 = cb[9 * 64];
    const float mm = __builtin_fmaxf(mref, m2);
    const float f1 = __builtin_amdgcn_exp2f(mref - mm), f2 = __builtin_amdgcn_exp2f(m2 - mm);
    const float l = o[8] * f1 + cb[8 * 64] * f2;
    const float inv = __builtin_amdgcn_rcpf(l);
    const float a1 = f1 * inv, a2 = f2 * inv;
    float v[8];
#pragma unroll
    for (int r = 0; r < 8; ++r) v[r] = o[r] * a1 + cb[r * 64] * a2;
    bf16_t* op = O + tile_off(q0 + r32, head * 16, DM / 16) + (r32 & 15) * 16 + 4 * hh;
    *(u32x2*)(op) = pack4(v[0], v[1], v[2], v[3]);
    *(u32x2*)(op + 8) = pack4(v[4], v[5], v[6], v[7]);
  }
  asm volatile("s_waitcnt vmcnt(0)" ::: "memory");
}

constexpr int QKV_NST = 4, WO_NST = 6, F1_NST = 5, F2_NST = 6;
#define NL_SMALL 4
#define QKV0_K gemm_kernel<64, 192, 1, 4, 512, 1536, QKV_NST, 0, 1, 4, 0>
#define QKV1_K gemm_kernel<64, 192, 1, 4, 512, 1536, QKV_NST, 0, 0, 4, 1>
#define WO_K gemm_kernel<64, 64, 2, 2, 512, 512, WO_NST, 1, 0, NL_SMALL, 1>
#define F1_K gemm_kernel<128, 112, 4, 1, 512, 1792, F1_NST, 2, 0, 4, 1>
#define F2_K gemm_kernel<64, 64, 2, 2, 1792, 512, F2_NST, 3, 0, NL_SMALL, 1>
constexpr int QKV_LDS = QKV_NST * (64 + 192) * 128 + 4096, WO_LDS = WO_NST * 128 * 128 + 4096,
              F1_LDS = F1_NST * (128 + 112) * 128 + 4096, F2_LDS = F2_NST * 128 * 128 + 4096;

extern "C" void kernel_launch(void* const* d_in, const int* in_sizes, int n_in, void* d_out, int out_size, void* d_ws,
                              size_t ws_size, hipStream_t stream) {
  const bf16_t* x = (const bf16_t*)d_in[0];
  const bf16_t* wqkv = (const bf16_t*)d_in[1];
  const bf16_t* wo = (const bf16_t*)d_in[2];
  const bf16_t* w1 = (const bf16_t*)d_in[4];
  const bf16_t* b1 = (const bf16_t*)d_in[5];
  const bf16_t* w2 = (const bf16_t*)d_in[6];
  const bf16_t* b2 = (const bf16_t*)d_in[7];
  char* ws = (char*)d_ws;
  bf16_t* Qb = (bf16_t*)(ws + (0 << 20));
  bf16_t* Kb = (bf16_t*)(ws + (2 << 20));
  bf16_t* Vb = (bf16_t*)(ws + (4 << 20));
  bf16_t* Ob = (bf16_t*)(ws + (6 << 20));
  bf16_t* X1 = (bf16_t*)(ws + (8 << 20));
  bf16_t* Hb = (bf16_t*)(ws + (10 << 20));
  bf16_t* X2 = (bf16_t*)(ws + (18 << 20));
  float* St1 = (float*)(ws + (20 << 20));
  float* St2 = (float*)(ws + (21 << 20));
  bf16_t* out = (bf16_t*)d_out;

  hipFuncSetAttribute((const void*)QKV0_K, hipFuncAttributeMaxDynamicSharedMemorySize, QKV_LDS);
  hipFuncSetAttribute((const void*)QKV1_K, hipFuncAttributeMaxDynamicSharedMemorySize, QKV_LDS);
  hipFuncSetAttribute((const void*)WO_K, hipFuncAttributeMaxDynamicSharedMemorySize, WO_LDS);
  hipFuncSetAttribute((const void*)F1_K, hipFuncAttributeMaxDynamicSharedMemorySize, F1_LDS);
  hipFuncSetAttribute((const void*)F2_K, hipFuncAttributeMaxDynamicSharedMemorySize, F2_LDS);
  hipFuncSetAttribute((const void*)attn_kernel, hipFuncAttributeMaxDynamicSharedMemorySize, A_LDS);

  for (int layer = 0; layer < 2; ++layer) {
    const bf16_t* xin = layer == 0 ? x : X2;
    bf16_t* xout = layer == 0 ? X2 : out;
    const char* none = (const char*)x;
    if (layer == 0) QKV0_K<<<256, 768, QKV_LDS, stream>>>(xin, wqkv, nullptr, nullptr, Qb, Kb, Vb, nullptr, none, 0, 0);
    else            QKV1_K<<<256, 768, QKV_LDS, stream>>>(xin, wqkv, nullptr, nullptr, Qb, Kb, Vb, St2, none, 0, 0);
    attn_kernel<<<256, 1024, A_LDS, stream>>>(Qb, Kb, Vb, Ob, (const char*)wo, DM * DM * 2 / 128, (const char*)w1, DM * FF * 2 / 128);
    WO_K<<<256, 512 + 64 * NL_SMALL, WO_LDS, stream>>>(Ob, wo, nullptr, xin, X1, nullptr, nullptr, St1, none, 0, (layer ? 1 : 0) | 2);
    F1_K<<<256, 768, F1_LDS, stream>>>(X1, w1, b1, nullptr, Hb, nullptr, nullptr, St1, (const char*)w2, FF * DM * 2 / 128, 0);
    F2_K<<<256, 512 + 64 * NL_SMALL, F2_LDS, stream>>>(Hb, w2, b2, X1, xout, nullptr, nullptr, St2, (const char*)wqkv,
                                       layer == 0 ? DM * 3 * DM * 2 / 128 : 0, 1 | (layer ? 0 : 2));
  }
}
```
